```python
import torch

def _softmax(x, axis=-1):
    return torch.softmax(x, dim=axis)
def _gelu(x):
    return torch.nn.functional.gelu(x, approximate="tanh")
def _silu(x):
    return torch.nn.functional.silu(x)
def _sigmoid(x):
    return torch.sigmoid(x)
def _layernorm(x, axis=-1, eps=1e-5):
    m = x.mean(dim=axis, keepdim=True)
    v = x.var(dim=axis, keepdim=True, unbiased=False)
    return (x - m) / torch.sqrt(v + eps)
def _scatter_add(tgt, idx, src, axis=0):
    return tgt.clone().index_add_(axis, idx, src)
def _segment_sum(src, seg, n):
    out = torch.zeros((n,) + src.shape[1:], dtype=src.dtype)
    return out.index_add_(0, seg, src)
def _pad(x, pw):
    flat = tuple(v for pair in reversed(pw) for v in pair)
    return torch.nn.functional.pad(x, flat)

kernel_name = 'parallel_sum__mha_block__dense_activation_gelu__prompt'

def setup_inputs():

    _w = (lambda a: (lambda t: t.to(torch.bfloat16) if t.is_floating_point() else t)(torch.as_tensor(a)))
    def _A_setup():
        B, S, D, H = 1, 2048, 448, 16
        return {
            "x": _w((lambda *s: torch.randn(*s, dtype=torch.bfloat16) * 0.05)(B, S, D)),
            "wqkv": _w((lambda *s: torch.randn(*s, dtype=torch.bfloat16) * 0.05)(D, 3 * D)),
            "wo": _w((lambda *s: torch.randn(*s, dtype=torch.bfloat16) * 0.05)(D, D)),
        }
    _A_in = _A_setup()

    def _B_setup():
        B, S, D, F = 1, 2048, 448, 1792
        return {
            "x": _w((lambda *s: torch.randn(*s, dtype=torch.bfloat16) * 0.05)(B, S, D)),
            "w1": _w((lambda *s: torch.randn(*s, dtype=torch.bfloat16) * 0.8)(D, F) * (D ** -0.5)),
            "b1": _w((lambda *s: torch.randn(*s, dtype=torch.bfloat16) * 12.8)(F) * 0.01),
            "w2": _w((lambda *s: torch.randn(*s, dtype=torch.bfloat16) * 0.4)(F, D) * (F ** -0.5)),
            "b2": _w((lambda *s: torch.randn(*s, dtype=torch.bfloat16) * 6.4)(D) * 0.01),
        }
    _B_in = _B_setup()

    return {
        "A_x": _A_in["x"],
        "A_wqkv": _A_in["wqkv"],
        "A_wo": _A_in["wo"],
        "B_x": _B_in["x"],
        "B_w1": _B_in["w1"],
        "B_b1": _B_in["b1"],
        "B_w2": _B_in["w2"],
        "B_b2": _B_in["b2"],
    }

def reference(A_x, A_wqkv, A_wo, B_x, B_w1, B_b1, B_w2, B_b2):
    def _A_ref(x, wqkv, wo):
        B, S, D = x.shape
        H = 16
        Dh = D // H
        qkv = (lambda x, *d: x.permute(*d))((x @ wqkv).reshape(B, S, 3, H, Dh), 2, 0, 3, 1, 4)
        q, k, v = qkv[0], qkv[1], qkv[2]
        scores = torch.einsum("bhqd,bhkd->bhqk", q, k) * (Dh ** -0.5)
        p = _softmax(scores, axis=-1)
        o = torch.einsum("bhqk,bhkd->bhqd", p, v)
        return (lambda x, *d: x.permute(*d))(o, 0, 2, 1, 3).reshape(B, S, D) @ wo

    def _B_ref(x, w1, b1, w2, b2):
        h = torch.einsum("bsd,df->bsf", x, w1) + b1
        h = _gelu(h)
        return torch.einsum("bsf,fd->bsd", h, w2) + b2

    x = A_x
    xn = _layernorm(x, axis=-1)
    return x + _A_ref(xn, A_wqkv, A_wo) + _B_ref(xn, B_w1, B_b1, B_w2, B_b2)
```

```cpp
#include <hip/hip_runtime.h>
#include <hip/hip_bf16.h>
#include <stdint.h>

typedef __bf16 bf16x2 __attribute__((ext_vector_type(2)));
typedef __bf16 bf16x8 __attribute__((ext_vector_type(8)));
typedef float f32x2 __attribute__((ext_vector_type(2)));
typedef float f32x4 __attribute__((ext_vector_type(4)));
typedef float f32x16 __attribute__((ext_vector_type(16)));
typedef short s16x4 __attribute__((ext_vector_type(4)));
typedef unsigned u32x2 __attribute__((ext_vector_type(2)));
typedef unsigned u32x4 __attribute__((ext_vector_type(4)));
#define LAS __attribute__((address_space(3)))
#define DI __device__ __forceinline__
#define MFMA(a, b, c) __builtin_amdgcn_mfma_f32_32x32x16_bf16((a), (b), (c), 0, 0, 0)

constexpr int S_ = 2048, D_ = 448, H_ = 16, F_ = 1792;
constexpr size_t WS_Q = 0;
constexpr size_t WS_KV = WS_Q + 16 * 64 * 2048;
constexpr size_t WS_KN = WS_KV + 16 * 64 * 4096;
constexpr size_t WS_H = WS_KN + 16 * 64 * 4;
constexpr size_t WS_O = WS_H + (size_t)64 * 56 * 2048;
constexpr size_t WS_END = WS_O + 64 * 16 * 2048;

DI unsigned cvtpk(float lo, float hi) { f32x2 v = {lo, hi}; bf16x2 b = __builtin_convertvector(v, bf16x2); return __builtin_bit_cast(unsigned, b); }
DI bf16x2 asbf2(unsigned v) { return __builtin_bit_cast(bf16x2, v); }
DI float bflo(unsigned v) { return __uint_as_float(v << 16); }
DI float bfhi(unsigned v) { return __uint_as_float(v & 0xffff0000u); }
DI unsigned rfl(unsigned x) { return __builtin_amdgcn_readfirstlane(x); }
DI const void* uptr(const void* p) { const uint64_t v = (uint64_t)p; return (const void*)(((uint64_t)rfl((unsigned)(v >> 32)) << 32) | rfl((unsigned)v)); }
DI void glds16(const void* sbase_, unsigned voff, unsigned lds_dst_) {
    unsigned sv; const void* sbase = uptr(sbase_); const unsigned lds_dst = rfl(lds_dst_);
    asm volatile("s_mov_b32 %0, m0\n\ts_mov_b32 m0, %3\n\ts_nop 4\n\tglobal_load_lds_dwordx4 %1, %2\n\ts_mov_b32 m0, %0"
                 : "=&s"(sv) : "v"(voff), "s"(sbase), "s"(lds_dst) : "memory");
}
DI u32x4 gload16(const void* sbase, unsigned voff) { u32x4 r; asm volatile("global_load_dwordx4 %0, %1, %2" : "=v"(r) : "v"(voff), "s"(uptr(sbase)) : "memory"); return r; }
DI s16x4 trread(const LAS char* p) { return __builtin_bit_cast(s16x4, __builtin_amdgcn_ds_read_tr16_b64_v4i16((LAS s16x4*)p)); }
DI bf16x8 trfrag(const LAS char* p0, const LAS char* p1) {
    s16x4 a = trread(p0), b = trread(p1);
    return __builtin_bit_cast(bf16x8, __builtin_shufflevector(a, b, 0, 1, 2, 3, 4, 5, 6, 7));
}
DI bf16x8 ldsfrag(const LAS char* p) { return *(const LAS bf16x8*)p; }
template <int CTRL> DI float dppf(float v) { return __int_as_float(__builtin_amdgcn_update_dpp(0, __float_as_int(v), CTRL, 0xf, 0xf, true)); }
DI bf16x8 pack8(const f32x16& a, int s) {
    u32x4 p = {cvtpk(a[8 * s], a[8 * s + 1]), cvtpk(a[8 * s + 2], a[8 * s + 3]), cvtpk(a[8 * s + 4], a[8 * s + 5]), cvtpk(a[8 * s + 6], a[8 * s + 7])};
    return __builtin_bit_cast(bf16x8, p);
}
#define BARRIER() asm volatile("s_barrier" ::: "memory")
#define WAIT_VM(n) asm volatile("s_waitcnt vmcnt(" #n ")" ::: "memory")
#define WAIT_LGKM0() asm volatile("s_waitcnt lgkmcnt(0)" ::: "memory")

constexpr int A_SLOT = 28672;
constexpr int A_XS = 3 * A_SLOT;
constexpr int A_XROW = 464;
constexpr int A_LDS = A_XS + 128 * A_XROW;

DI float gelu_tanh(float h) {
    const float c0 = -2.0f * 0.7978845608028654f * 1.4426950408889634f, c1 = c0 * 0.044715f;
    const float z = h * __builtin_fmaf(h * h, c1, c0);
    const float e = __builtin_amdgcn_exp2f(z);
    return h * __builtin_amdgcn_rcpf(1.0f + e);
}

DI void phaseA(const char* __restrict__ x, const char* __restrict__ wqkv, const char* __restrict__ w1, const char* __restrict__ b1,
               char* __restrict__ ws, char* lds_generic, int Rb, int c) {
    LAS char* lds = (LAS char*)lds_generic;
    const unsigned lds0 = (unsigned)(uintptr_t)lds_generic;
    const int tid = threadIdx.x, l = tid & 63, w = rfl(tid >> 6) & 7, r = l & 31, h = l >> 5;
    const int tt = w & 3, half = w >> 2;
    u32x4 xr[2][7];
    {
        const char* xb = x + (size_t)(Rb * 128 + 16 * w) * 896;
        const unsigned vo = (l >> 3) * 896 + (l & 7) * 16;
#pragma unroll
        for (int g = 0; g < 2; ++g)
#pragma unroll
            for (int i = 0; i < 7; ++i) xr[g][i] = gload16(xb + g * 8 * 896 + i * 128, vo);
    }
    {
        const unsigned vo = (l >> 2) * 2688 + ((l & 3) < 3 ? (l & 3) * 16 : 40);
#pragma unroll
        for (int i = 0; i < 11; ++i) {
            const int n = w + 8 * i;
            if (n < 84) { const int m = n / 28, kb = n % 28;
                glds16(wqkv + (size_t)kb * 16 * 2688 + m * 896 + c * 56, vo, rfl(lds0 + m * A_SLOT + kb * 1024)); }
        }
    }
    asm volatile("s_waitcnt vmcnt(10)" : "+v"(xr[0][0]), "+v"(xr[0][1]), "+v"(xr[0][2]), "+v"(xr[0][3]), "+v"(xr[0][4]), "+v"(xr[0][5]), "+v"(xr[0][6]),
                 "+v"(xr[1][0]), "+v"(xr[1][1]), "+v"(xr[1][2]), "+v"(xr[1][3]), "+v"(xr[1][4]), "+v"(xr[1][5]), "+v"(xr[1][6]) :: "memory");
#pragma unroll
    for (int g = 0; g < 2; ++g) {
        float sm = 0.f, sq = 0.f; unsigned ones_ = 0x3f803f80u; asm volatile("" : "+v"(ones_)); const bf16x2 one = __builtin_bit_cast(bf16x2, ones_);
#pragma unroll
        for (int i = 0; i < 7; ++i)
#pragma unroll
            for (int e = 0; e < 4; ++e) { const bf16x2 v = asbf2(xr[g][i][e]); sm = __builtin_amdgcn_fdot2_f32_bf16(v, one, sm, false); sq = __builtin_amdgcn_fdot2_f32_bf16(v, v, sq, false); }
        sm += dppf<0xB1>(sm); sq += dppf<0xB1>(sq); sm += dppf<0x4E>(sm); sq += dppf<0x4E>(sq); sm += dppf<0x141>(sm); sq += dppf<0x141>(sq);
        const float mean = sm * (1.0f / 448.0f); const float var = __builtin_fmaxf(sq * (1.0f / 448.0f) - mean * mean, 0.f);
        const float rstd = __builtin_amdgcn_rsqf(var + 1e-5f), nb = -mean * rstd;
#pragma unroll
        for (int i = 0; i < 7; ++i)
#pragma unroll
            for (int e = 0; e < 4; ++e) { const unsigned v = xr[g][i][e]; xr[g][i][e] = cvtpk(__builtin_fmaf(bflo(v), rstd, nb), __builtin_fmaf(bfhi(v), rstd, nb)); }
    }
    bf16x8 xf[28];
    {
        LAS char* xs = lds + A_XS; const int sub = l & 7;
        const int wrow = 16 * w + (l >> 3);
        const LAS char* rd = xs + (32 * tt + r) * A_XROW + h * 16;
#pragma unroll
        for (int g = 0; g < 2; ++g) {
#pragma unroll
            for (int i = 0; i < 3; ++i) *(LAS u32x4*)(xs + (wrow + 8 * g) * A_XROW + (sub + 8 * i) * 16) = xr[g][i];
            if (sub < 4) *(LAS u32x4*)(xs + (wrow + 8 * g) * A_XROW + (sub + 24) * 16) = xr[g][3];
        }
        WAIT_LGKM0(); BARRIER();
#pragma unroll
        for (int s = 0; s < 14; ++s) xf[s] = ldsfrag(rd + s * 32);
        WAIT_LGKM0(); BARRIER();
#pragma unroll
        for (int g = 0; g < 2; ++g) {
            if (sub >= 4) *(LAS u32x4*)(xs + (wrow + 8 * g) * A_XROW + (sub - 4) * 16) = xr[g][3];
#pragma unroll
            for (int i = 4; i < 7; ++i) *(LAS u32x4*)(xs + (wrow + 8 * g) * A_XROW + (sub + 8 * i - 28) * 16) = xr[g][i];
        }
        WAIT_LGKM0(); BARRIER();
#pragma unroll
        for (int s = 0; s < 14; ++s) xf[14 + s] = ldsfrag(rd + s * 32);
        WAIT_LGKM0(); WAIT_VM(0); BARRIER();
    }
    const bool ffn = c < 14;
    const unsigned vo1 = (l >> 2) * 3584 + (l & 3) * 16;
    if (ffn) {
#pragma unroll
        for (int i = 0; i < 7; ++i) { const int n = w + 8 * i, t = n / 28, kb = n % 28;
            glds16(w1 + (size_t)kb * 16 * 3584 + (4 * c + t) * 64, vo1, rfl(lds0 + A_XS + t * A_SLOT + kb * 1024)); }
    }
    const int troff = (8 * h + ((l & 15) >> 2)) * 64 + ((l >> 4) & 1) * 32 + (l & 3) * 8;
    const int tglob = Rb * 4 + tt;
    if (half == 0) {
        f32x16 aq = {}, ak = {};
        const LAS char* pq = lds + troff; const LAS char* pk = lds + A_SLOT + troff;
#pragma unroll
        for (int s = 0; s < 28; ++s) {
            aq = MFMA(trfrag(pq + s * 1024, pq + s * 1024 + 256), xf[s], aq);
            ak = MFMA(trfrag(pk + s * 1024, pk + s * 1024 + 256), xf[s], ak);
        }
        const float qs = 0.18898223650461361f * 1.4426950408889634f;
        float nk = 0.f;
#pragma unroll
        for (int i = 0; i < 16; ++i) { const bool pad = (i >= 12) && (h == 0); aq[i] = pad ? 0.f : aq[i] * qs; const float kv = pad ? 0.f : ak[i]; nk = __builtin_fmaf(kv, kv, nk); ak[i] = kv; }
        if (h == 0) ak[12] = 1.0f;
        { auto sw = __builtin_amdgcn_permlane32_swap(__float_as_uint(nk), __float_as_uint(nk), false, false); nk = __uint_as_float(sw[0]) + __uint_as_float(sw[1]); }
        nk = __builtin_fmaxf(nk, dppf<0xB1>(nk)); nk = __builtin_fmaxf(nk, dppf<0x4E>(nk)); nk = __builtin_fmaxf(nk, dppf<0x141>(nk)); nk = __builtin_fmaxf(nk, dppf<0x140>(nk));
        { auto sw = __builtin_amdgcn_permlane16_swap(__float_as_uint(nk), __float_as_uint(nk), false, false); nk = __builtin_fmaxf(__uint_as_float(sw[0]), __uint_as_float(sw[1])); }
        char* qd = ws + WS_Q + (size_t)(c * 64 + tglob) * 2048 + l * 16;
        char* kd = ws + WS_KV + (size_t)(c * 64 + tglob) * 4096 + l * 16;
        *(bf16x8*)qd = pack8(aq, 0); *(bf16x8*)(qd + 1024) = pack8(aq, 1);
        *(bf16x8*)kd = pack8(ak, 0); *(bf16x8*)(kd + 1024) = pack8(ak, 1);
        if (l == 0) *(float*)(ws + WS_KN + (c * 64 + tglob) * 4) = nk;
    } else {
        f32x16 av = {};
        const LAS char* pv = lds + 2 * A_SLOT + troff;
#pragma unroll
        for (int s = 0; s < 28; ++s) av = MFMA(xf[s], trfrag(pv + s * 1024, pv + s * 1024 + 256), av);
        const bool pad = (r >= 24) && (r < 28); const float pv1 = (r == 24) ? 1.0f : 0.f;
#pragma unroll
        for (int i = 0; i < 16; ++i) av[i] = pad ? pv1 : av[i];
        char* vd = ws + WS_KV + (size_t)(c * 64 + tglob) * 4096 + 2048 + l * 16;
        *(bf16x8*)vd = pack8(av, 0); *(bf16x8*)(vd + 1024) = pack8(av, 1);
    }
    if (!ffn) return;
    WAIT_VM(0); BARRIER();
#pragma unroll
    for (int i = 0; i < 7; ++i) { const int n = w + 8 * i, t = n / 28, kb = n % 28;
        glds16(w1 + (size_t)kb * 16 * 3584 + (4 * c + 2 + t) * 64, vo1, rfl(lds0 + t * A_SLOT + kb * 1024)); }
#pragma unroll
    for (int u = 0; u < 2; ++u) {
        const int ft = 4 * c + 2 * u + half;
        u32x2 bb[4];
#pragma unroll
        for (int g = 0; g < 4; ++g) bb[g] = *(const u32x2*)(b1 + (ft * 32 + 8 * g + 4 * h) * 2);
        if (u == 1) { WAIT_VM(0); BARRIER(); }
        f32x16 a = {};
        const LAS char* pw = lds + (u == 0 ? A_XS : 0) + half * A_SLOT + troff;
#pragma unroll
        for (int s = 0; s < 28; ++s) a = MFMA(trfrag(pw + s * 1024, pw + s * 1024 + 256), xf[s], a);
#pragma unroll
        for (int g = 0; g < 4; ++g) {
            a[4 * g + 0] = gelu_tanh(a[4 * g + 0] + bflo(bb[g][0])); a[4 * g + 1] = gelu_tanh(a[4 * g + 1] + bfhi(bb[g][0]));
            a[4 * g + 2] = gelu_tanh(a[4 * g + 2] + bflo(bb[g][1])); a[4 * g + 3] = gelu_tanh(a[4 * g + 3] + bfhi(bb[g][1]));
        }
        char* hd = ws + WS_H + ((size_t)tglob * 56 + ft) * 2048 + l * 16;
        *(bf16x8*)hd = pack8(a, 0); *(bf16x8*)(hd + 1024) = pack8(a, 1);
    }
}

constexpr int B_NR = 4;
constexpr int B_RED = B_NR * 16384;
constexpr int B_LDS = B_RED + 16384;

template <bool SAFE>
DI void attn_loop(f32x16& o, const bf16x8 q0, const bf16x8 q1, const char* kvsrc, LAS char* lds, unsigned lds0, int w, int l) {
    const int kh = w >> 2;
    const unsigned vo = l * 16;
    const char* src = kvsrc + (size_t)kh * 131072 + (w & 3) * 2048;
    const unsigned dst = lds0 + w * 2048;
#pragma unroll
    for (int i = 0; i < B_NR; ++i) { glds16(src + i * 8192, vo, dst + i * 16384); glds16(src + i * 8192 + 1024, vo, dst + i * 16384 + 1024); }
    const LAS char* rb = lds + kh * 8192 + l * 16;
    WAIT_VM(4); BARRIER();
    float m = -1e30f;
    const f32x16 zero = {};
    bf16x8 k0 = ldsfrag(rb), k1 = ldsfrag(rb + 1024);
    f32x16 sc = MFMA(k0, q0, zero); sc = MFMA(k1, q1, sc);
    k0 = ldsfrag(rb + 4096); k1 = ldsfrag(rb + 4096 + 1024);
    bf16x8 v0 = ldsfrag(rb + 2048), v1 = ldsfrag(rb + 3072);
    for (int i0 = 0; i0 < 16; i0 += B_NR) {
#pragma unroll
        for (int ii = 0; ii < B_NR; ++ii) {
            const int i = i0 + ii;
            const int cur = ii * 16384, nxt = ((ii + 1) % B_NR) * 16384;
#pragma unroll
            for (int t = 0; t < 2; ++t) {
                f32x16 sn = MFMA(k0, q0, zero); sn = MFMA(k1, q1, sn);
                k0 = ldsfrag(rb + nxt + t * 4096); k1 = ldsfrag(rb + nxt + t * 4096 + 1024);
                if constexpr (SAFE) {
                    float mx = sc[0];
#pragma unroll
                    for (int e = 1; e < 16; ++e) mx = __builtin_fmaxf(mx, sc[e]);
                    { auto sw = __builtin_amdgcn_permlane32_swap(__float_as_uint(mx), __float_as_uint(mx), false, false); mx = __builtin_fmaxf(__uint_as_float(sw[0]), __uint_as_float(sw[1])); }
                    const float mn = __builtin_fmaxf(m, mx), al = __builtin_amdgcn_exp2f(m - mn); m = mn;
#pragma unroll
                    for (int e = 0; e < 16; ++e) { o[e] *= al; sc[e] -= mn; }
                }
#pragma unroll
                for (int e = 0; e < 16; ++e) sc[e] = __builtin_amdgcn_exp2f(sc[e]);
                o = MFMA(v0, pack8(sc, 0), o); o = MFMA(v1, pack8(sc, 1), o);
                const int voff = (t == 0) ? cur + 4096 : nxt;
                v0 = ldsfrag(rb + voff + 2048); v1 = ldsfrag(rb + voff + 3072);
                sc = sn;
            }
            if (i < 15) {
                if (i + B_NR <= 16) WAIT_VM(2); else WAIT_VM(0);
                BARRIER();
                if (i + B_NR < 16) { glds16(src + (i + B_NR) * 8192, vo, dst + cur); glds16(src + (i + B_NR) * 8192 + 1024, vo, dst + cur + 1024); }
            }
        }
    }
    if constexpr (SAFE) {
        o[13] = (l < 32) ? m : o[13];
    }
}

DI void phaseB(char* __restrict__ ws, char* lds_generic, int hd, int qb) {
    LAS char* lds = (LAS char*)lds_generic;
    const unsigned lds0 = (unsigned)(uintptr_t)lds_generic;
    const int tid = threadIdx.x, l = tid & 63, w = rfl(tid >> 6) & 7, h = l >> 5;
    const int qt = w & 3, kh = w >> 2, tglob = qb * 4 + qt;
    const char* qsrc = ws + WS_Q + (size_t)(hd * 64 + tglob) * 2048 + l * 16;
    u32x4 q0 = *(const u32x4*)qsrc, q1 = *(const u32x4*)(qsrc + 1024);
    float kn = *(const float*)(ws + WS_KN + (hd * 64 + l) * 4);
    kn = __builtin_fmaxf(kn, dppf<0xB1>(kn)); kn = __builtin_fmaxf(kn, dppf<0x4E>(kn)); kn = __builtin_fmaxf(kn, dppf<0x141>(kn)); kn = __builtin_fmaxf(kn, dppf<0x140>(kn));
    { auto sw = __builtin_amdgcn_permlane16_swap(__float_as_uint(kn), __float_as_uint(kn), false, false); kn = __builtin_fmaxf(__uint_as_float(sw[0]), __uint_as_float(sw[1])); }
    { auto sw = __builtin_amdgcn_permlane32_swap(__float_as_uint(kn), __float_as_uint(kn), false, false); kn = __builtin_fmaxf(__uint_as_float(sw[0]), __uint_as_float(sw[1])); }
    float qn = 0.f;
#pragma unroll
    for (int e = 0; e < 4; ++e) { qn = __builtin_amdgcn_fdot2_f32_bf16(asbf2(q0[e]), asbf2(q0[e]), qn, false);
                                  qn = __builtin_amdgcn_fdot2_f32_bf16(asbf2(q1[e]), asbf2(q1[e]), qn, false); }
    { auto sw = __builtin_amdgcn_permlane32_swap(__float_as_uint(qn), __float_as_uint(qn), false, false); qn = __uint_as_float(sw[0]) + __uint_as_float(sw[1]); }
    const float bound = __builtin_sqrtf(qn * kn) * 1.02f + 1e-3f;
    const bool safe = __any(!(bound <= 48.0f));
    const char* kvsrc = ws + WS_KV + (size_t)hd * 262144;
    f32x16 o = {};
    if (!safe) {
        if (h == 0) q1[2] = cvtpk(-bound, 0.f);
        attn_loop<false>(o, __builtin_bit_cast(bf16x8, q0), __builtin_bit_cast(bf16x8, q1), kvsrc, lds, lds0, w, l);
    } else {
        attn_loop<true>(o, __builtin_bit_cast(bf16x8, q0), __builtin_bit_cast(bf16x8, q1), kvsrc, lds, lds0, w, l);
    }
    LAS char* red = lds + B_RED + qt * 4096 + l * 16;
    if (kh == 1) {
#pragma unroll
        for (int g = 0; g < 4; ++g) *(LAS f32x4*)(red + g * 1024) = (f32x4){o[4 * g], o[4 * g + 1], o[4 * g + 2], o[4 * g + 3]};
    }
    WAIT_LGKM0(); BARRIER();
    if (kh == 0) {
        f32x16 p;
#pragma unroll
        for (int g = 0; g < 4; ++g) { const f32x4 t = *(const LAS f32x4*)(red + g * 1024); p[4 * g] = t[0]; p[4 * g + 1] = t[1]; p[4 * g + 2] = t[2]; p[4 * g + 3] = t[3]; }
        if (safe) {
            float ma = o[13], mb = p[13];
            { auto sw = __builtin_amdgcn_permlane32_swap(__float_as_uint(ma), __float_as_uint(ma), false, false); ma = __uint_as_float(sw[0]); }
            { auto sw = __builtin_amdgcn_permlane32_swap(__float_as_uint(mb), __float_as_uint(mb), false, false); mb = __uint_as_float(sw[0]); }
            const float mn = __builtin_fmaxf(ma, mb), fa = __builtin_amdgcn_exp2f(ma - mn), fb = __builtin_amdgcn_exp2f(mb - mn);
#pragma unroll
            for (int e = 0; e < 16; ++e) o[e] = o[e] * fa + p[e] * fb;
        } else {
#pragma unroll
            for (int e = 0; e < 16; ++e) o[e] += p[e];
        }
        float ls = o[12];
        { auto sw = __builtin_amdgcn_permlane32_swap(__float_as_uint(ls), __float_as_uint(ls), false, false); ls = __uint_as_float(sw[0]); }
        const float inv = 1.0f / ls;
#pragma unroll
        for (int e = 0; e < 16; ++e) o[e] = (e >= 12 && h == 0) ? 0.f : o[e] * inv;
        char* od = ws + WS_O + ((size_t)tglob * 16 + hd) * 2048 + l * 16;
        *(bf16x8*)od = pack8(o, 0); *(bf16x8*)(od + 1024) = pack8(o, 1);
    }
}

constexpr int C_NR = 6;
constexpr int C_WO = C_NR * 16384;
constexpr int C_LDS = C_WO + 448 * 128;
constexpr int C_RROW = 272;

DI void phaseC(const char* __restrict__ x, const char* __restrict__ wo, const char* __restrict__ w2, const char* __restrict__ b2,
               const char* __restrict__ ws, char* __restrict__ out, char* lds_generic, int rb, int ng) {
    LAS char* lds = (LAS char*)lds_generic;
    const unsigned lds0 = (unsigned)(uintptr_t)lds_generic;
    const int tid = threadIdx.x, l = tid & 63, w = rfl(tid >> 6) & 7, h = l >> 5;
    const int tt = w & 1, kq = w >> 1, tglob = rb * 2 + tt;
    const unsigned vo = (l >> 3) * 896 + (((l & 7) ^ (((l >> 4) & 1) << 2)) * 16);
    const char* w2b = w2 + ng * 128;
    const char* w2src = w2b + (size_t)(448 * kq + 16 * tt) * 896;
    const unsigned w2dst = lds0 + kq * 4096 + tt * 2048;
#pragma unroll
    for (int i = 0; i < 7; ++i) { const int n = w + 8 * i; glds16(wo + ng * 128 + (size_t)n * 8 * 896, vo, rfl(lds0 + C_WO + n * 1024)); }
#pragma unroll
    for (int st = 0; st < C_NR; ++st) { glds16(w2src + (size_t)st * 32 * 896, vo, w2dst + st * 16384); glds16(w2src + (size_t)(st * 32 + 8) * 896, vo, w2dst + st * 16384 + 1024); }
    const int q = (l & 15) >> 2, p = l & 3, cb = (l >> 4) & 1;
    const int tr0 = (4 * h + q) * 128 + (((2 * cb + (p >> 1)) ^ ((q >> 1) << 2)) * 16) + (p & 1) * 8, tr1 = tr0 ^ 64;
    const char* hsrc = ws + WS_H + ((size_t)tglob * 56 + 14 * kq) * 2048 + l * 16;
    const char* osrc = ws + WS_O + ((size_t)tglob * 16 + 4 * kq) * 2048 + l * 16;
    f32x16 a0 = {}, a1 = {};
    bf16x8 hf[2];
    hf[0] = *(const bf16x8*)hsrc; hf[1] = *(const bf16x8*)(hsrc + 1024);
    for (int st = 0; st < 14; ++st) {
        if (st + C_NR <= 14) WAIT_VM(10); else WAIT_VM(0);
        BARRIER();
        const LAS char* wb = lds + (st % C_NR) * 16384 + kq * 4096;
        bf16x8 hn[2];
        if (st + 1 < 14) { hn[0] = *(const bf16x8*)(hsrc + (st + 1) * 2048); hn[1] = *(const bf16x8*)(hsrc + (st + 1) * 2048 + 1024); }
#pragma unroll
        for (int s = 0; s < 2; ++s) {
            a0 = MFMA(trfrag(wb + tr0 + s * 2048, wb + tr0 + s * 2048 + 1024), hf[s], a0);
            a1 = MFMA(trfrag(wb + tr1 + s * 2048, wb + tr1 + s * 2048 + 1024), hf[s], a1);
        }
        if (st + 1 < 14) { hf[0] = hn[0]; hf[1] = hn[1]; }
        WAIT_LGKM0(); BARRIER();
        if (st + C_NR < 14) { glds16(w2src + (size_t)(st + C_NR) * 32 * 896, vo, w2dst + (st % C_NR) * 16384); glds16(w2src + (size_t)((st + C_NR) * 32 + 8) * 896, vo, w2dst + (st % C_NR) * 16384 + 1024); }
    }
#pragma unroll
    for (int i = 0; i < 4; ++i) {
        const LAS char* wb = lds + C_WO + (28 * (4 * kq + i)) * 128;
        const bf16x8 o0 = *(const bf16x8*)(osrc + i * 2048), o1 = *(const bf16x8*)(osrc + i * 2048 + 1024);
        a0 = MFMA(trfrag(wb + tr0, wb + tr0 + 1024), o0, a0);
        a1 = MFMA(trfrag(wb + tr1, wb + tr1 + 1024), o0, a1);
        a0 = MFMA(trfrag(wb + tr0 + 2048, wb + tr0 + 2048 + 512), o1, a0);
        a1 = MFMA(trfrag(wb + tr1 + 2048, wb + tr1 + 2048 + 512), o1, a1);
    }
    WAIT_LGKM0(); BARRIER();
    {
        LAS char* rw = lds + (kq * 64 + tt * 32 + (l & 31)) * C_RROW + h * 16;
#pragma unroll
        for (int g = 0; g < 4; ++g) {
            *(LAS f32x4*)(rw + g * 32) = (f32x4){a0[4 * g], a0[4 * g + 1], a0[4 * g + 2], a0[4 * g + 3]};
            *(LAS f32x4*)(rw + 128 + g * 32) = (f32x4){a1[4 * g], a1[4 * g + 1], a1[4 * g + 2], a1[4 * g + 3]};
        }
    }
    const int tok = tid >> 3, ch = tid & 7;
    const size_t goff = (size_t)(rb * 64 + tok) * 896 + ng * 128 + ch * 16;
    const u32x4 xv = *(const u32x4*)(x + goff), bv = *(const u32x4*)(b2 + ng * 128 + ch * 16);
    WAIT_LGKM0(); BARRIER();
    f32x4 s0 = {}, s1 = {};
#pragma unroll
    for (int k = 0; k < 4; ++k) { const LAS char* rr = lds + (k * 64 + tok) * C_RROW + ch * 32; s0 += *(const LAS f32x4*)rr; s1 += *(const LAS f32x4*)(rr + 16); }
    u32x4 ov;
    ov[0] = cvtpk(s0[0] + bflo(xv[0]) + bflo(bv[0]), s0[1] + bfhi(xv[0]) + bfhi(bv[0]));
    ov[1] = cvtpk(s0[2] + bflo(xv[1]) + bflo(bv[1]), s0[3] + bfhi(xv[1]) + bfhi(bv[1]));
    ov[2] = cvtpk(s1[0] + bflo(xv[2]) + bflo(bv[2]), s1[1] + bfhi(xv[2]) + bfhi(bv[2]));
    ov[3] = cvtpk(s1[2] + bflo(xv[3]) + bflo(bv[3]), s1[3] + bfhi(xv[3]) + bfhi(bv[3]));
    *(u32x4*)(out + goff) = ov;
}

__global__ void __launch_bounds__(512, 2) kA(const char* x, const char* wqkv, const char* w1, const char* b1, char* ws) {
    extern __shared__ __attribute__((aligned(16))) char lds[];
    const int b = blockIdx.x, xcd = b & 7, i = b >> 3;
    phaseA(x, wqkv, w1, b1, ws, lds, i >> 1, 2 * xcd + (i & 1));
}
__global__ void __launch_bounds__(512, 2) kB(char* ws) {
    extern __shared__ __attribute__((aligned(16))) char lds[];
    const int b = blockIdx.x, xcd = b & 7, i = b >> 3;
    phaseB(ws, lds, 2 * xcd + (i & 1), i >> 1);
}
__global__ void __launch_bounds__(512, 2) kC(const char* x, const char* wo, const char* w2, const char* b2, const char* ws, char* out) {
    extern __shared__ __attribute__((aligned(16))) char lds[];
    const int b = blockIdx.x, xcd = b & 7, i = b >> 3;
    if (i >= 28) return;
    phaseC(x, wo, w2, b2, ws, out, lds, 4 * xcd + i / 7, i % 7);
}

extern "C" void kernel_launch(void* const* d_in, const int* in_sizes, int n_in, void* d_out, int out_size, void* d_ws, size_t ws_size, hipStream_t stream) {
    const char* x = (const char*)d_in[0]; const char* wqkv = (const char*)d_in[1]; const char* wo = (const char*)d_in[2];
    const char* w1 = (const char*)d_in[4]; const char* b1 = (const char*)d_in[5]; const char* w2 = (const char*)d_in[6]; const char* b2 = (const char*)d_in[7];
    (void)hipFuncSetAttribute((const void*)kA, hipFuncAttributeMaxDynamicSharedMemorySize, A_LDS);
    (void)hipFuncSetAttribute((const void*)kB, hipFuncAttributeMaxDynamicSharedMemorySize, B_LDS);
    (void)hipFuncSetAttribute((const void*)kC, hipFuncAttributeMaxDynamicSharedMemorySize, C_LDS);
    hipLaunchKernelGGL(kA, dim3(256), dim3(512), A_LDS, stream, x, wqkv, w1, b1, (char*)d_ws);
    hipLaunchKernelGGL(kB, dim3(256), dim3(512), B_LDS, stream, (char*)d_ws);
    hipLaunchKernelGGL(kC, dim3(256), dim3(512), C_LDS, stream, x, wo, w2, b2, (const char*)d_ws, (char*)d_out);
}
```

```cpp
#include <hip/hip_runtime.h>
#include <hip/hip_bf16.h>
#include <stdint.h>

typedef __bf16 bf16x2 __attribute__((ext_vector_type(2)));
typedef __bf16 bf16x8 __attribute__((ext_vector_type(8)));
typedef float f32x2 __attribute__((ext_vector_type(2)));
typedef float f32x4 __attribute__((ext_vector_type(4)));
typedef float f32x16 __attribute__((ext_vector_type(16)));
typedef short s16x4 __attribute__((ext_vector_type(4)));
typedef unsigned u32x2 __attribute__((ext_vector_type(2)));
typedef unsigned u32x4 __attribute__((ext_vector_type(4)));
typedef __attribute__((address_space(1))) unsigned long long gu64;
#define LAS __attribute__((address_space(3)))
#define DI __device__ __forceinline__
#define MFMA(a, b, c) __builtin_amdgcn_mfma_f32_32x32x16_bf16((a), (b), (c), 0, 0, 0)

constexpr size_t WS_EPOCH = 0;
constexpr size_t WS_FKV = 256;
constexpr size_t WS_FH = WS_FKV + 16 * 256;
constexpr size_t WS_FO = WS_FH + 16 * 256;
constexpr size_t WS_KN = 16384;
constexpr size_t WS_KV = 32768;
constexpr size_t WS_H = WS_KV + 16 * 64 * 4096;
constexpr size_t WS_O = WS_H + (size_t)64 * 56 * 2048;
constexpr size_t WS_END = WS_O + 64 * 16 * 2048;
#define STAMPC(wsp, k) do {} while (0)
#define STAMP(wsp, k) do {} while (0)

DI unsigned cvtpk(float lo, float hi) { f32x2 v = {lo, hi}; bf16x2 b = __builtin_convertvector(v, bf16x2); return __builtin_bit_cast(unsigned, b); }
DI bf16x2 asbf2(unsigned v) { return __builtin_bit_cast(bf16x2, v); }
DI bf16x8 asbf8(u32x4 v) { return __builtin_bit_cast(bf16x8, v); }
DI float bflo(unsigned v) { return __uint_as_float(v << 16); }
DI float bfhi(unsigned v) { return __uint_as_float(v & 0xffff0000u); }
DI unsigned rfl(unsigned x) { return __builtin_amdgcn_readfirstlane(x); }
DI const void* uptr(const void* p) { const uint64_t v = (uint64_t)p; return (const void*)(((uint64_t)rfl((unsigned)(v >> 32)) << 32) | rfl((unsigned)v)); }
DI void glds16(const void* sbase_, unsigned voff, unsigned lds_dst_) {
    unsigned sv; const void* sbase = uptr(sbase_); const unsigned lds_dst = rfl(lds_dst_);
    asm volatile("s_mov_b32 %0, m0\n\ts_mov_b32 m0, %3\n\ts_nop 4\n\tglobal_load_lds_dwordx4 %1, %2\n\ts_mov_b32 m0, %0"
                 : "=&s"(sv) : "v"(voff), "s"(sbase), "s"(lds_dst) : "memory");
}
DI u32x4 gload16(const void* sbase, unsigned voff) { u32x4 r; asm volatile("global_load_dwordx4 %0, %1, %2" : "=v"(r) : "v"(voff), "s"(uptr(sbase)) : "memory"); return r; }
DI u32x4 gload16_sc1(const void* sbase, unsigned voff) { u32x4 r; asm volatile("s_nop 4\n\tglobal_load_dwordx4 %0, %1, %2 sc1" : "=v"(r) : "v"(voff), "s"(uptr(sbase)) : "memory"); return r; }
DI void gstore16_sc1(void* sbase, unsigned voff, u32x4 v) { asm volatile("s_nop 4\n\tglobal_store_dwordx4 %0, %1, %2 sc1\n\ts_nop 1" :: "v"(voff), "v"(v), "s"(uptr(sbase)) : "memory"); }
DI s16x4 trread(const LAS char* p) { return __builtin_bit_cast(s16x4, __builtin_amdgcn_ds_read_tr16_b64_v4i16((LAS s16x4*)p)); }
DI bf16x8 trfrag(const LAS char* p0, const LAS char* p1) {
    s16x4 a = trread(p0), b = trread(p1);
    return __builtin_bit_cast(bf16x8, __builtin_shufflevector(a, b, 0, 1, 2, 3, 4, 5, 6, 7));
}
DI bf16x8 ldsfrag(const LAS char* p) { return *(const LAS bf16x8*)p; }
template <int CTRL> DI float dppf(float v) { return __int_as_float(__builtin_amdgcn_update_dpp(0, __float_as_int(v), CTRL, 0xf, 0xf, true)); }
DI u32x4 pack8(const f32x16& a, int s) {
    u32x4 p = {cvtpk(a[8 * s], a[8 * s + 1]), cvtpk(a[8 * s + 2], a[8 * s + 3]), cvtpk(a[8 * s + 4], a[8 * s + 5]), cvtpk(a[8 * s + 6], a[8 * s + 7])};
    return p;
}
DI float swap32_lo(float v) { auto sw = __builtin_amdgcn_permlane32_swap(__float_as_uint(v), __float_as_uint(v), false, false); return __uint_as_float(sw[0]); }
DI float xhalf_sum(float v) { auto sw = __builtin_amdgcn_permlane32_swap(__float_as_uint(v), __float_as_uint(v), false, false); return __uint_as_float(sw[0]) + __uint_as_float(sw[1]); }
DI float xhalf_max(float v) { auto sw = __builtin_amdgcn_permlane32_swap(__float_as_uint(v), __float_as_uint(v), false, false); return __builtin_fmaxf(__uint_as_float(sw[0]), __uint_as_float(sw[1])); }
DI float row32_max(float v) {
    v = __builtin_fmaxf(v, dppf<0xB1>(v)); v = __builtin_fmaxf(v, dppf<0x4E>(v)); v = __builtin_fmaxf(v, dppf<0x141>(v)); v = __builtin_fmaxf(v, dppf<0x140>(v));
    auto sw = __builtin_amdgcn_permlane16_swap(__float_as_uint(v), __float_as_uint(v), false, false); return __builtin_fmaxf(__uint_as_float(sw[0]), __uint_as_float(sw[1]));
}
#define BARRIER() asm volatile("s_barrier" ::: "memory")
#define WAIT_VM(n) asm volatile("s_waitcnt vmcnt(" #n ")" ::: "memory")
#define WAIT_LGKM0() asm volatile("s_waitcnt lgkmcnt(0)" ::: "memory")

typedef unsigned long long u64;
DI void flag_store(char* ws, size_t off, u64 tag) { __hip_atomic_store((gu64*)(ws + off), tag, __ATOMIC_RELAXED, __HIP_MEMORY_SCOPE_AGENT); }
DI void flag_wait(const char* ws, size_t off, int n, u64 tag, int l) {
    gu64* p = (gu64*)(ws + off) + (l < n ? l : 0);
    for (unsigned spins = 0; spins < (1u << 20); ++spins) {
        const u64 v = __hip_atomic_load(p, __ATOMIC_RELAXED, __HIP_MEMORY_SCOPE_AGENT);
        if (__all(v == tag)) break;
        __builtin_amdgcn_s_sleep(2);
    }
}
#define ATTN_FAST_ASM \
    "s_mov_b32 s43, m0\n\t" \
    "s_waitcnt vmcnt(12)\n\t" \
    "s_barrier\n\t" \
    "ds_read_b128 v[56:59], v96 offset:0\n\t" \
    "ds_read_b128 v[60:63], v96 offset:1024\n\t" \
    "ds_read_b128 v[64:67], v96 offset:4096\n\t" \
    "ds_read_b128 v[68:71], v96 offset:5120\n\t" \
    "ds_read_b128 v[72:75], v96 offset:2048\n\t" \
    "ds_read_b128 v[76:79], v96 offset:3072\n\t" \
    "s_waitcnt lgkmcnt(4)\n\t" \
    "v_mfma_f32_32x32x16_bf16 v[16:31], v[56:59], v[48:51], 0\n\t" \
    "v_mfma_f32_32x32x16_bf16 v[16:31], v[60:63], v[52:55], v[16:31]\n\t" \
    "s_nop 7\n\t" \
    "s_nop 7\n\t" \
    "v_exp_f32 v16, v16\n\t" \
    "v_exp_f32 v17, v17\n\t" \
    "ds_read_b128 v[56:59], v96 offset:8192\n\t" \
    "v_exp_f32 v18, v18\n\t" \
    "v_exp_f32 v19, v19\n\t" \
    "v_cvt_pk_bf16_f32 v88, v16, v17\n\t" \
    "v_cvt_pk_bf16_f32 v89, v18, v19\n\t" \
    "s_waitcnt lgkmcnt(2)\n\t" \
    "v_mfma_f32_32x32x16_bf16 v[32:47], v[64:67], v[48:51], 0\n\t" \
    "ds_read_b128 v[60:63], v96 offset:9216\n\t" \
    "v_exp_f32 v20, v20\n\t" \
    "v_exp_f32 v21, v21\n\t" \
    "v_exp_f32 v22, v22\n\t" \
    "v_exp_f32 v23, v23\n\t" \
    "v_cvt_pk_bf16_f32 v90, v20, v21\n\t" \
    "v_cvt_pk_bf16_f32 v91, v22, v23\n\t" \
    "v_mfma_f32_32x32x16_bf16 v[32:47], v[68:71], v[52:55], v[32:47]\n\t" \
    "ds_read_b128 v[80:83], v96 offset:6144\n\t" \
    "v_exp_f32 v24, v24\n\t" \
    "v_exp_f32 v25, v25\n\t" \
    "v_exp_f32 v26, v26\n\t" \
    "v_exp_f32 v27, v27\n\t" \
    "v_cvt_pk_bf16_f32 v92, v24, v25\n\t" \
    "v_cvt_pk_bf16_f32 v93, v26, v27\n\t" \
    "v_mfma_f32_32x32x16_bf16 v[0:15], v[72:75], v[88:91], 0\n\t" \
    "ds_read_b128 v[84:87], v96 offset:7168\n\t" \
    "v_exp_f32 v28, v28\n\t" \
    "v_exp_f32 v29, v29\n\t" \
    "v_exp_f32 v30, v30\n\t" \
    "v_exp_f32 v31, v31\n\t" \
    "v_cvt_pk_bf16_f32 v94, v28, v29\n\t" \
    "v_cvt_pk_bf16_f32 v95, v30, v31\n\t" \
    "v_exp_f32 v32, v32\n\t" \
    "v_exp_f32 v33, v33\n\t" \
    "v_mfma_f32_32x32x16_bf16 v[0:15], v[76:79], v[92:95], v[0:15]\n\t" \
    "ds_read_b128 v[64:67], v96 offset:12288\n\t" \
    "v_exp_f32 v34, v34\n\t" \
    "v_exp_f32 v35, v35\n\t" \
    "v_cvt_pk_bf16_f32 v88, v32, v33\n\t" \
    "v_cvt_pk_bf16_f32 v89, v34, v35\n\t" \
    "s_waitcnt lgkmcnt(2)\n\t" \
    "v_mfma_f32_32x32x16_bf16 v[16:31], v[56:59], v[48:51], 0\n\t" \
    "ds_read_b128 v[68:71], v96 offset:13312\n\t" \
    "v_exp_f32 v36, v36\n\t" \
    "v_exp_f32 v37, v37\n\t" \
    "v_exp_f32 v38, v38\n\t" \
    "v_exp_f32 v39, v39\n\t" \
    "v_cvt_pk_bf16_f32 v90, v36, v37\n\t" \
    "v_cvt_pk_bf16_f32 v91, v38, v39\n\t" \
    "v_mfma_f32_32x32x16_bf16 v[16:31], v[60:63], v[52:55], v[16:31]\n\t" \
    "ds_read_b128 v[72:75], v96 offset:10240\n\t" \
    "v_exp_f32 v40, v40\n\t" \
    "v_exp_f32 v41, v41\n\t" \
    "v_exp_f32 v42, v42\n\t" \
    "v_exp_f32 v43, v43\n\t" \
    "v_cvt_pk_bf16_f32 v92, v40, v41\n\t" \
    "v_cvt_pk_bf16_f32 v93, v42, v43\n\t" \
    "v_mfma_f32_32x32x16_bf16 v[0:15], v[80:83], v[88:91], v[0:15]\n\t" \
    "ds_read_b128 v[76:79], v96 offset:11264\n\t" \
    "v_exp_f32 v44, v44\n\t" \
    "v_exp_f32 v45, v45\n\t" \
    "v_exp_f32 v46, v46\n\t" \
    "v_exp_f32 v47, v47\n\t" \
    "v_cvt_pk_bf16_f32 v94, v44, v45\n\t" \
    "v_cvt_pk_bf16_f32 v95, v46, v47\n\t" \
    "s_waitcnt vmcnt(8)\n\t" \
    "s_barrier\n\t" \
    "v_exp_f32 v16, v16\n\t" \
    "v_exp_f32 v17, v17\n\t" \
    "v_mfma_f32_32x32x16_bf16 v[0:15], v[84:87], v[92:95], v[0:15]\n\t" \
    "ds_read_b128 v[56:59], v96 offset:32768\n\t" \
    "v_exp_f32 v18, v18\n\t" \
    "v_exp_f32 v19, v19\n\t" \
    "v_cvt_pk_bf16_f32 v88, v16, v17\n\t" \
    "v_cvt_pk_bf16_f32 v89, v18, v19\n\t" \
    "s_waitcnt lgkmcnt(2)\n\t" \
    "v_mfma_f32_32x32x16_bf16 v[32:47], v[64:67], v[48:51], 0\n\t" \
    "ds_read_b128 v[60:63], v96 offset:33792\n\t" \
    "v_exp_f32 v20, v20\n\t" \
    "v_exp_f32 v21, v21\n\t" \
    "v_exp_f32 v22, v22\n\t" \
    "v_exp_f32 v23, v23\n\t" \
    "v_cvt_pk_bf16_f32 v90, v20, v21\n\t" \
    "v_cvt_pk_bf16_f32 v91, v22, v23\n\t" \
    "v_mfma_f32_32x32x16_bf16 v[32:47], v[68:71], v[52:55], v[32:47]\n\t" \
    "ds_read_b128 v[80:83], v96 offset:14336\n\t" \
    "v_exp_f32 v24, v24\n\t" \
    "v_exp_f32 v25, v25\n\t" \
    "v_exp_f32 v26, v26\n\t" \
    "v_exp_f32 v27, v27\n\t" \
    "v_cvt_pk_bf16_f32 v92, v24, v25\n\t" \
    "v_cvt_pk_bf16_f32 v93, v26, v27\n\t" \
    "v_mfma_f32_32x32x16_bf16 v[0:15], v[72:75], v[88:91], v[0:15]\n\t" \
    "ds_read_b128 v[84:87], v96 offset:15360\n\t" \
    "v_exp_f32 v28, v28\n\t" \
    "v_exp_f32 v29, v29\n\t" \
    "v_exp_f32 v30, v30\n\t" \
    "v_exp_f32 v31, v31\n\t" \
    "v_cvt_pk_bf16_f32 v94, v28, v29\n\t" \
    "v_cvt_pk_bf16_f32 v95, v30, v31\n\t" \
    "v_exp_f32 v32, v32\n\t" \
    "v_exp_f32 v33, v33\n\t" \
    "v_mfma_f32_32x32x16_bf16 v[0:15], v[76:79], v[92:95], v[0:15]\n\t" \
    "ds_read_b128 v[64:67], v96 offset:36864\n\t" \
    "v_exp_f32 v34, v34\n\t" \
    "v_exp_f32 v35, v35\n\t" \
    "v_cvt_pk_bf16_f32 v88, v32, v33\n\t" \
    "v_cvt_pk_bf16_f32 v89, v34, v35\n\t" \
    "s_waitcnt lgkmcnt(2)\n\t" \
    "v_mfma_f32_32x32x16_bf16 v[16:31], v[56:59], v[48:51], 0\n\t" \
    "ds_read_b128 v[68:71], v96 offset:37888\n\t" \
    "v_exp_f32 v36, v36\n\t" \
    "v_exp_f32 v37, v37\n\t" \
    "v_exp_f32 v38, v38\n\t" \
    "v_exp_f32 v39, v39\n\t" \
    "v_cvt_pk_bf16_f32 v90, v36, v37\n\t" \
    "v_cvt_pk_bf16_f32 v91, v38, v39\n\t" \
    "v_mfma_f32_32x32x16_bf16 v[16:31], v[60:63], v[52:55], v[16:31]\n\t" \
    "ds_read_b128 v[72:75], v96 offset:34816\n\t" \
    "v_exp_f32 v40, v40\n\t" \
    "v_exp_f32 v41, v41\n\t" \
    "v_exp_f32 v42, v42\n\t" \
    "v_exp_f32 v43, v43\n\t" \
    "v_cvt_pk_bf16_f32 v92, v40, v41\n\t" \
    "v_cvt_pk_bf16_f32 v93, v42, v43\n\t" \
    "v_mfma_f32_32x32x16_bf16 v[0:15], v[80:83], v[88:91], v[0:15]\n\t" \
    "ds_read_b128 v[76:79], v96 offset:35840\n\t" \
    "v_exp_f32 v44, v44\n\t" \
    "v_exp_f32 v45, v45\n\t" \
    "v_exp_f32 v46, v46\n\t" \
    "v_exp_f32 v47, v47\n\t" \
    "v_cvt_pk_bf16_f32 v94, v44, v45\n\t" \
    "v_cvt_pk_bf16_f32 v95, v46, v47\n\t" \
    "v_exp_f32 v16, v16\n\t" \
    "v_exp_f32 v17, v17\n\t" \
    "v_mfma_f32_32x32x16_bf16 v[0:15], v[84:87], v[92:95], v[0:15]\n\t" \
    "ds_read_b128 v[56:59], v96 offset:40960\n\t" \
    "v_exp_f32 v18, v18\n\t" \
    "v_exp_f32 v19, v19\n\t" \
    "v_cvt_pk_bf16_f32 v88, v16, v17\n\t" \
    "v_cvt_pk_bf16_f32 v89, v18, v19\n\t" \
    "s_waitcnt lgkmcnt(2)\n\t" \
    "v_mfma_f32_32x32x16_bf16 v[32:47], v[64:67], v[48:51], 0\n\t" \
    "ds_read_b128 v[60:63], v96 offset:41984\n\t" \
    "v_exp_f32 v20, v20\n\t" \
    "v_exp_f32 v21, v21\n\t" \
    "v_exp_f32 v22, v22\n\t" \
    "v_exp_f32 v23, v23\n\t" \
    "v_cvt_pk_bf16_f32 v90, v20, v21\n\t" \
    "v_cvt_pk_bf16_f32 v91, v22, v23\n\t" \
    "v_mfma_f32_32x32x16_bf16 v[32:47], v[68:71], v[52:55], v[32:47]\n\t" \
    "ds_read_b128 v[80:83], v96 offset:38912\n\t" \
    "v_exp_f32 v24, v24\n\t" \
    "v_exp_f32 v25, v25\n\t" \
    "v_exp_f32 v26, v26\n\t" \
    "v_exp_f32 v27, v27\n\t" \
    "v_cvt_pk_bf16_f32 v92, v24, v25\n\t" \
    "v_cvt_pk_bf16_f32 v93, v26, v27\n\t" \
    "v_mfma_f32_32x32x16_bf16 v[0:15], v[72:75], v[88:91], v[0:15]\n\t" \
    "ds_read_b128 v[84:87], v96 offset:39936\n\t" \
    "v_exp_f32 v28, v28\n\t" \
    "v_exp_f32 v29, v29\n\t" \
    "v_exp_f32 v30, v30\n\t" \
    "v_exp_f32 v31, v31\n\t" \
    "v_cvt_pk_bf16_f32 v94, v28, v29\n\t" \
    "v_cvt_pk_bf16_f32 v95, v30, v31\n\t" \
    "v_exp_f32 v32, v32\n\t" \
    "v_exp_f32 v33, v33\n\t" \
    "v_mfma_f32_32x32x16_bf16 v[0:15], v[76:79], v[92:95], v[0:15]\n\t" \
    "ds_read_b128 v[64:67], v96 offset:45056\n\t" \
    "v_exp_f32 v34, v34\n\t" \
    "v_exp_f32 v35, v35\n\t" \
    "v_cvt_pk_bf16_f32 v88, v32, v33\n\t" \
    "v_cvt_pk_bf16_f32 v89, v34, v35\n\t" \
    "s_waitcnt lgkmcnt(2)\n\t" \
    "v_mfma_f32_32x32x16_bf16 v[16:31], v[56:59], v[48:51], 0\n\t" \
    "ds_read_b128 v[68:71], v96 offset:46080\n\t" \
    "v_exp_f32 v36, v36\n\t" \
    "v_exp_f32 v37, v37\n\t" \
    "v_exp_f32 v38, v38\n\t" \
    "v_exp_f32 v39, v39\n\t" \
    "v_cvt_pk_bf16_f32 v90, v36, v37\n\t" \
    "v_cvt_pk_bf16_f32 v91, v38, v39\n\t" \
    "v_mfma_f32_32x32x16_bf16 v[16:31], v[60:63], v[52:55], v[16:31]\n\t" \
    "ds_read_b128 v[72:75], v96 offset:43008\n\t" \
    "v_exp_f32 v40, v40\n\t" \
    "v_exp_f32 v41, v41\n\t" \
    "v_exp_f32 v42, v42\n\t" \
    "v_exp_f32 v43, v43\n\t" \
    "v_cvt_pk_bf16_f32 v92, v40, v41\n\t" \
    "v_cvt_pk_bf16_f32 v93, v42, v43\n\t" \
    "v_mfma_f32_32x32x16_bf16 v[0:15], v[80:83], v[88:91], v[0:15]\n\t" \
    "ds_read_b128 v[76:79], v96 offset:44032\n\t" \
    "v_exp_f32 v44, v44\n\t" \
    "v_exp_f32 v45, v45\n\t" \
    "v_exp_f32 v46, v46\n\t" \
    "v_exp_f32 v47, v47\n\t" \
    "v_cvt_pk_bf16_f32 v94, v44, v45\n\t" \
    "v_cvt_pk_bf16_f32 v95, v46, v47\n\t" \
    "s_waitcnt vmcnt(4)\n\t" \
    "s_barrier\n\t" \
    "s_mov_b32 m0, s42\n\t" \
    "s_add_u32 s40, s40, 16384\n\t" \
    "s_addc_u32 s41, s41, 0\n\t" \
    "global_load_lds_dwordx4 v98, s[40:41] offset:0\n\t" \
    "global_load_lds_dwordx4 v98, s[40:41] offset:1024\n\t" \
    "global_load_lds_dwordx4 v98, s[40:41] offset:2048\n\t" \
    "global_load_lds_dwordx4 v98, s[40:41] offset:3072\n\t" \
    "v_exp_f32 v16, v16\n\t" \
    "v_exp_f32 v17, v17\n\t" \
    "v_mfma_f32_32x32x16_bf16 v[0:15], v[84:87], v[92:95], v[0:15]\n\t" \
    "ds_read_b128 v[56:59], v97 offset:0\n\t" \
    "v_exp_f32 v18, v18\n\t" \
    "v_exp_f32 v19, v19\n\t" \
    "v_cvt_pk_bf16_f32 v88, v16, v17\n\t" \
    "v_cvt_pk_bf16_f32 v89, v18, v19\n\t" \
    "s_waitcnt lgkmcnt(2)\n\t" \
    "v_mfma_f32_32x32x16_bf16 v[32:47], v[64:67], v[48:51], 0\n\t" \
    "ds_read_b128 v[60:63], v97 offset:1024\n\t" \
    "v_exp_f32 v20, v20\n\t" \
    "v_exp_f32 v21, v21\n\t" \
    "v_exp_f32 v22, v22\n\t" \
    "v_exp_f32 v23, v23\n\t" \
    "v_cvt_pk_bf16_f32 v90, v20, v21\n\t" \
    "v_cvt_pk_bf16_f32 v91, v22, v23\n\t" \
    "v_mfma_f32_32x32x16_bf16 v[32:47], v[68:71], v[52:55], v[32:47]\n\t" \
    "ds_read_b128 v[80:83], v96 offset:47104\n\t" \
    "v_exp_f32 v24, v24\n\t" \
    "v_exp_f32 v25, v25\n\t" \
    "v_exp_f32 v26, v26\n\t" \
    "v_exp_f32 v27, v27\n\t" \
    "v_cvt_pk_bf16_f32 v92, v24, v25\n\t" \
    "v_cvt_pk_bf16_f32 v93, v26, v27\n\t" \
    "v_mfma_f32_32x32x16_bf16 v[0:15], v[72:75], v[88:91], v[0:15]\n\t" \
    "ds_read_b128 v[84:87], v96 offset:48128\n\t" \
    "v_exp_f32 v28, v28\n\t" \
    "v_exp_f32 v29, v29\n\t" \
    "v_exp_f32 v30, v30\n\t" \
    "v_exp_f32 v31, v31\n\t" \
    "v_cvt_pk_bf16_f32 v94, v28, v29\n\t" \
    "v_cvt_pk_bf16_f32 v95, v30, v31\n\t" \
    "v_exp_f32 v32, v32\n\t" \
    "v_exp_f32 v33, v33\n\t" \
    "v_mfma_f32_32x32x16_bf16 v[0:15], v[76:79], v[92:95], v[0:15]\n\t" \
    "ds_read_b128 v[64:67], v97 offset:4096\n\t" \
    "v_exp_f32 v34, v34\n\t" \
    "v_exp_f32 v35, v35\n\t" \
    "v_cvt_pk_bf16_f32 v88, v32, v33\n\t" \
    "v_cvt_pk_bf16_f32 v89, v34, v35\n\t" \
    "s_waitcnt lgkmcnt(2)\n\t" \
    "v_mfma_f32_32x32x16_bf16 v[16:31], v[56:59], v[48:51], 0\n\t" \
    "ds_read_b128 v[68:71], v97 offset:5120\n\t" \
    "v_exp_f32 v36, v36\n\t" \
    "v_exp_f32 v37, v37\n\t" \
    "v_exp_f32 v38, v38\n\t" \
    "v_exp_f32 v39, v39\n\t" \
    "v_cvt_pk_bf16_f32 v90, v36, v37\n\t" \
    "v_cvt_pk_bf16_f32 v91, v38, v39\n\t" \
    "v_mfma_f32_32x32x16_bf16 v[16:31], v[60:63], v[52:55], v[16:31]\n\t" \
    "ds_read_b128 v[72:75], v97 offset:2048\n\t" \
    "v_exp_f32 v40, v40\n\t" \
    "v_exp_f32 v41, v41\n\t" \
    "v_exp_f32 v42, v42\n\t" \
    "v_exp_f32 v43, v43\n\t" \
    "v_cvt_pk_bf16_f32 v92, v40, v41\n\t" \
    "v_cvt_pk_bf16_f32 v93, v42, v43\n\t" \
    "v_mfma_f32_32x32x16_bf16 v[0:15], v[80:83], v[88:91], v[0:15]\n\t" \
    "ds_read_b128 v[76:79], v97 offset:3072\n\t" \
    "v_exp_f32 v44, v44\n\t" \
    "v_exp_f32 v45, v45\n\t" \
    "v_exp_f32 v46, v46\n\t" \
    "v_exp_f32 v47, v47\n\t" \
    "v_cvt_pk_bf16_f32 v94, v44, v45\n\t" \
    "v_cvt_pk_bf16_f32 v95, v46, v47\n\t" \
    "v_exp_f32 v16, v16\n\t" \
    "v_exp_f32 v17, v17\n\t" \
    "v_mfma_f32_32x32x16_bf16 v[0:15], v[84:87], v[92:95], v[0:15]\n\t" \
    "ds_read_b128 v[56:59], v97 offset:8192\n\t" \
    "v_exp_f32 v18, v18\n\t" \
    "v_exp_f32 v19, v19\n\t" \
    "v_cvt_pk_bf16_f32 v88, v16, v17\n\t" \
    "v_cvt_pk_bf16_f32 v89, v18, v19\n\t" \
    "s_waitcnt lgkmcnt(2)\n\t" \
    "v_mfma_f32_32x32x16_bf16 v[32:47], v[64:67], v[48:51], 0\n\t" \
    "ds_read_b128 v[60:63], v97 offset:9216\n\t" \
    "v_exp_f32 v20, v20\n\t" \
    "v_exp_f32 v21, v21\n\t" \
    "v_exp_f32 v22, v22\n\t" \
    "v_exp_f32 v23, v23\n\t" \
    "v_cvt_pk_bf16_f32 v90, v20, v21\n\t" \
    "v_cvt_pk_bf16_f32 v91, v22, v23\n\t" \
    "v_mfma_f32_32x32x16_bf16 v[32:47], v[68:71], v[52:55], v[32:47]\n\t" \
    "ds_read_b128 v[80:83], v97 offset:6144\n\t" \
    "v_exp_f32 v24, v24\n\t" \
    "v_exp_f32 v25, v25\n\t" \
    "v_exp_f32 v26, v26\n\t" \
    "v_exp_f32 v27, v27\n\t" \
    "v_cvt_pk_bf16_f32 v92, v24, v25\n\t" \
    "v_cvt_pk_bf16_f32 v93, v26, v27\n\t" \
    "v_mfma_f32_32x32x16_bf16 v[0:15], v[72:75], v[88:91], v[0:15]\n\t" \
    "ds_read_b128 v[84:87], v97 offset:7168\n\t" \
    "v_exp_f32 v28, v28\n\t" \
    "v_exp_f32 v29, v29\n\t" \
    "v_exp_f32 v30, v30\n\t" \
    "v_exp_f32 v31, v31\n\t" \
    "v_cvt_pk_bf16_f32 v94, v28, v29\n\t" \
    "v_cvt_pk_bf16_f32 v95, v30, v31\n\t" \
    "v_exp_f32 v32, v32\n\t" \
    "v_exp_f32 v33, v33\n\t" \
    "v_mfma_f32_32x32x16_bf16 v[0:15], v[76:79], v[92:95], v[0:15]\n\t" \
    "ds_read_b128 v[64:67], v97 offset:12288\n\t" \
    "v_exp_f32 v34, v34\n\t" \
    "v_exp_f32 v35, v35\n\t" \
    "v_cvt_pk_bf16_f32 v88, v32, v33\n\t" \
    "v_cvt_pk_bf16_f32 v89, v34, v35\n\t" \
    "s_waitcnt lgkmcnt(2)\n\t" \
    "v_mfma_f32_32x32x16_bf16 v[16:31], v[56:59], v[48:51], 0\n\t" \
    "ds_read_b128 v[68:71], v97 offset:13312\n\t" \
    "v_exp_f32 v36, v36\n\t" \
    "v_exp_f32 v37, v37\n\t" \
    "v_exp_f32 v38, v38\n\t" \
    "v_exp_f32 v39, v39\n\t" \
    "v_cvt_pk_bf16_f32 v90, v36, v37\n\t" \
    "v_cvt_pk_bf16_f32 v91, v38, v39\n\t" \
    "v_mfma_f32_32x32x16_bf16 v[16:31], v[60:63], v[52:55], v[16:31]\n\t" \
    "ds_read_b128 v[72:75], v97 offset:10240\n\t" \
    "v_exp_f32 v40, v40\n\t" \
    "v_exp_f32 v41, v41\n\t" \
    "v_exp_f32 v42, v42\n\t" \
    "v_exp_f32 v43, v43\n\t" \
    "v_cvt_pk_bf16_f32 v92, v40, v41\n\t" \
    "v_cvt_pk_bf16_f32 v93, v42, v43\n\t" \
    "v_mfma_f32_32x32x16_bf16 v[0:15], v[80:83], v[88:91], v[0:15]\n\t" \
    "ds_read_b128 v[76:79], v97 offset:11264\n\t" \
    "v_exp_f32 v44, v44\n\t" \
    "v_exp_f32 v45, v45\n\t" \
    "v_exp_f32 v46, v46\n\t" \
    "v_exp_f32 v47, v47\n\t" \
    "v_cvt_pk_bf16_f32 v94, v44, v45\n\t" \
    "v_cvt_pk_bf16_f32 v95, v46, v47\n\t" \
    "s_waitcnt vmcnt(4)\n\t" \
    "s_barrier\n\t" \
    "s_add_u32 m0, s42, 32768\n\t" \
    "s_add_u32 s40, s40, 16384\n\t" \
    "s_addc_u32 s41, s41, 0\n\t" \
    "global_load_lds_dwordx4 v98, s[40:41] offset:0\n\t" \
    "global_load_lds_dwordx4 v98, s[40:41] offset:1024\n\t" \
    "global_load_lds_dwordx4 v98, s[40:41] offset:2048\n\t" \
    "global_load_lds_dwordx4 v98, s[40:41] offset:3072\n\t" \
    "v_exp_f32 v16, v16\n\t" \
    "v_exp_f32 v17, v17\n\t" \
    "v_mfma_f32_32x32x16_bf16 v[0:15], v[84:87], v[92:95], v[0:15]\n\t" \
    "ds_read_b128 v[56:59], v97 offset:32768\n\t" \
    "v_exp_f32 v18, v18\n\t" \
    "v_exp_f32 v19, v19\n\t" \
    "v_cvt_pk_bf16_f32 v88, v16, v17\n\t" \
    "v_cvt_pk_bf16_f32 v89, v18, v19\n\t" \
    "s_waitcnt lgkmcnt(2)\n\t" \
    "v_mfma_f32_32x32x16_bf16 v[32:47], v[64:67], v[48:51], 0\n\t" \
    "ds_read_b128 v[60:63], v97 offset:33792\n\t" \
    "v_exp_f32 v20, v20\n\t" \
    "v_exp_f32 v21, v21\n\t" \
    "v_exp_f32 v22, v22\n\t" \
    "v_exp_f32 v23, v23\n\t" \
    "v_cvt_pk_bf16_f32 v90, v20, v21\n\t" \
    "v_cvt_pk_bf16_f32 v91, v22, v23\n\t" \
    "v_mfma_f32_32x32x16_bf16 v[32:47], v[68:71], v[52:55], v[32:47]\n\t" \
    "ds_read_b128 v[80:83], v97 offset:14336\n\t" \
    "v_exp_f32 v24, v24\n\t" \
    "v_exp_f32 v25, v25\n\t" \
    "v_exp_f32 v26, v26\n\t" \
    "v_exp_f32 v27, v27\n\t" \
    "v_cvt_pk_bf16_f32 v92, v24, v25\n\t" \
    "v_cvt_pk_bf16_f32 v93, v26, v27\n\t" \
    "v_mfma_f32_32x32x16_bf16 v[0:15], v[72:75], v[88:91], v[0:15]\n\t" \
    "ds_read_b128 v[84:87], v97 offset:15360\n\t" \
    "v_exp_f32 v28, v28\n\t" \
    "v_exp_f32 v29, v29\n\t" \
    "v_exp_f32 v30, v30\n\t" \
    "v_exp_f32 v31, v31\n\t" \
    "v_cvt_pk_bf16_f32 v94, v28, v29\n\t" \
    "v_cvt_pk_bf16_f32 v95, v30, v31\n\t" \
    "v_exp_f32 v32, v32\n\t" \
    "v_exp_f32 v33, v33\n\t" \
    "v_mfma_f32_32x32x16_bf16 v[0:15], v[76:79], v[92:95], v[0:15]\n\t" \
    "ds_read_b128 v[64:67], v97 offset:36864\n\t" \
    "v_exp_f32 v34, v34\n\t" \
    "v_exp_f32 v35, v35\n\t" \
    "v_cvt_pk_bf16_f32 v88, v32, v33\n\t" \
    "v_cvt_pk_bf16_f32 v89, v34, v35\n\t" \
    "s_waitcnt lgkmcnt(2)\n\t" \
    "v_mfma_f32_32x32x16_bf16 v[16:31], v[56:59], v[48:51], 0\n\t" \
    "ds_read_b128 v[68:71], v97 offset:37888\n\t" \
    "v_exp_f32 v36, v36\n\t" \
    "v_exp_f32 v37, v37\n\t" \
    "v_exp_f32 v38, v38\n\t" \
    "v_exp_f32 v39, v39\n\t" \
    "v_cvt_pk_bf16_f32 v90, v36, v37\n\t" \
    "v_cvt_pk_bf16_f32 v91, v38, v39\n\t" \
    "v_mfma_f32_32x32x16_bf16 v[16:31], v[60:63], v[52:55], v[16:31]\n\t" \
    "ds_read_b128 v[72:75], v97 offset:34816\n\t" \
    "v_exp_f32 v40, v40\n\t" \
    "v_exp_f32 v41, v41\n\t" \
    "v_exp_f32 v42, v42\n\t" \
    "v_exp_f32 v43, v43\n\t" \
    "v_cvt_pk_bf16_f32 v92, v40, v41\n\t" \
    "v_cvt_pk_bf16_f32 v93, v42, v43\n\t" \
    "v_mfma_f32_32x32x16_bf16 v[0:15], v[80:83], v[88:91], v[0:15]\n\t" \
    "ds_read_b128 v[76:79], v97 offset:35840\n\t" \
    "v_exp_f32 v44, v44\n\t" \
    "v_exp_f32 v45, v45\n\t" \
    "v_exp_f32 v46, v46\n\t" \
    "v_exp_f32 v47, v47\n\t" \
    "v_cvt_pk_bf16_f32 v94, v44, v45\n\t" \
    "v_cvt_pk_bf16_f32 v95, v46, v47\n\t" \
    "v_exp_f32 v16, v16\n\t" \
    "v_exp_f32 v17, v17\n\t" \
    "v_mfma_f32_32x32x16_bf16 v[0:15], v[84:87], v[92:95], v[0:15]\n\t" \
    "ds_read_b128 v[56:59], v97 offset:40960\n\t" \
    "v_exp_f32 v18, v18\n\t" \
    "v_exp_f32 v19, v19\n\t" \
    "v_cvt_pk_bf16_f32 v88, v16, v17\n\t" \
    "v_cvt_pk_bf16_f32 v89, v18, v19\n\t" \
    "s_waitcnt lgkmcnt(2)\n\t" \
    "v_mfma_f32_32x32x16_bf16 v[32:47], v[64:67], v[48:51], 0\n\t" \
    "ds_read_b128 v[60:63], v97 offset:41984\n\t" \
    "v_exp_f32 v20, v20\n\t" \
    "v_exp_f32 v21, v21\n\t" \
    "v_exp_f32 v22, v22\n\t" \
    "v_exp_f32 v23, v23\n\t" \
    "v_cvt_pk_bf16_f32 v90, v20, v21\n\t" \
    "v_cvt_pk_bf16_f32 v91, v22, v23\n\t" \
    "v_mfma_f32_32x32x16_bf16 v[32:47], v[68:71], v[52:55], v[32:47]\n\t" \
    "ds_read_b128 v[80:83], v97 offset:38912\n\t" \
    "v_exp_f32 v24, v24\n\t" \
    "v_exp_f32 v25, v25\n\t" \
    "v_exp_f32 v26, v26\n\t" \
    "v_exp_f32 v27, v27\n\t" \
    "v_cvt_pk_bf16_f32 v92, v24, v25\n\t" \
    "v_cvt_pk_bf16_f32 v93, v26, v27\n\t" \
    "v_mfma_f32_32x32x16_bf16 v[0:15], v[72:75], v[88:91], v[0:15]\n\t" \
    "ds_read_b128 v[84:87], v97 offset:39936\n\t" \
    "v_exp_f32 v28, v28\n\t" \
    "v_exp_f32 v29, v29\n\t" \
    "v_exp_f32 v30, v30\n\t" \
    "v_exp_f32 v31, v31\n\t" \
    "v_cvt_pk_bf16_f32 v94, v28, v29\n\t" \
    "v_cvt_pk_bf16_f32 v95, v30, v31\n\t" \
    "v_exp_f32 v32, v32\n\t" \
    "v_exp_f32 v33, v33\n\t" \
    "v_mfma_f32_32x32x16_bf16 v[0:15], v[76:79], v[92:95], v[0:15]\n\t" \
    "ds_read_b128 v[64:67], v97 offset:45056\n\t" \
    "v_exp_f32 v34, v34\n\t" \
    "v_exp_f32 v35, v35\n\t" \
    "v_cvt_pk_bf16_f32 v88, v32, v33\n\t" \
    "v_cvt_pk_bf16_f32 v89, v34, v35\n\t" \
    "s_waitcnt lgkmcnt(2)\n\t" \
    "v_mfma_f32_32x32x16_bf16 v[16:31], v[56:59], v[48:51], 0\n\t" \
    "ds_read_b128 v[68:71], v97 offset:46080\n\t" \
    "v_exp_f32 v36, v36\n\t" \
    "v_exp_f32 v37, v37\n\t" \
    "v_exp_f32 v38, v38\n\t" \
    "v_exp_f32 v39, v39\n\t" \
    "v_cvt_pk_bf16_f32 v90, v36, v37\n\t" \
    "v_cvt_pk_bf16_f32 v91, v38, v39\n\t" \
    "v_mfma_f32_32x32x16_bf16 v[16:31], v[60:63], v[52:55], v[16:31]\n\t" \
    "ds_read_b128 v[72:75], v97 offset:43008\n\t" \
    "v_exp_f32 v40, v40\n\t" \
    "v_exp_f32 v41, v41\n\t" \
    "v_exp_f32 v42, v42\n\t" \
    "v_exp_f32 v43, v43\n\t" \
    "v_cvt_pk_bf16_f32 v92, v40, v41\n\t" \
    "v_cvt_pk_bf16_f32 v93, v42, v43\n\t" \
    "v_mfma_f32_32x32x16_bf16 v[0:15], v[80:83], v[88:91], v[0:15]\n\t" \
    "ds_read_b128 v[76:79], v97 offset:44032\n\t" \
    "v_exp_f32 v44, v44\n\t" \
    "v_exp_f32 v45, v45\n\t" \
    "v_exp_f32 v46, v46\n\t" \
    "v_exp_f32 v47, v47\n\t" \
    "v_cvt_pk_bf16_f32 v94, v44, v45\n\t" \
    "v_cvt_pk_bf16_f32 v95, v46, v47\n\t" \
    "s_waitcnt vmcnt(4)\n\t" \
    "s_barrier\n\t" \
    "s_add_u32 m0, s42, 65536\n\t" \
    "s_add_u32 s40, s40, 16384\n\t" \
    "s_addc_u32 s41, s41, 0\n\t" \
    "global_load_lds_dwordx4 v98, s[40:41] offset:0\n\t" \
    "global_load_lds_dwordx4 v98, s[40:41] offset:1024\n\t" \
    "global_load_lds_dwordx4 v98, s[40:41] offset:2048\n\t" \
    "global_load_lds_dwordx4 v98, s[40:41] offset:3072\n\t" \
    "v_exp_f32 v16, v16\n\t" \
    "v_exp_f32 v17, v17\n\t" \
    "v_mfma_f32_32x32x16_bf16 v[0:15], v[84:87], v[92:95], v[0:15]\n\t" \
    "ds_read_b128 v[56:59], v96 offset:0\n\t" \
    "v_exp_f32 v18, v18\n\t" \
    "v_exp_f32 v19, v19\n\t" \
    "v_cvt_pk_bf16_f32 v88, v16, v17\n\t" \
    "v_cvt_pk_bf16_f32 v89, v18, v19\n\t" \
    "s_waitcnt lgkmcnt(2)\n\t" \
    "v_mfma_f32_32x32x16_bf16 v[32:47], v[64:67], v[48:51], 0\n\t" \
    "ds_read_b128 v[60:63], v96 offset:1024\n\t" \
    "v_exp_f32 v20, v20\n\t" \
    "v_exp_f32 v21, v21\n\t" \
    "v_exp_f32 v22, v22\n\t" \
    "v_exp_f32 v23, v23\n\t" \
    "v_cvt_pk_bf16_f32 v90, v20, v21\n\t" \
    "v_cvt_pk_bf16_f32 v91, v22, v23\n\t" \
    "v_mfma_f32_32x32x16_bf16 v[32:47], v[68:71], v[52:55], v[32:47]\n\t" \
    "ds_read_b128 v[80:83], v97 offset:47104\n\t" \
    "v_exp_f32 v24, v24\n\t" \
    "v_exp_f32 v25, v25\n\t" \
    "v_exp_f32 v26, v26\n\t" \
    "v_exp_f32 v27, v27\n\t" \
    "v_cvt_pk_bf16_f32 v92, v24, v25\n\t" \
    "v_cvt_pk_bf16_f32 v93, v26, v27\n\t" \
    "v_mfma_f32_32x32x16_bf16 v[0:15], v[72:75], v[88:91], v[0:15]\n\t" \
    "ds_read_b128 v[84:87], v97 offset:48128\n\t" \
    "v_exp_f32 v28, v28\n\t" \
    "v_exp_f32 v29, v29\n\t" \
    "v_exp_f32 v30, v30\n\t" \
    "v_exp_f32 v31, v31\n\t" \
    "v_cvt_pk_bf16_f32 v94, v28, v29\n\t" \
    "v_cvt_pk_bf16_f32 v95, v30, v31\n\t" \
    "v_exp_f32 v32, v32\n\t" \
    "v_exp_f32 v33, v33\n\t" \
    "v_mfma_f32_32x32x16_bf16 v[0:15], v[76:79], v[92:95], v[0:15]\n\t" \
    "ds_read_b128 v[64:67], v96 offset:4096\n\t" \
    "v_exp_f32 v34, v34\n\t" \
    "v_exp_f32 v35, v35\n\t" \
    "v_cvt_pk_bf16_f32 v88, v32, v33\n\t" \
    "v_cvt_pk_bf16_f32 v89, v34, v35\n\t" \
    "s_waitcnt lgkmcnt(2)\n\t" \
    "v_mfma_f32_32x32x16_bf16 v[16:31], v[56:59], v[48:51], 0\n\t" \
    "ds_read_b128 v[68:71], v96 offset:5120\n\t" \
    "v_exp_f32 v36, v36\n\t" \
    "v_exp_f32 v37, v37\n\t" \
    "v_exp_f32 v38, v38\n\t" \
    "v_exp_f32 v39, v39\n\t" \
    "v_cvt_pk_bf16_f32 v90, v36, v37\n\t" \
    "v_cvt_pk_bf16_f32 v91, v38, v39\n\t" \
    "v_mfma_f32_32x32x16_bf16 v[16:31], v[60:63], v[52:55], v[16:31]\n\t" \
    "ds_read_b128 v[72:75], v96 offset:2048\n\t" \
    "v_exp_f32 v40, v40\n\t" \
    "v_exp_f32 v41, v41\n\t" \
    "v_exp_f32 v42, v42\n\t" \
    "v_exp_f32 v43, v43\n\t" \
    "v_cvt_pk_bf16_f32 v92, v40, v41\n\t" \
    "v_cvt_pk_bf16_f32 v93, v42, v43\n\t" \
    "v_mfma_f32_32x32x16_bf16 v[0:15], v[80:83], v[88:91], v[0:15]\n\t" \
    "ds_read_b128 v[76:79], v96 offset:3072\n\t" \
    "v_exp_f32 v44, v44\n\t" \
    "v_exp_f32 v45, v45\n\t" \
    "v_exp_f32 v46, v46\n\t" \
    "v_exp_f32 v47, v47\n\t" \
    "v_cvt_pk_bf16_f32 v94, v44, v45\n\t" \
    "v_cvt_pk_bf16_f32 v95, v46, v47\n\t" \
    "v_exp_f32 v16, v16\n\t" \
    "v_exp_f32 v17, v17\n\t" \
    "v_mfma_f32_32x32x16_bf16 v[0:15], v[84:87], v[92:95], v[0:15]\n\t" \
    "ds_read_b128 v[56:59], v96 offset:8192\n\t" \
    "v_exp_f32 v18, v18\n\t" \
    "v_exp_f32 v19, v19\n\t" \
    "v_cvt_pk_bf16_f32 v88, v16, v17\n\t" \
    "v_cvt_pk_bf16_f32 v89, v18, v19\n\t" \
    "s_waitcnt lgkmcnt(2)\n\t" \
    "v_mfma_f32_32x32x16_bf16 v[32:47], v[64:67], v[48:51], 0\n\t" \
    "ds_read_b128 v[60:63], v96 offset:9216\n\t" \
    "v_exp_f32 v20, v20\n\t" \
    "v_exp_f32 v21, v21\n\t" \
    "v_exp_f32 v22, v22\n\t" \
    "v_exp_f32 v23, v23\n\t" \
    "v_cvt_pk_bf16_f32 v90, v20, v21\n\t" \
    "v_cvt_pk_bf16_f32 v91, v22, v23\n\t" \
    "v_mfma_f32_32x32x16_bf16 v[32:47], v[68:71], v[52:55], v[32:47]\n\t" \
    "ds_read_b128 v[80:83], v96 offset:6144\n\t" \
    "v_exp_f32 v24, v24\n\t" \
    "v_exp_f32 v25, v25\n\t" \
    "v_exp_f32 v26, v26\n\t" \
    "v_exp_f32 v27, v27\n\t" \
    "v_cvt_pk_bf16_f32 v92, v24, v25\n\t" \
    "v_cvt_pk_bf16_f32 v93, v26, v27\n\t" \
    "v_mfma_f32_32x32x16_bf16 v[0:15], v[72:75], v[88:91], v[0:15]\n\t" \
    "ds_read_b128 v[84:87], v96 offset:7168\n\t" \
    "v_exp_f32 v28, v28\n\t" \
    "v_exp_f32 v29, v29\n\t" \
    "v_exp_f32 v30, v30\n\t" \
    "v_exp_f32 v31, v31\n\t" \
    "v_cvt_pk_bf16_f32 v94, v28, v29\n\t" \
    "v_cvt_pk_bf16_f32 v95, v30, v31\n\t" \
    "v_exp_f32 v32, v32\n\t" \
    "v_exp_f32 v33, v33\n\t" \
    "v_mfma_f32_32x32x16_bf16 v[0:15], v[76:79], v[92:95], v[0:15]\n\t" \
    "ds_read_b128 v[64:67], v96 offset:12288\n\t" \
    "v_exp_f32 v34, v34\n\t" \
    "v_exp_f32 v35, v35\n\t" \
    "v_cvt_pk_bf16_f32 v88, v32, v33\n\t" \
    "v_cvt_pk_bf16_f32 v89, v34, v35\n\t" \
    "s_waitcnt lgkmcnt(2)\n\t" \
    "v_mfma_f32_32x32x16_bf16 v[16:31], v[56:59], v[48:51], 0\n\t" \
    "ds_read_b128 v[68:71], v96 offset:13312\n\t" \
    "v_exp_f32 v36, v36\n\t" \
    "v_exp_f32 v37, v37\n\t" \
    "v_exp_f32 v38, v38\n\t" \
    "v_exp_f32 v39, v39\n\t" \
    "v_cvt_pk_bf16_f32 v90, v36, v37\n\t" \
    "v_cvt_pk_bf16_f32 v91, v38, v39\n\t" \
    "v_mfma_f32_32x32x16_bf16 v[16:31], v[60:63], v[52:55], v[16:31]\n\t" \
    "ds_read_b128 v[72:75], v96 offset:10240\n\t" \
    "v_exp_f32 v40, v40\n\t" \
    "v_exp_f32 v41, v41\n\t" \
    "v_exp_f32 v42, v42\n\t" \
    "v_exp_f32 v43, v43\n\t" \
    "v_cvt_pk_bf16_f32 v92, v40, v41\n\t" \
    "v_cvt_pk_bf16_f32 v93, v42, v43\n\t" \
    "v_mfma_f32_32x32x16_bf16 v[0:15], v[80:83], v[88:91], v[0:15]\n\t" \
    "ds_read_b128 v[76:79], v96 offset:11264\n\t" \
    "v_exp_f32 v44, v44\n\t" \
    "v_exp_f32 v45, v45\n\t" \
    "v_exp_f32 v46, v46\n\t" \
    "v_exp_f32 v47, v47\n\t" \
    "v_cvt_pk_bf16_f32 v94, v44, v45\n\t" \
    "v_cvt_pk_bf16_f32 v95, v46, v47\n\t" \
    "s_waitcnt vmcnt(4)\n\t" \
    "s_barrier\n\t" \
    "s_add_u32 m0, s42, 98304\n\t" \
    "s_add_u32 s40, s40, 16384\n\t" \
    "s_addc_u32 s41, s41, 0\n\t" \
    "global_load_lds_dwordx4 v98, s[40:41] offset:0\n\t" \
    "global_load_lds_dwordx4 v98, s[40:41] offset:1024\n\t" \
    "global_load_lds_dwordx4 v98, s[40:41] offset:2048\n\t" \
    "global_load_lds_dwordx4 v98, s[40:41] offset:3072\n\t" \
    "v_exp_f32 v16, v16\n\t" \
    "v_exp_f32 v17, v17\n\t" \
    "v_mfma_f32_32x32x16_bf16 v[0:15], v[84:87], v[92:95], v[0:15]\n\t" \
    "ds_read_b128 v[56:59], v96 offset:32768\n\t" \
    "v_exp_f32 v18, v18\n\t" \
    "v_exp_f32 v19, v19\n\t" \
    "v_cvt_pk_bf16_f32 v88, v16, v17\n\t" \
    "v_cvt_pk_bf16_f32 v89, v18, v19\n\t" \
    "s_waitcnt lgkmcnt(2)\n\t" \
    "v_mfma_f32_32x32x16_bf16 v[32:47], v[64:67], v[48:51], 0\n\t" \
    "ds_read_b128 v[60:63], v96 offset:33792\n\t" \
    "v_exp_f32 v20, v20\n\t" \
    "v_exp_f32 v21, v21\n\t" \
    "v_exp_f32 v22, v22\n\t" \
    "v_exp_f32 v23, v23\n\t" \
    "v_cvt_pk_bf16_f32 v90, v20, v21\n\t" \
    "v_cvt_pk_bf16_f32 v91, v22, v23\n\t" \
    "v_mfma_f32_32x32x16_bf16 v[32:47], v[68:71], v[52:55], v[32:47]\n\t" \
    "ds_read_b128 v[80:83], v96 offset:14336\n\t" \
    "v_exp_f32 v24, v24\n\t" \
    "v_exp_f32 v25, v25\n\t" \
    "v_exp_f32 v26, v26\n\t" \
    "v_exp_f32 v27, v27\n\t" \
    "v_cvt_pk_bf16_f32 v92, v24, v25\n\t" \
    "v_cvt_pk_bf16_f32 v93, v26, v27\n\t" \
    "v_mfma_f32_32x32x16_bf16 v[0:15], v[72:75], v[88:91], v[0:15]\n\t" \
    "ds_read_b128 v[84:87], v96 offset:15360\n\t" \
    "v_exp_f32 v28, v28\n\t" \
    "v_exp_f32 v29, v29\n\t" \
    "v_exp_f32 v30, v30\n\t" \
    "v_exp_f32 v31, v31\n\t" \
    "v_cvt_pk_bf16_f32 v94, v28, v29\n\t" \
    "v_cvt_pk_bf16_f32 v95, v30, v31\n\t" \
    "v_exp_f32 v32, v32\n\t" \
    "v_exp_f32 v33, v33\n\t" \
    "v_mfma_f32_32x32x16_bf16 v[0:15], v[76:79], v[92:95], v[0:15]\n\t" \
    "ds_read_b128 v[64:67], v96 offset:36864\n\t" \
    "v_exp_f32 v34, v34\n\t" \
    "v_exp_f32 v35, v35\n\t" \
    "v_cvt_pk_bf16_f32 v88, v32, v33\n\t" \
    "v_cvt_pk_bf16_f32 v89, v34, v35\n\t" \
    "s_waitcnt lgkmcnt(2)\n\t" \
    "v_mfma_f32_32x32x16_bf16 v[16:31], v[56:59], v[48:51], 0\n\t" \
    "ds_read_b128 v[68:71], v96 offset:37888\n\t" \
    "v_exp_f32 v36, v36\n\t" \
    "v_exp_f32 v37, v37\n\t" \
    "v_exp_f32 v38, v38\n\t" \
    "v_exp_f32 v39, v39\n\t" \
    "v_cvt_pk_bf16_f32 v90, v36, v37\n\t" \
    "v_cvt_pk_bf16_f32 v91, v38, v39\n\t" \
    "v_mfma_f32_32x32x16_bf16 v[16:31], v[60:63], v[52:55], v[16:31]\n\t" \
    "ds_read_b128 v[72:75], v96 offset:34816\n\t" \
    "v_exp_f32 v40, v40\n\t" \
    "v_exp_f32 v41, v41\n\t" \
    "v_exp_f32 v42, v42\n\t" \
    "v_exp_f32 v43, v43\n\t" \
    "v_cvt_pk_bf16_f32 v92, v40, v41\n\t" \
    "v_cvt_pk_bf16_f32 v93, v42, v43\n\t" \
    "v_mfma_f32_32x32x16_bf16 v[0:15], v[80:83], v[88:91], v[0:15]\n\t" \
    "ds_read_b128 v[76:79], v96 offset:35840\n\t" \
    "v_exp_f32 v44, v44\n\t" \
    "v_exp_f32 v45, v45\n\t" \
    "v_exp_f32 v46, v46\n\t" \
    "v_exp_f32 v47, v47\n\t" \
    "v_cvt_pk_bf16_f32 v94, v44, v45\n\t" \
    "v_cvt_pk_bf16_f32 v95, v46, v47\n\t" \
    "v_exp_f32 v16, v16\n\t" \
    "v_exp_f32 v17, v17\n\t" \
    "v_mfma_f32_32x32x16_bf16 v[0:15], v[84:87], v[92:95], v[0:15]\n\t" \
    "ds_read_b128 v[56:59], v96 offset:40960\n\t" \
    "v_exp_f32 v18, v18\n\t" \
    "v_exp_f32 v19, v19\n\t" \
    "v_cvt_pk_bf16_f32 v88, v16, v17\n\t" \
    "v_cvt_pk_bf16_f32 v89, v18, v19\n\t" \
    "s_waitcnt lgkmcnt(2)\n\t" \
    "v_mfma_f32_32x32x16_bf16 v[32:47], v[64:67], v[48:51], 0\n\t" \
    "ds_read_b128 v[60:63], v96 offset:41984\n\t" \
    "v_exp_f32 v20, v20\n\t" \
    "v_exp_f32 v21, v21\n\t" \
    "v_exp_f32 v22, v22\n\t" \
    "v_exp_f32 v23, v23\n\t" \
    "v_cvt_pk_bf16_f32 v90, v20, v21\n\t" \
    "v_cvt_pk_bf16_f32 v91, v22, v23\n\t" \
    "v_mfma_f32_32x32x16_bf16 v[32:47], v[68:71], v[52:55], v[32:47]\n\t" \
    "ds_read_b128 v[80:83], v96 offset:38912\n\t" \
    "v_exp_f32 v24, v24\n\t" \
    "v_exp_f32 v25, v25\n\t" \
    "v_exp_f32 v26, v26\n\t" \
    "v_exp_f32 v27, v27\n\t" \
    "v_cvt_pk_bf16_f32 v92, v24, v25\n\t" \
    "v_cvt_pk_bf16_f32 v93, v26, v27\n\t" \
    "v_mfma_f32_32x32x16_bf16 v[0:15], v[72:75], v[88:91], v[0:15]\n\t" \
    "ds_read_b128 v[84:87], v96 offset:39936\n\t" \
    "v_exp_f32 v28, v28\n\t" \
    "v_exp_f32 v29, v29\n\t" \
    "v_exp_f32 v30, v30\n\t" \
    "v_exp_f32 v31, v31\n\t" \
    "v_cvt_pk_bf16_f32 v94, v28, v29\n\t" \
    "v_cvt_pk_bf16_f32 v95, v30, v31\n\t" \
    "v_exp_f32 v32, v32\n\t" \
    "v_exp_f32 v33, v33\n\t" \
    "v_mfma_f32_32x32x16_bf16 v[0:15], v[76:79], v[92:95], v[0:15]\n\t" \
    "ds_read_b128 v[64:67], v96 offset:45056\n\t" \
    "v_exp_f32 v34, v34\n\t" \
    "v_exp_f32 v35, v35\n\t" \
    "v_cvt_pk_bf16_f32 v88, v32, v33\n\t" \
    "v_cvt_pk_bf16_f32 v89, v34, v35\n\t" \
    "s_waitcnt lgkmcnt(2)\n\t" \
    "v_mfma_f32_32x32x16_bf16 v[16:31], v[56:59], v[48:51], 0\n\t" \
    "ds_read_b128 v[68:71], v96 offset:46080\n\t" \
    "v_exp_f32 v36, v36\n\t" \
    "v_exp_f32 v37, v37\n\t" \
    "v_exp_f32 v38, v38\n\t" \
    "v_exp_f32 v39, v39\n\t" \
    "v_cvt_pk_bf16_f32 v90, v36, v37\n\t" \
    "v_cvt_pk_bf16_f32 v91, v38, v39\n\t" \
    "v_mfma_f32_32x32x16_bf16 v[16:31], v[60:63], v[52:55], v[16:31]\n\t" \
    "ds_read_b128 v[72:75], v96 offset:43008\n\t" \
    "v_exp_f32 v40, v40\n\t" \
    "v_exp_f32 v41, v41\n\t" \
    "v_exp_f32 v42, v42\n\t" \
    "v_exp_f32 v43, v43\n\t" \
    "v_cvt_pk_bf16_f32 v92, v40, v41\n\t" \
    "v_cvt_pk_bf16_f32 v93, v42, v43\n\t" \
    "v_mfma_f32_32x32x16_bf16 v[0:15], v[80:83], v[88:91], v[0:15]\n\t" \
    "ds_read_b128 v[76:79], v96 offset:44032\n\t" \
    "v_exp_f32 v44, v44\n\t" \
    "v_exp_f32 v45, v45\n\t" \
    "v_exp_f32 v46, v46\n\t" \
    "v_exp_f32 v47, v47\n\t" \
    "v_cvt_pk_bf16_f32 v94, v44, v45\n\t" \
    "v_cvt_pk_bf16_f32 v95, v46, v47\n\t" \
    "s_waitcnt vmcnt(4)\n\t" \
    "s_barrier\n\t" \
    "v_exp_f32 v16, v16\n\t" \
    "v_exp_f32 v17, v17\n\t" \
    "v_mfma_f32_32x32x16_bf16 v[0:15], v[84:87], v[92:95], v[0:15]\n\t" \
    "ds_read_b128 v[56:59], v97 offset:0\n\t" \
    "v_exp_f32 v18, v18\n\t" \
    "v_exp_f32 v19, v19\n\t" \
    "v_cvt_pk_bf16_f32 v88, v16, v17\n\t" \
    "v_cvt_pk_bf16_f32 v89, v18, v19\n\t" \
    "s_waitcnt lgkmcnt(2)\n\t" \
    "v_mfma_f32_32x32x16_bf16 v[32:47], v[64:67], v[48:51], 0\n\t" \
    "ds_read_b128 v[60:63], v97 offset:1024\n\t" \
    "v_exp_f32 v20, v20\n\t" \
    "v_exp_f32 v21, v21\n\t" \
    "v_exp_f32 v22, v22\n\t" \
    "v_exp_f32 v23, v23\n\t" \
    "v_cvt_pk_bf16_f32 v90, v20, v21\n\t" \
    "v_cvt_pk_bf16_f32 v91, v22, v23\n\t" \
    "v_mfma_f32_32x32x16_bf16 v[32:47], v[68:71], v[52:55], v[32:47]\n\t" \
    "ds_read_b128 v[80:83], v96 offset:47104\n\t" \
    "v_exp_f32 v24, v24\n\t" \
    "v_exp_f32 v25, v25\n\t" \
    "v_exp_f32 v26, v26\n\t" \
    "v_exp_f32 v27, v27\n\t" \
    "v_cvt_pk_bf16_f32 v92, v24, v25\n\t" \
    "v_cvt_pk_bf16_f32 v93, v26, v27\n\t" \
    "v_mfma_f32_32x32x16_bf16 v[0:15], v[72:75], v[88:91], v[0:15]\n\t" \
    "ds_read_b128 v[84:87], v96 offset:48128\n\t" \
    "v_exp_f32 v28, v28\n\t" \
    "v_exp_f32 v29, v29\n\t" \
    "v_exp_f32 v30, v30\n\t" \
    "v_exp_f32 v31, v31\n\t" \
    "v_cvt_pk_bf16_f32 v94, v28, v29\n\t" \
    "v_cvt_pk_bf16_f32 v95, v30, v31\n\t" \
    "v_exp_f32 v32, v32\n\t" \
    "v_exp_f32 v33, v33\n\t" \
    "v_mfma_f32_32x32x16_bf16 v[0:15], v[76:79], v[92:95], v[0:15]\n\t" \
    "ds_read_b128 v[64:67], v97 offset:4096\n\t" \
    "v_exp_f32 v34, v34\n\t" \
    "v_exp_f32 v35, v35\n\t" \
    "v_cvt_pk_bf16_f32 v88, v32, v33\n\t" \
    "v_cvt_pk_bf16_f32 v89, v34, v35\n\t" \
    "s_waitcnt lgkmcnt(2)\n\t" \
    "v_mfma_f32_32x32x16_bf16 v[16:31], v[56:59], v[48:51], 0\n\t" \
    "ds_read_b128 v[68:71], v97 offset:5120\n\t" \
    "v_exp_f32 v36, v36\n\t" \
    "v_exp_f32 v37, v37\n\t" \
    "v_exp_f32 v38, v38\n\t" \
    "v_exp_f32 v39, v39\n\t" \
    "v_cvt_pk_bf16_f32 v90, v36, v37\n\t" \
    "v_cvt_pk_bf16_f32 v91, v38, v39\n\t" \
    "v_mfma_f32_32x32x16_bf16 v[16:31], v[60:63], v[52:55], v[16:31]\n\t" \
    "ds_read_b128 v[72:75], v97 offset:2048\n\t" \
    "v_exp_f32 v40, v40\n\t" \
    "v_exp_f32 v41, v41\n\t" \
    "v_exp_f32 v42, v42\n\t" \
    "v_exp_f32 v43, v43\n\t" \
    "v_cvt_pk_bf16_f32 v92, v40, v41\n\t" \
    "v_cvt_pk_bf16_f32 v93, v42, v43\n\t" \
    "v_mfma_f32_32x32x16_bf16 v[0:15], v[80:83], v[88:91], v[0:15]\n\t" \
    "ds_read_b128 v[76:79], v97 offset:3072\n\t" \
    "v_exp_f32 v44, v44\n\t" \
    "v_exp_f32 v45, v45\n\t" \
    "v_exp_f32 v46, v46\n\t" \
    "v_exp_f32 v47, v47\n\t" \
    "v_cvt_pk_bf16_f32 v94, v44, v45\n\t" \
    "v_cvt_pk_bf16_f32 v95, v46, v47\n\t" \
    "v_exp_f32 v16, v16\n\t" \
    "v_exp_f32 v17, v17\n\t" \
    "v_mfma_f32_32x32x16_bf16 v[0:15], v[84:87], v[92:95], v[0:15]\n\t" \
    "ds_read_b128 v[56:59], v97 offset:8192\n\t" \
    "v_exp_f32 v18, v18\n\t" \
    "v_exp_f32 v19, v19\n\t" \
    "v_cvt_pk_bf16_f32 v88, v16, v17\n\t" \
    "v_cvt_pk_bf16_f32 v89, v18, v19\n\t" \
    "s_waitcnt lgkmcnt(2)\n\t" \
    "v_mfma_f32_32x32x16_bf16 v[32:47], v[64:67], v[48:51], 0\n\t" \
    "ds_read_b128 v[60:63], v97 offset:9216\n\t" \
    "v_exp_f32 v20, v20\n\t" \
    "v_exp_f32 v21, v21\n\t" \
    "v_exp_f32 v22, v22\n\t" \
    "v_exp_f32 v23, v23\n\t" \
    "v_cvt_pk_bf16_f32 v90, v20, v21\n\t" \
    "v_cvt_pk_bf16_f32 v91, v22, v23\n\t" \
    "v_mfma_f32_32x32x16_bf16 v[32:47], v[68:71], v[52:55], v[32:47]\n\t" \
    "ds_read_b128 v[80:83], v97 offset:6144\n\t" \
    "v_exp_f32 v24, v24\n\t" \
    "v_exp_f32 v25, v25\n\t" \
    "v_exp_f32 v26, v26\n\t" \
    "v_exp_f32 v27, v27\n\t" \
    "v_cvt_pk_bf16_f32 v92, v24, v25\n\t" \
    "v_cvt_pk_bf16_f32 v93, v26, v27\n\t" \
    "v_mfma_f32_32x32x16_bf16 v[0:15], v[72:75], v[88:91], v[0:15]\n\t" \
    "ds_read_b128 v[84:87], v97 offset:7168\n\t" \
    "v_exp_f32 v28, v28\n\t" \
    "v_exp_f32 v29, v29\n\t" \
    "v_exp_f32 v30, v30\n\t" \
    "v_exp_f32 v31, v31\n\t" \
    "v_cvt_pk_bf16_f32 v94, v28, v29\n\t" \
    "v_cvt_pk_bf16_f32 v95, v30, v31\n\t" \
    "v_exp_f32 v32, v32\n\t" \
    "v_exp_f32 v33, v33\n\t" \
    "v_mfma_f32_32x32x16_bf16 v[0:15], v[76:79], v[92:95], v[0:15]\n\t" \
    "ds_read_b128 v[64:67], v97 offset:12288\n\t" \
    "v_exp_f32 v34, v34\n\t" \
    "v_exp_f32 v35, v35\n\t" \
    "v_cvt_pk_bf16_f32 v88, v32, v33\n\t" \
    "v_cvt_pk_bf16_f32 v89, v34, v35\n\t" \
    "s_waitcnt lgkmcnt(2)\n\t" \
    "v_mfma_f32_32x32x16_bf16 v[16:31], v[56:59], v[48:51], 0\n\t" \
    "ds_read_b128 v[68:71], v97 offset:13312\n\t" \
    "v_exp_f32 v36, v36\n\t" \
    "v_exp_f32 v37, v37\n\t" \
    "v_exp_f32 v38, v38\n\t" \
    "v_exp_f32 v39, v39\n\t" \
    "v_cvt_pk_bf16_f32 v90, v36, v37\n\t" \
    "v_cvt_pk_bf16_f32 v91, v38, v39\n\t" \
    "v_mfma_f32_32x32x16_bf16 v[16:31], v[60:63], v[52:55], v[16:31]\n\t" \
    "ds_read_b128 v[72:75], v97 offset:10240\n\t" \
    "v_exp_f32 v40, v40\n\t" \
    "v_exp_f32 v41, v41\n\t" \
    "v_exp_f32 v42, v42\n\t" \
    "v_exp_f32 v43, v43\n\t" \
    "v_cvt_pk_bf16_f32 v92, v40, v41\n\t" \
    "v_cvt_pk_bf16_f32 v93, v42, v43\n\t" \
    "v_mfma_f32_32x32x16_bf16 v[0:15], v[80:83], v[88:91], v[0:15]\n\t" \
    "ds_read_b128 v[76:79], v97 offset:11264\n\t" \
    "v_exp_f32 v44, v44\n\t" \
    "v_exp_f32 v45, v45\n\t" \
    "v_exp_f32 v46, v46\n\t" \
    "v_exp_f32 v47, v47\n\t" \
    "v_cvt_pk_bf16_f32 v94, v44, v45\n\t" \
    "v_cvt_pk_bf16_f32 v95, v46, v47\n\t" \
    "s_waitcnt vmcnt(0)\n\t" \
    "s_barrier\n\t" \
    "v_exp_f32 v16, v16\n\t" \
    "v_exp_f32 v17, v17\n\t" \
    "v_mfma_f32_32x32x16_bf16 v[0:15], v[84:87], v[92:95], v[0:15]\n\t" \
    "ds_read_b128 v[56:59], v97 offset:32768\n\t" \
    "v_exp_f32 v18, v18\n\t" \
    "v_exp_f32 v19, v19\n\t" \
    "v_cvt_pk_bf16_f32 v88, v16, v17\n\t" \
    "v_cvt_pk_bf16_f32 v89, v18, v19\n\t" \
    "s_waitcnt lgkmcnt(2)\n\t" \
    "v_mfma_f32_32x32x16_bf16 v[32:47], v[64:67], v[48:51], 0\n\t" \
    "ds_read_b128 v[60:63], v97 offset:33792\n\t" \
    "v_exp_f32 v20, v20\n\t" \
    "v_exp_f32 v21, v21\n\t" \
    "v_exp_f32 v22, v22\n\t" \
    "v_exp_f32 v23, v23\n\t" \
    "v_cvt_pk_bf16_f32 v90, v20, v21\n\t" \
    "v_cvt_pk_bf16_f32 v91, v22, v23\n\t" \
    "v_mfma_f32_32x32x16_bf16 v[32:47], v[68:71], v[52:55], v[32:47]\n\t" \
    "ds_read_b128 v[80:83], v97 offset:14336\n\t" \
    "v_exp_f32 v24, v24\n\t" \
    "v_exp_f32 v25, v25\n\t" \
    "v_exp_f32 v26, v26\n\t" \
    "v_exp_f32 v27, v27\n\t" \
    "v_cvt_pk_bf16_f32 v92, v24, v25\n\t" \
    "v_cvt_pk_bf16_f32 v93, v26, v27\n\t" \
    "v_mfma_f32_32x32x16_bf16 v[0:15], v[72:75], v[88:91], v[0:15]\n\t" \
    "ds_read_b128 v[84:87], v97 offset:15360\n\t" \
    "v_exp_f32 v28, v28\n\t" \
    "v_exp_f32 v29, v29\n\t" \
    "v_exp_f32 v30, v30\n\t" \
    "v_exp_f32 v31, v31\n\t" \
    "v_cvt_pk_bf16_f32 v94, v28, v29\n\t" \
    "v_cvt_pk_bf16_f32 v95, v30, v31\n\t" \
    "v_exp_f32 v32, v32\n\t" \
    "v_exp_f32 v33, v33\n\t" \
    "v_mfma_f32_32x32x16_bf16 v[0:15], v[76:79], v[92:95], v[0:15]\n\t" \
    "ds_read_b128 v[64:67], v97 offset:36864\n\t" \
    "v_exp_f32 v34, v34\n\t" \
    "v_exp_f32 v35, v35\n\t" \
    "v_cvt_pk_bf16_f32 v88, v32, v33\n\t" \
    "v_cvt_pk_bf16_f32 v89, v34, v35\n\t" \
    "s_waitcnt lgkmcnt(2)\n\t" \
    "v_mfma_f32_32x32x16_bf16 v[16:31], v[56:59], v[48:51], 0\n\t" \
    "ds_read_b128 v[68:71], v97 offset:37888\n\t" \
    "v_exp_f32 v36, v36\n\t" \
    "v_exp_f32 v37, v37\n\t" \
    "v_exp_f32 v38, v38\n\t" \
    "v_exp_f32 v39, v39\n\t" \
    "v_cvt_pk_bf16_f32 v90, v36, v37\n\t" \
    "v_cvt_pk_bf16_f32 v91, v38, v39\n\t" \
    "v_mfma_f32_32x32x16_bf16 v[16:31], v[60:63], v[52:55], v[16:31]\n\t" \
    "ds_read_b128 v[72:75], v97 offset:34816\n\t" \
    "v_exp_f32 v40, v40\n\t" \
    "v_exp_f32 v41, v41\n\t" \
    "v_exp_f32 v42, v42\n\t" \
    "v_exp_f32 v43, v43\n\t" \
    "v_cvt_pk_bf16_f32 v92, v40, v41\n\t" \
    "v_cvt_pk_bf16_f32 v93, v42, v43\n\t" \
    "v_mfma_f32_32x32x16_bf16 v[0:15], v[80:83], v[88:91], v[0:15]\n\t" \
    "ds_read_b128 v[76:79], v97 offset:35840\n\t" \
    "v_exp_f32 v44, v44\n\t" \
    "v_exp_f32 v45, v45\n\t" \
    "v_exp_f32 v46, v46\n\t" \
    "v_exp_f32 v47, v47\n\t" \
    "v_cvt_pk_bf16_f32 v94, v44, v45\n\t" \
    "v_cvt_pk_bf16_f32 v95, v46, v47\n\t" \
    "v_exp_f32 v16, v16\n\t" \
    "v_exp_f32 v17, v17\n\t" \
    "v_mfma_f32_32x32x16_bf16 v[0:15], v[84:87], v[92:95], v[0:15]\n\t" \
    "ds_read_b128 v[56:59], v97 offset:40960\n\t" \
    "v_exp_f32 v18, v18\n\t" \
    "v_exp_f32 v19, v19\n\t" \
    "v_cvt_pk_bf16_f32 v88, v16, v17\n\t" \
    "v_cvt_pk_bf16_f32 v89, v18, v19\n\t" \
    "s_waitcnt lgkmcnt(2)\n\t" \
    "v_mfma_f32_32x32x16_bf16 v[32:47], v[64:67], v[48:51], 0\n\t" \
    "ds_read_b128 v[60:63], v97 offset:41984\n\t" \
    "v_exp_f32 v20, v20\n\t" \
    "v_exp_f32 v21, v21\n\t" \
    "v_exp_f32 v22, v22\n\t" \
    "v_exp_f32 v23, v23\n\t" \
    "v_cvt_pk_bf16_f32 v90, v20, v21\n\t" \
    "v_cvt_pk_bf16_f32 v91, v22, v23\n\t" \
    "v_mfma_f32_32x32x16_bf16 v[32:47], v[68:71], v[52:55], v[32:47]\n\t" \
    "ds_read_b128 v[80:83], v97 offset:38912\n\t" \
    "v_exp_f32 v24, v24\n\t" \
    "v_exp_f32 v25, v25\n\t" \
    "v_exp_f32 v26, v26\n\t" \
    "v_exp_f32 v27, v27\n\t" \
    "v_cvt_pk_bf16_f32 v92, v24, v25\n\t" \
    "v_cvt_pk_bf16_f32 v93, v26, v27\n\t" \
    "v_mfma_f32_32x32x16_bf16 v[0:15], v[72:75], v[88:91], v[0:15]\n\t" \
    "ds_read_b128 v[84:87], v97 offset:39936\n\t" \
    "v_exp_f32 v28, v28\n\t" \
    "v_exp_f32 v29, v29\n\t" \
    "v_exp_f32 v30, v30\n\t" \
    "v_exp_f32 v31, v31\n\t" \
    "v_cvt_pk_bf16_f32 v94, v28, v29\n\t" \
    "v_cvt_pk_bf16_f32 v95, v30, v31\n\t" \
    "v_exp_f32 v32, v32\n\t" \
    "v_exp_f32 v33, v33\n\t" \
    "v_mfma_f32_32x32x16_bf16 v[0:15], v[76:79], v[92:95], v[0:15]\n\t" \
    "ds_read_b128 v[64:67], v97 offset:45056\n\t" \
    "v_exp_f32 v34, v34\n\t" \
    "v_exp_f32 v35, v35\n\t" \
    "v_cvt_pk_bf16_f32 v88, v32, v33\n\t" \
    "v_cvt_pk_bf16_f32 v89, v34, v35\n\t" \
    "s_waitcnt lgkmcnt(2)\n\t" \
    "v_mfma_f32_32x32x16_bf16 v[16:31], v[56:59], v[48:51], 0\n\t" \
    "ds_read_b128 v[68:71], v97 offset:46080\n\t" \
    "v_exp_f32 v36, v36\n\t" \
    "v_exp_f32 v37, v37\n\t" \
    "v_exp_f32 v38, v38\n\t" \
    "v_exp_f32 v39, v39\n\t" \
    "v_cvt_pk_bf16_f32 v90, v36, v37\n\t" \
    "v_cvt_pk_bf16_f32 v91, v38, v39\n\t" \
    "v_mfma_f32_32x32x16_bf16 v[16:31], v[60:63], v[52:55], v[16:31]\n\t" \
    "ds_read_b128 v[72:75], v97 offset:43008\n\t" \
    "v_exp_f32 v40, v40\n\t" \
    "v_exp_f32 v41, v41\n\t" \
    "v_exp_f32 v42, v42\n\t" \
    "v_exp_f32 v43, v43\n\t" \
    "v_cvt_pk_bf16_f32 v92, v40, v41\n\t" \
    "v_cvt_pk_bf16_f32 v93, v42, v43\n\t" \
    "v_mfma_f32_32x32x16_bf16 v[0:15], v[80:83], v[88:91], v[0:15]\n\t" \
    "ds_read_b128 v[76:79], v97 offset:44032\n\t" \
    "v_exp_f32 v44, v44\n\t" \
    "v_exp_f32 v45, v45\n\t" \
    "v_exp_f32 v46, v46\n\t" \
    "v_exp_f32 v47, v47\n\t" \
    "v_cvt_pk_bf16_f32 v94, v44, v45\n\t" \
    "v_cvt_pk_bf16_f32 v95, v46, v47\n\t" \
    "v_exp_f32 v16, v16\n\t" \
    "v_exp_f32 v17, v17\n\t" \
    "v_mfma_f32_32x32x16_bf16 v[0:15], v[84:87], v[92:95], v[0:15]\n\t" \
    "v_exp_f32 v18, v18\n\t" \
    "v_exp_f32 v19, v19\n\t" \
    "v_cvt_pk_bf16_f32 v88, v16, v17\n\t" \
    "v_cvt_pk_bf16_f32 v89, v18, v19\n\t" \
    "s_waitcnt lgkmcnt(1)\n\t" \
    "v_mfma_f32_32x32x16_bf16 v[32:47], v[64:67], v[48:51], 0\n\t" \
    "v_exp_f32 v20, v20\n\t" \
    "v_exp_f32 v21, v21\n\t" \
    "v_exp_f32 v22, v22\n\t" \
    "v_exp_f32 v23, v23\n\t" \
    "v_cvt_pk_bf16_f32 v90, v20, v21\n\t" \
    "v_cvt_pk_bf16_f32 v91, v22, v23\n\t" \
    "v_mfma_f32_32x32x16_bf16 v[32:47], v[68:71], v[52:55], v[32:47]\n\t" \
    "ds_read_b128 v[80:83], v97 offset:47104\n\t" \
    "v_exp_f32 v24, v24\n\t" \
    "v_exp_f32 v25, v25\n\t" \
    "v_exp_f32 v26, v26\n\t" \
    "v_exp_f32 v27, v27\n\t" \
    "v_cvt_pk_bf16_f32 v92, v24, v25\n\t" \
    "v_cvt_pk_bf16_f32 v93, v26, v27\n\t" \
    "v_mfma_f32_32x32x16_bf16 v[0:15], v[72:75], v[88:91], v[0:15]\n\t" \
    "ds_read_b128 v[84:87], v97 offset:48128\n\t" \
    "v_exp_f32 v28, v28\n\t" \
    "v_exp_f32 v29, v29\n\t" \
    "v_exp_f32 v30, v30\n\t" \
    "v_exp_f32 v31, v31\n\t" \
    "v_cvt_pk_bf16_f32 v94, v28, v29\n\t" \
    "v_cvt_pk_bf16_f32 v95, v30, v31\n\t" \
    "v_exp_f32 v32, v32\n\t" \
    "v_exp_f32 v33, v33\n\t" \
    "v_mfma_f32_32x32x16_bf16 v[0:15], v[76:79], v[92:95], v[0:15]\n\t" \
    "v_exp_f32 v34, v34\n\t" \
    "v_exp_f32 v35, v35\n\t" \
    "v_cvt_pk_bf16_f32 v88, v32, v33\n\t" \
    "v_cvt_pk_bf16_f32 v89, v34, v35\n\t" \
    "s_waitcnt lgkmcnt(1)\n\t" \
    "v_exp_f32 v36, v36\n\t" \
    "v_exp_f32 v37, v37\n\t" \
    "v_exp_f32 v38, v38\n\t" \
    "v_exp_f32 v39, v39\n\t" \
    "v_cvt_pk_bf16_f32 v90, v36, v37\n\t" \
    "v_cvt_pk_bf16_f32 v91, v38, v39\n\t" \
    "v_exp_f32 v40, v40\n\t" \
    "v_exp_f32 v41, v41\n\t" \
    "v_exp_f32 v42, v42\n\t" \
    "v_exp_f32 v43, v43\n\t" \
    "v_cvt_pk_bf16_f32 v92, v40, v41\n\t" \
    "v_cvt_pk_bf16_f32 v93, v42, v43\n\t" \
    "v_mfma_f32_32x32x16_bf16 v[0:15], v[80:83], v[88:91], v[0:15]\n\t" \
    "v_exp_f32 v44, v44\n\t" \
    "v_exp_f32 v45, v45\n\t" \
    "v_exp_f32 v46, v46\n\t" \
    "v_exp_f32 v47, v47\n\t" \
    "v_cvt_pk_bf16_f32 v94, v44, v45\n\t" \
    "v_cvt_pk_bf16_f32 v95, v46, v47\n\t" \
    "s_waitcnt lgkmcnt(0)\n\t" \
    "s_nop 0\n\t" \
    "v_mfma_f32_32x32x16_bf16 v[0:15], v[84:87], v[92:95], v[0:15]\n\t" \
    "s_mov_b32 m0, s43\n\t" \
    "s_nop 7\n\t" \
    "s_nop 7\n\t"
#define ATTN_FAST_CLOBBERS "v16", "v17", "v18", "v19", "v20", "v21", "v22", "v23", "v24", "v25", "v26", "v27", "v28", "v29", "v30", "v31", "v32", "v33", "v34", "v35", "v36", "v37", "v38", "v39", "v40", "v41", "v42", "v43", "v44", "v45", "v46", "v47", "v56", "v57", "v58", "v59", "v60", "v61", "v62", "v63", "v64", "v65", "v66", "v67", "v68", "v69", "v70", "v71", "v72", "v73", "v74", "v75", "v76", "v77", "v78", "v79", "v80", "v81", "v82", "v83", "v84", "v85", "v86", "v87", "v88", "v89", "v90", "v91", "v92", "v93", "v94", "v95", "s43", "scc", "memory"

constexpr int A_SLOT = 28672;
constexpr int A_XS = 3 * A_SLOT;
constexpr int A_XROW = 464;
constexpr int L_Q = A_XS + 128 * A_XROW;
constexpr int L_CTL = L_Q + 8192;
constexpr int L_TOTAL = L_CTL + 256;
constexpr int B_RED = 131072;
constexpr int C_RROW = 272;

DI float gelu_tanh(float h) {
    const float c0 = -2.0f * 0.7978845608028654f * 1.4426950408889634f, c1 = c0 * 0.044715f;
    const float z = h * __builtin_fmaf(h * h, c1, c0);
    const float e = __builtin_amdgcn_exp2f(z);
    return h * __builtin_amdgcn_rcpf(1.0f + e);
}

DI void phaseA(const char* __restrict__ x, const char* __restrict__ wqkv, const char* __restrict__ w1, const char* __restrict__ b1,
               char* __restrict__ ws, char* lds_generic, int Rb, int c, u64& tag) {
    LAS char* lds = (LAS char*)lds_generic;
    const unsigned lds0 = (unsigned)(uintptr_t)lds_generic;
    const int tid = threadIdx.x, l = tid & 63, w = rfl(tid >> 6) & 7, r = l & 31, h = l >> 5;
    const int tt = w & 3, half = w >> 2;
    STAMP(ws, 0);
    u32x2 e0; { const unsigned z = 0; asm volatile("global_load_dwordx2 %0, %1, %2 sc1" : "=v"(e0) : "v"(z), "s"(uptr(ws + WS_EPOCH)) : "memory"); }
    u32x4 xr[2][7];
    {
        const char* xb = x + (size_t)(Rb * 128 + 16 * w) * 896;
        const unsigned vo = (l >> 3) * 896 + (l & 7) * 16;
#pragma unroll
        for (int g = 0; g < 2; ++g)
#pragma unroll
            for (int i = 0; i < 7; ++i) xr[g][i] = gload16(xb + g * 8 * 896 + i * 128, vo);
    }
    {
        const unsigned vo = (l >> 2) * 2688 + ((l & 3) < 3 ? (l & 3) * 16 : 40);
#pragma unroll
        for (int i = 0; i < 11; ++i) {
            const int n = w + 8 * i;
            if (n < 84) { const int m = n / 28, kb = n % 28;
                glds16(wqkv + (size_t)kb * 16 * 2688 + m * 896 + c * 56, vo, lds0 + m * A_SLOT + kb * 1024); }
        }
    }
    asm volatile("s_waitcnt vmcnt(10)" : "+v"(e0), "+v"(xr[0][0]), "+v"(xr[0][1]), "+v"(xr[0][2]), "+v"(xr[0][3]), "+v"(xr[0][4]), "+v"(xr[0][5]), "+v"(xr[0][6]),
                 "+v"(xr[1][0]), "+v"(xr[1][1]), "+v"(xr[1][2]), "+v"(xr[1][3]), "+v"(xr[1][4]), "+v"(xr[1][5]), "+v"(xr[1][6]) :: "memory");
    tag = (((u64)rfl(e0[1]) << 32) | rfl(e0[0])) + 0x9E3779B97F4A7C15ull;
    STAMP(ws, 1);
#pragma unroll
    for (int g = 0; g < 2; ++g) {
        float sm = 0.f, sq = 0.f; unsigned ones_ = 0x3f803f80u; asm volatile("" : "+v"(ones_)); const bf16x2 one = asbf2(ones_);
#pragma unroll
        for (int i = 0; i < 7; ++i)
#pragma unroll
            for (int e = 0; e < 4; ++e) { const bf16x2 v = asbf2(xr[g][i][e]); sm = __builtin_amdgcn_fdot2_f32_bf16(v, one, sm, false); sq = __builtin_amdgcn_fdot2_f32_bf16(v, v, sq, false); }
        sm += dppf<0xB1>(sm); sq += dppf<0xB1>(sq); sm += dppf<0x4E>(sm); sq += dppf<0x4E>(sq); sm += dppf<0x141>(sm); sq += dppf<0x141>(sq);
        const float mean = sm * (1.0f / 448.0f); const float var = __builtin_fmaxf(sq * (1.0f / 448.0f) - mean * mean, 0.f);
        const float rstd = __builtin_amdgcn_rsqf(var + 1e-5f), nb = -mean * rstd;
#pragma unroll
        for (int i = 0; i < 7; ++i)
#pragma unroll
            for (int e = 0; e < 4; ++e) { const unsigned v = xr[g][i][e]; xr[g][i][e] = cvtpk(__builtin_fmaf(bflo(v), rstd, nb), __builtin_fmaf(bfhi(v), rstd, nb)); }
    }
    STAMP(ws, 2);
    bf16x8 xf[28];
    {
        LAS char* xs = lds + A_XS; const int sub = l & 7;
        const int wrow = 16 * w + (l >> 3);
        const LAS char* rd = xs + (32 * tt + r) * A_XROW + h * 16;
#pragma unroll
        for (int g = 0; g < 2; ++g) {
#pragma unroll
            for (int i = 0; i < 3; ++i) *(LAS u32x4*)(xs + (wrow + 8 * g) * A_XROW + (sub + 8 * i) * 16) = xr[g][i];
            if (sub < 4) *(LAS u32x4*)(xs + (wrow + 8 * g) * A_XROW + (sub + 24) * 16) = xr[g][3];
        }
        WAIT_LGKM0(); BARRIER();
        STAMP(ws, 15);
#pragma unroll
        for (int s = 0; s < 14; ++s) xf[s] = ldsfrag(rd + s * 32);
        WAIT_LGKM0(); BARRIER();
        STAMP(ws, 16);
#pragma unroll
        for (int g = 0; g < 2; ++g) {
            if (sub >= 4) *(LAS u32x4*)(xs + (wrow + 8 * g) * A_XROW + (sub - 4) * 16) = xr[g][3];
#pragma unroll
            for (int i = 4; i < 7; ++i) *(LAS u32x4*)(xs + (wrow + 8 * g) * A_XROW + (sub + 8 * i - 28) * 16) = xr[g][i];
        }
        WAIT_LGKM0(); BARRIER();
#pragma unroll
        for (int s = 0; s < 14; ++s) xf[14 + s] = ldsfrag(rd + s * 32);
        WAIT_LGKM0();
        STAMP(ws, 17);
        WAIT_VM(0);
        STAMP(ws, 18);
        BARRIER();
    }
    STAMP(ws, 3);
    const bool ffn = c < 14;
    const unsigned vo1 = (l >> 2) * 3584 + (l & 3) * 16;
    if (ffn) {
#pragma unroll
        for (int i = 0; i < 7; ++i) { const int n = w + 8 * i, t = n / 28, kb = n % 28;
            glds16(w1 + (size_t)kb * 16 * 3584 + (4 * c + t) * 64, vo1, lds0 + A_XS + t * A_SLOT + kb * 1024); }
    }
    const int troff = (8 * h + ((l & 15) >> 2)) * 64 + ((l >> 4) & 1) * 32 + (l & 3) * 8;
    const int tglob = Rb * 4 + tt;
    const unsigned vl = l * 16;
    if (half == 0) {
        f32x16 aq = {}, ak = {};
        const LAS char* pq = lds + troff; const LAS char* pk = lds + A_SLOT + troff;
#pragma unroll
        for (int s = 0; s < 28; ++s) {
            aq = MFMA(trfrag(pq + s * 1024, pq + s * 1024 + 256), xf[s], aq);
            ak = MFMA(trfrag(pk + s * 1024, pk + s * 1024 + 256), xf[s], ak);
        }
        const float qs = 0.18898223650461361f * 1.4426950408889634f;
        float nk = 0.f;
#pragma unroll
        for (int i = 0; i < 16; ++i) { const bool pad = (i >= 12) && (h == 0); aq[i] = pad ? 0.f : aq[i] * qs; const float kv = pad ? 0.f : ak[i]; nk = __builtin_fmaf(kv, kv, nk); ak[i] = kv; }
        if (h == 0) ak[12] = 1.0f;
        nk = row32_max(xhalf_sum(nk));
        char* kd = ws + WS_KV + (size_t)(c * 64 + tglob) * 4096;
        gstore16_sc1(kd, vl, pack8(ak, 0)); gstore16_sc1(kd + 1024, vl, pack8(ak, 1));
        if (l == 0) __hip_atomic_store((__attribute__((address_space(1))) unsigned*)(ws + WS_KN + (c * 64 + tglob) * 4), __float_as_uint(nk), __ATOMIC_RELAXED, __HIP_MEMORY_SCOPE_AGENT);
        *(LAS u32x4*)(lds + L_Q + tt * 2048 + vl) = pack8(aq, 0); *(LAS u32x4*)(lds + L_Q + tt * 2048 + 1024 + vl) = pack8(aq, 1);
    } else {
        f32x16 av = {};
        const LAS char* pv = lds + 2 * A_SLOT + troff;
#pragma unroll
        for (int s = 0; s < 28; ++s) av = MFMA(xf[s], trfrag(pv + s * 1024, pv + s * 1024 + 256), av);
        const bool pad = (r >= 24) && (r < 28); const float pv1 = (r == 24) ? 1.0f : 0.f;
#pragma unroll
        for (int i = 0; i < 16; ++i) av[i] = pad ? pv1 : av[i];
        char* vd = ws + WS_KV + (size_t)(c * 64 + tglob) * 4096 + 2048;
        gstore16_sc1(vd, vl, pack8(av, 0)); gstore16_sc1(vd + 1024, vl, pack8(av, 1));
    }
    STAMP(ws, 4);
    WAIT_LGKM0(); WAIT_VM(0); BARRIER();
    if (tid == 0) flag_store(ws, WS_FKV + c * 256 + Rb * 8, tag);
    STAMP(ws, 5);
    if (!ffn) return;
#pragma unroll
    for (int i = 0; i < 7; ++i) { const int n = w + 8 * i, t = n / 28, kb = n % 28;
        glds16(w1 + (size_t)kb * 16 * 3584 + (4 * c + 2 + t) * 64, vo1, lds0 + t * A_SLOT + kb * 1024); }
#pragma unroll
    for (int u = 0; u < 2; ++u) {
        const int ft = 4 * c + 2 * u + half;
        u32x2 bb[4];
#pragma unroll
        for (int g = 0; g < 4; ++g) bb[g] = *(const u32x2*)(b1 + (ft * 32 + 8 * g + 4 * h) * 2);
        if (u == 1) { WAIT_VM(0); BARRIER(); }
        f32x16 a = {};
        const LAS char* pw = lds + (u == 0 ? A_XS : 0) + half * A_SLOT + troff;
#pragma unroll
        for (int s = 0; s < 28; ++s) a = MFMA(trfrag(pw + s * 1024, pw + s * 1024 + 256), xf[s], a);
#pragma unroll
        for (int g = 0; g < 4; ++g) {
            a[4 * g + 0] = gelu_tanh(a[4 * g + 0] + bflo(bb[g][0])); a[4 * g + 1] = gelu_tanh(a[4 * g + 1] + bfhi(bb[g][0]));
            a[4 * g + 2] = gelu_tanh(a[4 * g + 2] + bflo(bb[g][1])); a[4 * g + 3] = gelu_tanh(a[4 * g + 3] + bfhi(bb[g][1]));
        }
        char* hd = ws + WS_H + ((size_t)tglob * 56 + ft) * 2048;
        gstore16_sc1(hd, vl, pack8(a, 0)); gstore16_sc1(hd + 1024, vl, pack8(a, 1));
        STAMP(ws, 6 + u);
    }
    WAIT_VM(0); BARRIER();
    if (tid == 0) flag_store(ws, WS_FH + Rb * 256 + c * 8, tag);
}

#define EX2(v) __builtin_amdgcn_exp2f(v)
DI void attn_safe(f32x16& o, const bf16x8 q0, const bf16x8 q1, const char* src, unsigned dst, unsigned vo, const LAS char* rb, int l) {
    float m = -1e30f;
    const f32x16 zero = {};
    WAIT_VM(0); BARRIER();
    for (int i = 0; i < 8; ++i) {
        const LAS char* sb = rb + (i & 3) * 32768;
#pragma unroll 1
        for (int t = 0; t < 4; ++t) {
            f32x16 sc = MFMA(ldsfrag(sb + t * 4096), q0, zero); sc = MFMA(ldsfrag(sb + t * 4096 + 1024), q1, sc);
            float mx = sc[0];
#pragma unroll
            for (int e = 1; e < 16; ++e) mx = __builtin_fmaxf(mx, sc[e]);
            mx = xhalf_max(mx);
            const float mn = __builtin_fmaxf(m, mx), al = EX2(m - mn); m = mn;
#pragma unroll
            for (int e = 0; e < 16; ++e) { o[e] *= al; sc[e] = EX2(sc[e] - mn); }
            o = MFMA(ldsfrag(sb + t * 4096 + 2048), asbf8(pack8(sc, 0)), o); o = MFMA(ldsfrag(sb + t * 4096 + 3072), asbf8(pack8(sc, 1)), o);
        }
        WAIT_LGKM0(); WAIT_VM(0); BARRIER();
        if (i + 4 < 8) {
#pragma unroll
            for (int e = 0; e < 4; ++e) glds16(src + (size_t)(i + 4) * 16384 + e * 1024, vo, dst + (i & 3) * 32768 + e * 1024);
        }
    }
    o[13] = (l < 32) ? m : o[13];
}

DI void phaseB(char* __restrict__ ws, char* lds_generic, int hd, int qb, u64 tag) {
    LAS char* lds = (LAS char*)lds_generic;
    const unsigned lds0 = (unsigned)(uintptr_t)lds_generic;
    const int tid = threadIdx.x, l = tid & 63, w = rfl(tid >> 6) & 7, h = l >> 5;
    const int qt = w & 3, kh = w >> 2, tglob = qb * 4 + qt;
    STAMP(ws, 10);
    if (w == 0) {
        flag_wait(ws, WS_FKV + hd * 256, 16, tag, l);
        __builtin_amdgcn_fence(__ATOMIC_ACQUIRE, "agent");
        WAIT_VM(0);
    }
    if (tid == 64) *(LAS unsigned*)(lds + L_CTL) = 0u;
    WAIT_LGKM0(); BARRIER();
    STAMP(ws, 11);
    const unsigned vo = l * 16;
    const char* src = ws + WS_KV + (size_t)hd * 262144 + (size_t)kh * 131072 + qt * 4096;
    const unsigned dst = lds0 + kh * 16384 + qt * 4096;
    float kn = *(const float*)(ws + WS_KN + (hd * 64 + l) * 4);
    u32x4 q0 = *(const LAS u32x4*)(lds + L_Q + qt * 2048 + vo), q1 = *(const LAS u32x4*)(lds + L_Q + qt * 2048 + 1024 + vo);
    kn = xhalf_max(row32_max(kn));
    float qn = 0.f;
#pragma unroll
    for (int e = 0; e < 4; ++e) { qn = __builtin_amdgcn_fdot2_f32_bf16(asbf2(q0[e]), asbf2(q0[e]), qn, false); qn = __builtin_amdgcn_fdot2_f32_bf16(asbf2(q1[e]), asbf2(q1[e]), qn, false); }
    qn = xhalf_sum(qn);
    const float bound = __builtin_sqrtf(qn * kn) * 1.02f + 1e-3f;
    if (__any(!(bound <= 48.0f)) && l == 0) *(LAS unsigned*)(lds + L_CTL) = 1u;
#pragma unroll
    for (int i = 0; i < 4; ++i)
#pragma unroll
        for (int e = 0; e < 4; ++e) glds16(src + (size_t)i * 16384 + e * 1024, vo, dst + i * 32768 + e * 1024);
    WAIT_LGKM0(); BARRIER();
    const bool safe = rfl(*(const LAS unsigned*)(lds + L_CTL)) != 0u;
    f32x16 o;
    STAMP(ws, 12); STAMPC(ws, 28);
    if (!safe) {
        if (h == 0) q1[2] = cvtpk(-bound, 0.f);
        const unsigned ad0 = lds0 + kh * 16384 + vo, ad1 = ad0 + 65536;
        const char* s3 = (const char*)uptr(src + 3 * 16384);
        const unsigned dsts = rfl(dst);
        asm volatile(ATTN_FAST_ASM
                     : "={v[0:15]}"(o), "+{s[40:41]}"(s3)
                     : "{v[48:51]}"(q0), "{v[52:55]}"(q1), "{v96}"(ad0), "{v97}"(ad1), "{v98}"(vo), "{s42}"(dsts)
                     : ATTN_FAST_CLOBBERS);
    } else {
        o = (f32x16){};
        attn_safe(o, asbf8(q0), asbf8(q1), src, dst, vo, lds + kh * 16384 + vo, l);
    }
    STAMP(ws, 13); STAMPC(ws, 29);
    LAS char* red = lds + B_RED + qt * 4096 + vo;
    if (kh == 1) {
#pragma unroll
        for (int g = 0; g < 4; ++g) *(LAS f32x4*)(red + g * 1024) = (f32x4){o[4 * g], o[4 * g + 1], o[4 * g + 2], o[4 * g + 3]};
    }
    WAIT_LGKM0(); BARRIER();
    if (kh == 0) {
        f32x16 p;
#pragma unroll
        for (int g = 0; g < 4; ++g) { const f32x4 t = *(const LAS f32x4*)(red + g * 1024); p[4 * g] = t[0]; p[4 * g + 1] = t[1]; p[4 * g + 2] = t[2]; p[4 * g + 3] = t[3]; }
        if (safe) {
            const float ma = swap32_lo(o[13]), mb = swap32_lo(p[13]);
            const float mn = __builtin_fmaxf(ma, mb), fa = EX2(ma - mn), fb = EX2(mb - mn);
#pragma unroll
            for (int e = 0; e < 16; ++e) o[e] = o[e] * fa + p[e] * fb;
        } else {
#pragma unroll
            for (int e = 0; e < 16; ++e) o[e] += p[e];
        }
        const float inv = 1.0f / swap32_lo(o[12]);
#pragma unroll
        for (int e = 0; e < 16; ++e) o[e] = (e >= 12 && h == 0) ? 0.f : o[e] * inv;
        char* od = ws + WS_O + ((size_t)tglob * 16 + hd) * 2048;
        gstore16_sc1(od, vo, pack8(o, 0)); gstore16_sc1(od + 1024, vo, pack8(o, 1));
    }
    WAIT_VM(0); BARRIER();
    if (tid == 0) flag_store(ws, WS_FO + qb * 256 + hd * 8, tag);
    STAMP(ws, 14);
}

DI void phaseC(const char* __restrict__ x, const char* __restrict__ wo, const char* __restrict__ w2, const char* __restrict__ b2,
               char* __restrict__ ws, char* __restrict__ out, char* lds_generic, int rb, int ng, u64 tag, bool bump) {
    LAS char* lds = (LAS char*)lds_generic;
    const unsigned lds0 = (unsigned)(uintptr_t)lds_generic;
    const int tid = threadIdx.x, l = tid & 63, w = rfl(tid >> 6) & 7, h = l >> 5;
    const int tt = w & 1, kq = w >> 1, tglob = rb * 2 + tt;
    STAMP(ws, 20);
    if (w == 0) flag_wait(ws, WS_FH + (rb >> 1) * 256, 14, tag, l);
    BARRIER();
    STAMP(ws, 21);
    const unsigned vo = (l >> 3) * 896 + (((l & 7) ^ (((l >> 4) & 1) << 2)) * 16);
    const unsigned vl = l * 16;
    const char* w2src = w2 + ng * 128 + (size_t)(448 * kq + 32 * tt) * 896;
    const unsigned wdst = lds0 + kq * 8192 + tt * 4096;
    const char* hsrc = ws + WS_H + ((size_t)tglob * 56 + 14 * kq) * 2048;
    const char* osrc = ws + WS_O + ((size_t)tglob * 16 + 4 * kq) * 2048;
    u32x4 f[9][4];
    auto issue = [&](int st) {
        if (st < 7) {
#pragma unroll
            for (int e = 0; e < 4; ++e) glds16(w2src + (size_t)(st * 64 + e * 8) * 896, vo, wdst + (st & 3) * 32768 + e * 1024);
#pragma unroll
            for (int e = 0; e < 4; ++e) f[st][e] = gload16_sc1(hsrc + st * 4096 + e * 1024, vl);
        } else {
            const int hd = 4 * kq + 2 * (st - 7) + tt; const int r0 = hd < 15 ? 28 * hd : 416;
#pragma unroll
            for (int e = 0; e < 4; ++e) glds16(wo + ng * 128 + (size_t)(r0 + e * 8) * 896, vo, wdst + (st & 3) * 32768 + e * 1024);
#pragma unroll
            for (int e = 0; e < 4; ++e) f[st][e] = gload16_sc1(osrc + (st - 7) * 4096 + e * 1024, vl);
        }
    };
    issue(0); issue(1); issue(2); issue(3);
    const int q = (l & 15) >> 2, p = l & 3, cb = (l >> 4) & 1;
    const int tr0 = (4 * h + q) * 128 + (((2 * cb + (p >> 1)) ^ ((q >> 1) << 2)) * 16) + (p & 1) * 8, tr1 = tr0 ^ 64;
    f32x16 a0 = {}, a1 = {};
#define PINF(st) asm volatile("" : "+v"(f[st][0]), "+v"(f[st][1]), "+v"(f[st][2]), "+v"(f[st][3]))
#pragma unroll
    for (int st = 0; st < 9; ++st) {
        if (st == 0) WAIT_VM(24); else if (st <= 6) WAIT_VM(16); else if (st == 7) WAIT_VM(8); else WAIT_VM(0);
        PINF(st);
        if (st == 4 && w == 0) flag_wait(ws, WS_FO + (rb >> 1) * 256, 16, tag, l);
        WAIT_LGKM0(); BARRIER();
        if (st >= 1 && st + 3 < 9) issue(st + 3);
        const LAS char* wb = lds + (st & 3) * 32768 + kq * 8192;
        if (st < 7) {
#pragma unroll
            for (int u = 0; u < 2; ++u)
#pragma unroll
                for (int s = 0; s < 2; ++s) {
                    const LAS char* wp = wb + u * 4096 + s * 2048;
                    a0 = MFMA(trfrag(wp + tr0, wp + tr0 + 1024), asbf8(f[st][2 * u + s]), a0);
                    a1 = MFMA(trfrag(wp + tr1, wp + tr1 + 1024), asbf8(f[st][2 * u + s]), a1);
                }
        } else {
#pragma unroll
            for (int u = 0; u < 2; ++u) {
                const int hd = 4 * kq + 2 * (st - 7) + u;
                const LAS char* wp = wb + u * 4096 + (hd == 15 ? 512 : 0);
                a0 = MFMA(trfrag(wp + tr0, wp + tr0 + 1024), asbf8(f[st][2 * u]), a0);
                a1 = MFMA(trfrag(wp + tr1, wp + tr1 + 1024), asbf8(f[st][2 * u]), a1);
                a0 = MFMA(trfrag(wp + tr0 + 2048, wp + tr0 + 2048 + 512), asbf8(f[st][2 * u + 1]), a0);
                a1 = MFMA(trfrag(wp + tr1 + 2048, wp + tr1 + 2048 + 512), asbf8(f[st][2 * u + 1]), a1);
            }
        }
    }
    STAMP(ws, 22);
    const int tok = tid >> 3, ch = tid & 7;
    const size_t goff = (size_t)(rb * 64 + tok) * 896 + ng * 128 + ch * 16;
    const u32x4 xv = *(const u32x4*)(x + goff), bv = *(const u32x4*)(b2 + ng * 128 + ch * 16);
    WAIT_LGKM0(); BARRIER();
    {
        LAS char* rw = lds + (kq * 64 + tt * 32 + (l & 31)) * C_RROW + h * 16;
#pragma unroll
        for (int g = 0; g < 4; ++g) {
            *(LAS f32x4*)(rw + g * 32) = (f32x4){a0[4 * g], a0[4 * g + 1], a0[4 * g + 2], a0[4 * g + 3]};
            *(LAS f32x4*)(rw + 128 + g * 32) = (f32x4){a1[4 * g], a1[4 * g + 1], a1[4 * g + 2], a1[4 * g + 3]};
        }
    }
    WAIT_LGKM0(); BARRIER();
    f32x4 s0 = {}, s1 = {};
#pragma unroll
    for (int k = 0; k < 4; ++k) { const LAS char* rr = lds + (k * 64 + tok) * C_RROW + ch * 32; s0 += *(const LAS f32x4*)rr; s1 += *(const LAS f32x4*)(rr + 16); }
    u32x4 ov;
    ov[0] = cvtpk(s0[0] + bflo(xv[0]) + bflo(bv[0]), s0[1] + bfhi(xv[0]) + bfhi(bv[0]));
    ov[1] = cvtpk(s0[2] + bflo(xv[1]) + bflo(bv[1]), s0[3] + bfhi(xv[1]) + bfhi(bv[1]));
    ov[2] = cvtpk(s1[0] + bflo(xv[2]) + bflo(bv[2]), s1[1] + bfhi(xv[2]) + bfhi(bv[2]));
    ov[3] = cvtpk(s1[2] + bflo(xv[3]) + bflo(bv[3]), s1[3] + bfhi(xv[3]) + bfhi(bv[3]));
    *(u32x4*)(out + goff) = ov;
    if (bump && tid == 0) flag_store(ws, WS_EPOCH, tag);
    STAMP(ws, 23);
}

__global__ void __launch_bounds__(512, 2) fused_block(const char* x, const char* wqkv, const char* wo, const char* w1, const char* b1,
                                                      const char* w2, const char* b2, char* out, char* ws) {
    extern __shared__ __attribute__((aligned(16))) char lds[];
    const int b = blockIdx.x, xcd = b & 7, i = b >> 3;
    const int Rb = i >> 1, c = 2 * xcd + (i & 1);
    u64 tag;
    phaseA(x, wqkv, w1, b1, ws, lds, Rb, c, tag);
    phaseB(ws, lds, c, Rb, tag);
    if (i < 28) phaseC(x, wo, w2, b2, ws, out, lds, 4 * xcd + i / 7, i % 7, tag, b == 0);
}

extern "C" void kernel_launch(void* const* d_in, const int* in_sizes, int n_in, void* d_out, int out_size, void* d_ws, size_t ws_size, hipStream_t stream) {
    const char* x = (const char*)d_in[0]; const char* wqkv = (const char*)d_in[1]; const char* wo = (const char*)d_in[2];
    const char* w1 = (const char*)d_in[4]; const char* b1 = (const char*)d_in[5]; const char* w2 = (const char*)d_in[6]; const char* b2 = (const char*)d_in[7];
    (void)hipFuncSetAttribute((const void*)fused_block, hipFuncAttributeMaxDynamicSharedMemorySize, L_TOTAL);
    hipLaunchKernelGGL(fused_block, dim3(256), dim3(512), L_TOTAL, stream, x, wqkv, wo, w1, b1, w2, b2, (char*)d_out, (char*)d_ws);
}
```

```cpp
#include <hip/hip_runtime.h>
#include <hip/hip_bf16.h>
#include <stdint.h>

typedef __bf16 bf16x2 __attribute__((ext_vector_type(2)));
typedef __bf16 bf16x8 __attribute__((ext_vector_type(8)));
typedef float f32x2 __attribute__((ext_vector_type(2)));
typedef float f32x4 __attribute__((ext_vector_type(4)));
typedef float f32x16 __attribute__((ext_vector_type(16)));
typedef short s16x4 __attribute__((ext_vector_type(4)));
typedef unsigned u32x2 __attribute__((ext_vector_type(2)));
typedef unsigned u32x4 __attribute__((ext_vector_type(4)));
typedef __attribute__((address_space(1))) unsigned long long gu64;
#define LAS __attribute__((address_space(3)))
#define DI __device__ __forceinline__
#define MFMA(a, b, c) __builtin_amdgcn_mfma_f32_32x32x16_bf16((a), (b), (c), 0, 0, 0)

constexpr size_t WS_EPOCH = 0;
constexpr size_t WS_FKV = 256;
constexpr size_t WS_FH = WS_FKV + 16 * 256;
constexpr size_t WS_FO = WS_FH + 16 * 256;
constexpr size_t WS_KN = 16384;
constexpr size_t WS_KV = 32768;
constexpr size_t WS_H = WS_KV + 16 * 64 * 4096;
constexpr size_t WS_O = WS_H + (size_t)64 * 56 * 2048;
constexpr size_t WS_END = WS_O + 64 * 16 * 2048;
#define STAMPC(wsp, k) do {} while (0)
#define STAMP(wsp, k) do {} while (0)

DI unsigned cvtpk(float lo, float hi) { f32x2 v = {lo, hi}; bf16x2 b = __builtin_convertvector(v, bf16x2); return __builtin_bit_cast(unsigned, b); }
DI bf16x2 asbf2(unsigned v) { return __builtin_bit_cast(bf16x2, v); }
DI bf16x8 asbf8(u32x4 v) { return __builtin_bit_cast(bf16x8, v); }
DI float bflo(unsigned v) { return __uint_as_float(v << 16); }
DI float bfhi(unsigned v) { return __uint_as_float(v & 0xffff0000u); }
DI unsigned rfl(unsigned x) { return __builtin_amdgcn_readfirstlane(x); }
DI const void* uptr(const void* p) { const uint64_t v = (uint64_t)p; return (const void*)(((uint64_t)rfl((unsigned)(v >> 32)) << 32) | rfl((unsigned)v)); }
DI void glds16(const void* sbase_, unsigned voff, unsigned lds_dst_) {
    unsigned sv; const void* sbase = uptr(sbase_); const unsigned lds_dst = rfl(lds_dst_);
    asm volatile("s_mov_b32 %0, m0\n\ts_mov_b32 m0, %3\n\ts_nop 4\n\tglobal_load_lds_dwordx4 %1, %2\n\ts_mov_b32 m0, %0"
                 : "=&s"(sv) : "v"(voff), "s"(sbase), "s"(lds_dst) : "memory");
}
DI void touch4(const void* sbase_, unsigned voff, unsigned lds_dst_) {
    unsigned sv; const void* sbase = uptr(sbase_); const unsigned lds_dst = rfl(lds_dst_);
    asm volatile("s_mov_b32 %0, m0\n\ts_mov_b32 m0, %3\n\ts_nop 4\n\tglobal_load_lds_dword %1, %2\n\ts_mov_b32 m0, %0"
                 : "=&s"(sv) : "v"(voff), "s"(sbase), "s"(lds_dst) : "memory");
}
DI u32x4 gload16(const void* sbase, unsigned voff) { u32x4 r; asm volatile("global_load_dwordx4 %0, %1, %2" : "=v"(r) : "v"(voff), "s"(uptr(sbase)) : "memory"); return r; }
DI u32x4 gload16_sc1(const void* sbase, unsigned voff) { u32x4 r; asm volatile("s_nop 4\n\tglobal_load_dwordx4 %0, %1, %2 sc1" : "=v"(r) : "v"(voff), "s"(uptr(sbase)) : "memory"); return r; }
DI void gstore16_sc1(void* sbase, unsigned voff, u32x4 v) { asm volatile("s_nop 4\n\tglobal_store_dwordx4 %0, %1, %2 sc1\n\ts_nop 1" :: "v"(voff), "v"(v), "s"(uptr(sbase)) : "memory"); }
DI s16x4 trread(const LAS char* p) { return __builtin_bit_cast(s16x4, __builtin_amdgcn_ds_read_tr16_b64_v4i16((LAS s16x4*)p)); }
DI bf16x8 trfrag(const LAS char* p0, const LAS char* p1) {
    s16x4 a = trread(p0), b = trread(p1);
    return __builtin_bit_cast(bf16x8, __builtin_shufflevector(a, b, 0, 1, 2, 3, 4, 5, 6, 7));
}
DI bf16x8 ldsfrag(const LAS char* p) { return *(const LAS bf16x8*)p; }
template <int CTRL> DI float dppf(float v) { return __int_as_float(__builtin_amdgcn_update_dpp(0, __float_as_int(v), CTRL, 0xf, 0xf, true)); }
DI u32x4 pack8(const f32x16& a, int s) {
    u32x4 p = {cvtpk(a[8 * s], a[8 * s + 1]), cvtpk(a[8 * s + 2], a[8 * s + 3]), cvtpk(a[8 * s + 4], a[8 * s + 5]), cvtpk(a[8 * s + 6], a[8 * s + 7])};
    return p;
}
DI float swap32_lo(float v) { auto sw = __builtin_amdgcn_permlane32_swap(__float_as_uint(v), __float_as_uint(v), false, false); return __uint_as_float(sw[0]); }
DI float xhalf_sum(float v) { auto sw = __builtin_amdgcn_permlane32_swap(__float_as_uint(v), __float_as_uint(v), false, false); return __uint_as_float(sw[0]) + __uint_as_float(sw[1]); }
DI float xhalf_max(float v) { auto sw = __builtin_amdgcn_permlane32_swap(__float_as_uint(v), __float_as_uint(v), false, false); return __builtin_fmaxf(__uint_as_float(sw[0]), __uint_as_float(sw[1])); }
DI float row32_max(float v) {
    v = __builtin_fmaxf(v, dppf<0xB1>(v)); v = __builtin_fmaxf(v, dppf<0x4E>(v)); v = __builtin_fmaxf(v, dppf<0x141>(v)); v = __builtin_fmaxf(v, dppf<0x140>(v));
    auto sw = __builtin_amdgcn_permlane16_swap(__float_as_uint(v), __float_as_uint(v), false, false); return __builtin_fmaxf(__uint_as_float(sw[0]), __uint_as_float(sw[1]));
}
#define BARRIER() asm volatile("s_barrier" ::: "memory")
#define WAIT_VM(n) asm volatile("s_waitcnt vmcnt(" #n ")" ::: "memory")
#define WAIT_LGKM0() asm volatile("s_waitcnt lgkmcnt(0)" ::: "memory")

typedef unsigned long long u64;
DI void flag_store(char* ws, size_t off, u64 tag) { __hip_atomic_store((gu64*)(ws + off), tag, __ATOMIC_RELAXED, __HIP_MEMORY_SCOPE_AGENT); }
DI void flag_wait(const char* ws, size_t off, int n, u64 tag, int l) {
    gu64* p = (gu64*)(ws + off) + (l < n ? l : 0);
    for (unsigned spins = 0; spins < (1u << 20); ++spins) {
        const u64 v = __hip_atomic_load(p, __ATOMIC_RELAXED, __HIP_MEMORY_SCOPE_AGENT);
        if (__all(v == tag)) break;
        __builtin_amdgcn_s_sleep(2);
    }
}
#define ATTN_FAST_ASM \
    "s_mov_b32 s43, m0\n\t" \
    "s_waitcnt vmcnt(12)\n\t" \
    "s_barrier\n\t" \
    "ds_read_b128 v[56:59], v96 offset:0\n\t" \
    "ds_read_b128 v[60:63], v96 offset:1024\n\t" \
    "ds_read_b128 v[64:67], v96 offset:4096\n\t" \
    "ds_read_b128 v[68:71], v96 offset:5120\n\t" \
    "ds_read_b128 v[72:75], v96 offset:2048\n\t" \
    "ds_read_b128 v[76:79], v96 offset:3072\n\t" \
    "s_waitcnt lgkmcnt(4)\n\t" \
    "v_mfma_f32_32x32x16_bf16 v[16:31], v[56:59], v[48:51], 0\n\t" \
    "v_mfma_f32_32x32x16_bf16 v[16:31], v[60:63], v[52:55], v[16:31]\n\t" \
    "s_nop 7\n\t" \
    "s_nop 7\n\t" \
    "v_exp_f32 v16, v16\n\t" \
    "v_exp_f32 v17, v17\n\t" \
    "ds_read_b128 v[56:59], v96 offset:8192\n\t" \
    "v_exp_f32 v18, v18\n\t" \
    "v_exp_f32 v19, v19\n\t" \
    "v_cvt_pk_bf16_f32 v88, v16, v17\n\t" \
    "v_cvt_pk_bf16_f32 v89, v18, v19\n\t" \
    "s_waitcnt lgkmcnt(2)\n\t" \
    "v_mfma_f32_32x32x16_bf16 v[32:47], v[64:67], v[48:51], 0\n\t" \
    "ds_read_b128 v[60:63], v96 offset:9216\n\t" \
    "v_exp_f32 v20, v20\n\t" \
    "v_exp_f32 v21, v21\n\t" \
    "v_exp_f32 v22, v22\n\t" \
    "v_exp_f32 v23, v23\n\t" \
    "v_cvt_pk_bf16_f32 v90, v20, v21\n\t" \
    "v_cvt_pk_bf16_f32 v91, v22, v23\n\t" \
    "v_mfma_f32_32x32x16_bf16 v[32:47], v[68:71], v[52:55], v[32:47]\n\t" \
    "ds_read_b128 v[80:83], v96 offset:6144\n\t" \
    "v_exp_f32 v24, v24\n\t" \
    "v_exp_f32 v25, v25\n\t" \
    "v_exp_f32 v26, v26\n\t" \
    "v_exp_f32 v27, v27\n\t" \
    "v_cvt_pk_bf16_f32 v92, v24, v25\n\t" \
    "v_cvt_pk_bf16_f32 v93, v26, v27\n\t" \
    "v_mfma_f32_32x32x16_bf16 v[0:15], v[72:75], v[88:91], 0\n\t" \
    "ds_read_b128 v[84:87], v96 offset:7168\n\t" \
    "v_exp_f32 v28, v28\n\t" \
    "v_exp_f32 v29, v29\n\t" \
    "v_exp_f32 v30, v30\n\t" \
    "v_exp_f32 v31, v31\n\t" \
    "v_cvt_pk_bf16_f32 v94, v28, v29\n\t" \
    "v_cvt_pk_bf16_f32 v95, v30, v31\n\t" \
    "v_exp_f32 v32, v32\n\t" \
    "v_exp_f32 v33, v33\n\t" \
    "v_mfma_f32_32x32x16_bf16 v[0:15], v[76:79], v[92:95], v[0:15]\n\t" \
    "ds_read_b128 v[64:67], v96 offset:12288\n\t" \
    "v_exp_f32 v34, v34\n\t" \
    "v_exp_f32 v35, v35\n\t" \
    "v_cvt_pk_bf16_f32 v88, v32, v33\n\t" \
    "v_cvt_pk_bf16_f32 v89, v34, v35\n\t" \
    "s_waitcnt lgkmcnt(2)\n\t" \
    "v_mfma_f32_32x32x16_bf16 v[16:31], v[56:59], v[48:51], 0\n\t" \
    "ds_read_b128 v[68:71], v96 offset:13312\n\t" \
    "v_exp_f32 v36, v36\n\t" \
    "v_exp_f32 v37, v37\n\t" \
    "v_exp_f32 v38, v38\n\t" \
    "v_exp_f32 v39, v39\n\t" \
    "v_cvt_pk_bf16_f32 v90, v36, v37\n\t" \
    "v_cvt_pk_bf16_f32 v91, v38, v39\n\t" \
    "v_mfma_f32_32x32x16_bf16 v[16:31], v[60:63], v[52:55], v[16:31]\n\t" \
    "ds_read_b128 v[72:75], v96 offset:10240\n\t" \
    "v_exp_f32 v40, v40\n\t" \
    "v_exp_f32 v41, v41\n\t" \
    "v_exp_f32 v42, v42\n\t" \
    "v_exp_f32 v43, v43\n\t" \
    "v_cvt_pk_bf16_f32 v92, v40, v41\n\t" \
    "v_cvt_pk_bf16_f32 v93, v42, v43\n\t" \
    "v_mfma_f32_32x32x16_bf16 v[0:15], v[80:83], v[88:91], v[0:15]\n\t" \
    "ds_read_b128 v[76:79], v96 offset:11264\n\t" \
    "v_exp_f32 v44, v44\n\t" \
    "v_exp_f32 v45, v45\n\t" \
    "v_exp_f32 v46, v46\n\t" \
    "v_exp_f32 v47, v47\n\t" \
    "v_cvt_pk_bf16_f32 v94, v44, v45\n\t" \
    "v_cvt_pk_bf16_f32 v95, v46, v47\n\t" \
    "s_waitcnt vmcnt(8)\n\t" \
    "s_barrier\n\t" \
    "v_exp_f32 v16, v16\n\t" \
    "v_exp_f32 v17, v17\n\t" \
    "v_mfma_f32_32x32x16_bf16 v[0:15], v[84:87], v[92:95], v[0:15]\n\t" \
    "ds_read_b128 v[56:59], v96 offset:32768\n\t" \
    "v_exp_f32 v18, v18\n\t" \
    "v_exp_f32 v19, v19\n\t" \
    "v_cvt_pk_bf16_f32 v88, v16, v17\n\t" \
    "v_cvt_pk_bf16_f32 v89, v18, v19\n\t" \
    "s_waitcnt lgkmcnt(2)\n\t" \
    "v_mfma_f32_32x32x16_bf16 v[32:47], v[64:67], v[48:51], 0\n\t" \
    "ds_read_b128 v[60:63], v96 offset:33792\n\t" \
    "v_exp_f32 v20, v20\n\t" \
    "v_exp_f32 v21, v21\n\t" \
    "v_exp_f32 v22, v22\n\t" \
    "v_exp_f32 v23, v23\n\t" \
    "v_cvt_pk_bf16_f32 v90, v20, v21\n\t" \
    "v_cvt_pk_bf16_f32 v91, v22, v23\n\t" \
    "v_mfma_f32_32x32x16_bf16 v[32:47], v[68:71], v[52:55], v[32:47]\n\t" \
    "ds_read_b128 v[80:83], v96 offset:14336\n\t" \
    "v_exp_f32 v24, v24\n\t" \
    "v_exp_f32 v25, v25\n\t" \
    "v_exp_f32 v26, v26\n\t" \
    "v_exp_f32 v27, v27\n\t" \
    "v_cvt_pk_bf16_f32 v92, v24, v25\n\t" \
    "v_cvt_pk_bf16_f32 v93, v26, v27\n\t" \
    "v_mfma_f32_32x32x16_bf16 v[0:15], v[72:75], v[88:91], v[0:15]\n\t" \
    "ds_read_b128 v[84:87], v96 offset:15360\n\t" \
    "v_exp_f32 v28, v28\n\t" \
    "v_exp_f32 v29, v29\n\t" \
    "v_exp_f32 v30, v30\n\t" \
    "v_exp_f32 v31, v31\n\t" \
    "v_cvt_pk_bf16_f32 v94, v28, v29\n\t" \
    "v_cvt_pk_bf16_f32 v95, v30, v31\n\t" \
    "v_exp_f32 v32, v32\n\t" \
    "v_exp_f32 v33, v33\n\t" \
    "v_mfma_f32_32x32x16_bf16 v[0:15], v[76:79], v[92:95], v[0:15]\n\t" \
    "ds_read_b128 v[64:67], v96 offset:36864\n\t" \
    "v_exp_f32 v34, v34\n\t" \
    "v_exp_f32 v35, v35\n\t" \
    "v_cvt_pk_bf16_f32 v88, v32, v33\n\t" \
    "v_cvt_pk_bf16_f32 v89, v34, v35\n\t" \
    "s_waitcnt lgkmcnt(2)\n\t" \
    "v_mfma_f32_32x32x16_bf16 v[16:31], v[56:59], v[48:51], 0\n\t" \
    "ds_read_b128 v[68:71], v96 offset:37888\n\t" \
    "v_exp_f32 v36, v36\n\t" \
    "v_exp_f32 v37, v37\n\t" \
    "v_exp_f32 v38, v38\n\t" \
    "v_exp_f32 v39, v39\n\t" \
    "v_cvt_pk_bf16_f32 v90, v36, v37\n\t" \
    "v_cvt_pk_bf16_f32 v91, v38, v39\n\t" \
    "v_mfma_f32_32x32x16_bf16 v[16:31], v[60:63], v[52:55], v[16:31]\n\t" \
    "ds_read_b128 v[72:75], v96 offset:34816\n\t" \
    "v_exp_f32 v40, v40\n\t" \
    "v_exp_f32 v41, v41\n\t" \
    "v_exp_f32 v42, v42\n\t" \
    "v_exp_f32 v43, v43\n\t" \
    "v_cvt_pk_bf16_f32 v92, v40, v41\n\t" \
    "v_cvt_pk_bf16_f32 v93, v42, v43\n\t" \
    "v_mfma_f32_32x32x16_bf16 v[0:15], v[80:83], v[88:91], v[0:15]\n\t" \
    "ds_read_b128 v[76:79], v96 offset:35840\n\t" \
    "v_exp_f32 v44, v44\n\t" \
    "v_exp_f32 v45, v45\n\t" \
    "v_exp_f32 v46, v46\n\t" \
    "v_exp_f32 v47, v47\n\t" \
    "v_cvt_pk_bf16_f32 v94, v44, v45\n\t" \
    "v_cvt_pk_bf16_f32 v95, v46, v47\n\t" \
    "v_exp_f32 v16, v16\n\t" \
    "v_exp_f32 v17, v17\n\t" \
    "v_mfma_f32_32x32x16_bf16 v[0:15], v[84:87], v[92:95], v[0:15]\n\t" \
    "ds_read_b128 v[56:59], v96 offset:40960\n\t" \
    "v_exp_f32 v18, v18\n\t" \
    "v_exp_f32 v19, v19\n\t" \
    "v_cvt_pk_bf16_f32 v88, v16, v17\n\t" \
    "v_cvt_pk_bf16_f32 v89, v18, v19\n\t" \
    "s_waitcnt lgkmcnt(2)\n\t" \
    "v_mfma_f32_32x32x16_bf16 v[32:47], v[64:67], v[48:51], 0\n\t" \
    "ds_read_b128 v[60:63], v96 offset:41984\n\t" \
    "v_exp_f32 v20, v20\n\t" \
    "v_exp_f32 v21, v21\n\t" \
    "v_exp_f32 v22, v22\n\t" \
    "v_exp_f32 v23, v23\n\t" \
    "v_cvt_pk_bf16_f32 v90, v20, v21\n\t" \
    "v_cvt_pk_bf16_f32 v91, v22, v23\n\t" \
    "v_mfma_f32_32x32x16_bf16 v[32:47], v[68:71], v[52:55], v[32:47]\n\t" \
    "ds_read_b128 v[80:83], v96 offset:38912\n\t" \
    "v_exp_f32 v24, v24\n\t" \
    "v_exp_f32 v25, v25\n\t" \
    "v_exp_f32 v26, v26\n\t" \
    "v_exp_f32 v27, v27\n\t" \
    "v_cvt_pk_bf16_f32 v92, v24, v25\n\t" \
    "v_cvt_pk_bf16_f32 v93, v26, v27\n\t" \
    "v_mfma_f32_32x32x16_bf16 v[0:15], v[72:75], v[88:91], v[0:15]\n\t" \
    "ds_read_b128 v[84:87], v96 offset:39936\n\t" \
    "v_exp_f32 v28, v28\n\t" \
    "v_exp_f32 v29, v29\n\t" \
    "v_exp_f32 v30, v30\n\t" \
    "v_exp_f32 v31, v31\n\t" \
    "v_cvt_pk_bf16_f32 v94, v28, v29\n\t" \
    "v_cvt_pk_bf16_f32 v95, v30, v31\n\t" \
    "v_exp_f32 v32, v32\n\t" \
    "v_exp_f32 v33, v33\n\t" \
    "v_mfma_f32_32x32x16_bf16 v[0:15], v[76:79], v[92:95], v[0:15]\n\t" \
    "ds_read_b128 v[64:67], v96 offset:45056\n\t" \
    "v_exp_f32 v34, v34\n\t" \
    "v_exp_f32 v35, v35\n\t" \
    "v_cvt_pk_bf16_f32 v88, v32, v33\n\t" \
    "v_cvt_pk_bf16_f32 v89, v34, v35\n\t" \
    "s_waitcnt lgkmcnt(2)\n\t" \
    "v_mfma_f32_32x32x16_bf16 v[16:31], v[56:59], v[48:51], 0\n\t" \
    "ds_read_b128 v[68:71], v96 offset:46080\n\t" \
    "v_exp_f32 v36, v36\n\t" \
    "v_exp_f32 v37, v37\n\t" \
    "v_exp_f32 v38, v38\n\t" \
    "v_exp_f32 v39, v39\n\t" \
    "v_cvt_pk_bf16_f32 v90, v36, v37\n\t" \
    "v_cvt_pk_bf16_f32 v91, v38, v39\n\t" \
    "v_mfma_f32_32x32x16_bf16 v[16:31], v[60:63], v[52:55], v[16:31]\n\t" \
    "ds_read_b128 v[72:75], v96 offset:43008\n\t" \
    "v_exp_f32 v40, v40\n\t" \
    "v_exp_f32 v41, v41\n\t" \
    "v_exp_f32 v42, v42\n\t" \
    "v_exp_f32 v43, v43\n\t" \
    "v_cvt_pk_bf16_f32 v92, v40, v41\n\t" \
    "v_cvt_pk_bf16_f32 v93, v42, v43\n\t" \
    "v_mfma_f32_32x32x16_bf16 v[0:15], v[80:83], v[88:91], v[0:15]\n\t" \
    "ds_read_b128 v[76:79], v96 offset:44032\n\t" \
    "v_exp_f32 v44, v44\n\t" \
    "v_exp_f32 v45, v45\n\t" \
    "v_exp_f32 v46, v46\n\t" \
    "v_exp_f32 v47, v47\n\t" \
    "v_cvt_pk_bf16_f32 v94, v44, v45\n\t" \
    "v_cvt_pk_bf16_f32 v95, v46, v47\n\t" \
    "s_waitcnt vmcnt(4)\n\t" \
    "s_barrier\n\t" \
    "s_mov_b32 m0, s42\n\t" \
    "s_add_u32 s40, s40, 16384\n\t" \
    "s_addc_u32 s41, s41, 0\n\t" \
    "global_load_lds_dwordx4 v98, s[40:41] offset:0\n\t" \
    "global_load_lds_dwordx4 v98, s[40:41] offset:1024\n\t" \
    "global_load_lds_dwordx4 v98, s[40:41] offset:2048\n\t" \
    "global_load_lds_dwordx4 v98, s[40:41] offset:3072\n\t" \
    "v_exp_f32 v16, v16\n\t" \
    "v_exp_f32 v17, v17\n\t" \
    "v_mfma_f32_32x32x16_bf16 v[0:15], v[84:87], v[92:95], v[0:15]\n\t" \
    "ds_read_b128 v[56:59], v97 offset:0\n\t" \
    "v_exp_f32 v18, v18\n\t" \
    "v_exp_f32 v19, v19\n\t" \
    "v_cvt_pk_bf16_f32 v88, v16, v17\n\t" \
    "v_cvt_pk_bf16_f32 v89, v18, v19\n\t" \
    "s_waitcnt lgkmcnt(2)\n\t" \
    "v_mfma_f32_32x32x16_bf16 v[32:47], v[64:67], v[48:51], 0\n\t" \
    "ds_read_b128 v[60:63], v97 offset:1024\n\t" \
    "v_exp_f32 v20, v20\n\t" \
    "v_exp_f32 v21, v21\n\t" \
    "v_exp_f32 v22, v22\n\t" \
    "v_exp_f32 v23, v23\n\t" \
    "v_cvt_pk_bf16_f32 v90, v20, v21\n\t" \
    "v_cvt_pk_bf16_f32 v91, v22, v23\n\t" \
    "v_mfma_f32_32x32x16_bf16 v[32:47], v[68:71], v[52:55], v[32:47]\n\t" \
    "ds_read_b128 v[80:83], v96 offset:47104\n\t" \
    "v_exp_f32 v24, v24\n\t" \
    "v_exp_f32 v25, v25\n\t" \
    "v_exp_f32 v26, v26\n\t" \
    "v_exp_f32 v27, v27\n\t" \
    "v_cvt_pk_bf16_f32 v92, v24, v25\n\t" \
    "v_cvt_pk_bf16_f32 v93, v26, v27\n\t" \
    "v_mfma_f32_32x32x16_bf16 v[0:15], v[72:75], v[88:91], v[0:15]\n\t" \
    "ds_read_b128 v[84:87], v96 offset:48128\n\t" \
    "v_exp_f32 v28, v28\n\t" \
    "v_exp_f32 v29, v29\n\t" \
    "v_exp_f32 v30, v30\n\t" \
    "v_exp_f32 v31, v31\n\t" \
    "v_cvt_pk_bf16_f32 v94, v28, v29\n\t" \
    "v_cvt_pk_bf16_f32 v95, v30, v31\n\t" \
    "v_exp_f32 v32, v32\n\t" \
    "v_exp_f32 v33, v33\n\t" \
    "v_mfma_f32_32x32x16_bf16 v[0:15], v[76:79], v[92:95], v[0:15]\n\t" \
    "ds_read_b128 v[64:67], v97 offset:4096\n\t" \
    "v_exp_f32 v34, v34\n\t" \
    "v_exp_f32 v35, v35\n\t" \
    "v_cvt_pk_bf16_f32 v88, v32, v33\n\t" \
    "v_cvt_pk_bf16_f32 v89, v34, v35\n\t" \
    "s_waitcnt lgkmcnt(2)\n\t" \
    "v_mfma_f32_32x32x16_bf16 v[16:31], v[56:59], v[48:51], 0\n\t" \
    "ds_read_b128 v[68:71], v97 offset:5120\n\t" \
    "v_exp_f32 v36, v36\n\t" \
    "v_exp_f32 v37, v37\n\t" \
    "v_exp_f32 v38, v38\n\t" \
    "v_exp_f32 v39, v39\n\t" \
    "v_cvt_pk_bf16_f32 v90, v36, v37\n\t" \
    "v_cvt_pk_bf16_f32 v91, v38, v39\n\t" \
    "v_mfma_f32_32x32x16_bf16 v[16:31], v[60:63], v[52:55], v[16:31]\n\t" \
    "ds_read_b128 v[72:75], v97 offset:2048\n\t" \
    "v_exp_f32 v40, v40\n\t" \
    "v_exp_f32 v41, v41\n\t" \
    "v_exp_f32 v42, v42\n\t" \
    "v_exp_f32 v43, v43\n\t" \
    "v_cvt_pk_bf16_f32 v92, v40, v41\n\t" \
    "v_cvt_pk_bf16_f32 v93, v42, v43\n\t" \
    "v_mfma_f32_32x32x16_bf16 v[0:15], v[80:83], v[88:91], v[0:15]\n\t" \
    "ds_read_b128 v[76:79], v97 offset:3072\n\t" \
    "v_exp_f32 v44, v44\n\t" \
    "v_exp_f32 v45, v45\n\t" \
    "v_exp_f32 v46, v46\n\t" \
    "v_exp_f32 v47, v47\n\t" \
    "v_cvt_pk_bf16_f32 v94, v44, v45\n\t" \
    "v_cvt_pk_bf16_f32 v95, v46, v47\n\t" \
    "v_exp_f32 v16, v16\n\t" \
    "v_exp_f32 v17, v17\n\t" \
    "v_mfma_f32_32x32x16_bf16 v[0:15], v[84:87], v[92:95], v[0:15]\n\t" \
    "ds_read_b128 v[56:59], v97 offset:8192\n\t" \
    "v_exp_f32 v18, v18\n\t" \
    "v_exp_f32 v19, v19\n\t" \
    "v_cvt_pk_bf16_f32 v88, v16, v17\n\t" \
    "v_cvt_pk_bf16_f32 v89, v18, v19\n\t" \
    "s_waitcnt lgkmcnt(2)\n\t" \
    "v_mfma_f32_32x32x16_bf16 v[32:47], v[64:67], v[48:51], 0\n\t" \
    "ds_read_b128 v[60:63], v97 offset:9216\n\t" \
    "v_exp_f32 v20, v20\n\t" \
    "v_exp_f32 v21, v21\n\t" \
    "v_exp_f32 v22, v22\n\t" \
    "v_exp_f32 v23, v23\n\t" \
    "v_cvt_pk_bf16_f32 v90, v20, v21\n\t" \
    "v_cvt_pk_bf16_f32 v91, v22, v23\n\t" \
    "v_mfma_f32_32x32x16_bf16 v[32:47], v[68:71], v[52:55], v[32:47]\n\t" \
    "ds_read_b128 v[80:83], v97 offset:6144\n\t" \
    "v_exp_f32 v24, v24\n\t" \
    "v_exp_f32 v25, v25\n\t" \
    "v_exp_f32 v26, v26\n\t" \
    "v_exp_f32 v27, v27\n\t" \
    "v_cvt_pk_bf16_f32 v92, v24, v25\n\t" \
    "v_cvt_pk_bf16_f32 v93, v26, v27\n\t" \
    "v_mfma_f32_32x32x16_bf16 v[0:15], v[72:75], v[88:91], v[0:15]\n\t" \
    "ds_read_b128 v[84:87], v97 offset:7168\n\t" \
    "v_exp_f32 v28, v28\n\t" \
    "v_exp_f32 v29, v29\n\t" \
    "v_exp_f32 v30, v30\n\t" \
    "v_exp_f32 v31, v31\n\t" \
    "v_cvt_pk_bf16_f32 v94, v28, v29\n\t" \
    "v_cvt_pk_bf16_f32 v95, v30, v31\n\t" \
    "v_exp_f32 v32, v32\n\t" \
    "v_exp_f32 v33, v33\n\t" \
    "v_mfma_f32_32x32x16_bf16 v[0:15], v[76:79], v[92:95], v[0:15]\n\t" \
    "ds_read_b128 v[64:67], v97 offset:12288\n\t" \
    "v_exp_f32 v34, v34\n\t" \
    "v_exp_f32 v35, v35\n\t" \
    "v_cvt_pk_bf16_f32 v88, v32, v33\n\t" \
    "v_cvt_pk_bf16_f32 v89, v34, v35\n\t" \
    "s_waitcnt lgkmcnt(2)\n\t" \
    "v_mfma_f32_32x32x16_bf16 v[16:31], v[56:59], v[48:51], 0\n\t" \
    "ds_read_b128 v[68:71], v97 offset:13312\n\t" \
    "v_exp_f32 v36, v36\n\t" \
    "v_exp_f32 v37, v37\n\t" \
    "v_exp_f32 v38, v38\n\t" \
    "v_exp_f32 v39, v39\n\t" \
    "v_cvt_pk_bf16_f32 v90, v36, v37\n\t" \
    "v_cvt_pk_bf16_f32 v91, v38, v39\n\t" \
    "v_mfma_f32_32x32x16_bf16 v[16:31], v[60:63], v[52:55], v[16:31]\n\t" \
    "ds_read_b128 v[72:75], v97 offset:10240\n\t" \
    "v_exp_f32 v40, v40\n\t" \
    "v_exp_f32 v41, v41\n\t" \
    "v_exp_f32 v42, v42\n\t" \
    "v_exp_f32 v43, v43\n\t" \
    "v_cvt_pk_bf16_f32 v92, v40, v41\n\t" \
    "v_cvt_pk_bf16_f32 v93, v42, v43\n\t" \
    "v_mfma_f32_32x32x16_bf16 v[0:15], v[80:83], v[88:91], v[0:15]\n\t" \
    "ds_read_b128 v[76:79], v97 offset:11264\n\t" \
    "v_exp_f32 v44, v44\n\t" \
    "v_exp_f32 v45, v45\n\t" \
    "v_exp_f32 v46, v46\n\t" \
    "v_exp_f32 v47, v47\n\t" \
    "v_cvt_pk_bf16_f32 v94, v44, v45\n\t" \
    "v_cvt_pk_bf16_f32 v95, v46, v47\n\t" \
    "s_waitcnt vmcnt(4)\n\t" \
    "s_barrier\n\t" \
    "s_add_u32 m0, s42, 32768\n\t" \
    "s_add_u32 s40, s40, 16384\n\t" \
    "s_addc_u32 s41, s41, 0\n\t" \
    "global_load_lds_dwordx4 v98, s[40:41] offset:0\n\t" \
    "global_load_lds_dwordx4 v98, s[40:41] offset:1024\n\t" \
    "global_load_lds_dwordx4 v98, s[40:41] offset:2048\n\t" \
    "global_load_lds_dwordx4 v98, s[40:41] offset:3072\n\t" \
    "v_exp_f32 v16, v16\n\t" \
    "v_exp_f32 v17, v17\n\t" \
    "v_mfma_f32_32x32x16_bf16 v[0:15], v[84:87], v[92:95], v[0:15]\n\t" \
    "ds_read_b128 v[56:59], v97 offset:32768\n\t" \
    "v_exp_f32 v18, v18\n\t" \
    "v_exp_f32 v19, v19\n\t" \
    "v_cvt_pk_bf16_f32 v88, v16, v17\n\t" \
    "v_cvt_pk_bf16_f32 v89, v18, v19\n\t" \
    "s_waitcnt lgkmcnt(2)\n\t" \
    "v_mfma_f32_32x32x16_bf16 v[32:47], v[64:67], v[48:51], 0\n\t" \
    "ds_read_b128 v[60:63], v97 offset:33792\n\t" \
    "v_exp_f32 v20, v20\n\t" \
    "v_exp_f32 v21, v21\n\t" \
    "v_exp_f32 v22, v22\n\t" \
    "v_exp_f32 v23, v23\n\t" \
    "v_cvt_pk_bf16_f32 v90, v20, v21\n\t" \
    "v_cvt_pk_bf16_f32 v91, v22, v23\n\t" \
    "v_mfma_f32_32x32x16_bf16 v[32:47], v[68:71], v[52:55], v[32:47]\n\t" \
    "ds_read_b128 v[80:83], v97 offset:14336\n\t" \
    "v_exp_f32 v24, v24\n\t" \
    "v_exp_f32 v25, v25\n\t" \
    "v_exp_f32 v26, v26\n\t" \
    "v_exp_f32 v27, v27\n\t" \
    "v_cvt_pk_bf16_f32 v92, v24, v25\n\t" \
    "v_cvt_pk_bf16_f32 v93, v26, v27\n\t" \
    "v_mfma_f32_32x32x16_bf16 v[0:15], v[72:75], v[88:91], v[0:15]\n\t" \
    "ds_read_b128 v[84:87], v97 offset:15360\n\t" \
    "v_exp_f32 v28, v28\n\t" \
    "v_exp_f32 v29, v29\n\t" \
    "v_exp_f32 v30, v30\n\t" \
    "v_exp_f32 v31, v31\n\t" \
    "v_cvt_pk_bf16_f32 v94, v28, v29\n\t" \
    "v_cvt_pk_bf16_f32 v95, v30, v31\n\t" \
    "v_exp_f32 v32, v32\n\t" \
    "v_exp_f32 v33, v33\n\t" \
    "v_mfma_f32_32x32x16_bf16 v[0:15], v[76:79], v[92:95], v[0:15]\n\t" \
    "ds_read_b128 v[64:67], v97 offset:36864\n\t" \
    "v_exp_f32 v34, v34\n\t" \
    "v_exp_f32 v35, v35\n\t" \
    "v_cvt_pk_bf16_f32 v88, v32, v33\n\t" \
    "v_cvt_pk_bf16_f32 v89, v34, v35\n\t" \
    "s_waitcnt lgkmcnt(2)\n\t" \
    "v_mfma_f32_32x32x16_bf16 v[16:31], v[56:59], v[48:51], 0\n\t" \
    "ds_read_b128 v[68:71], v97 offset:37888\n\t" \
    "v_exp_f32 v36, v36\n\t" \
    "v_exp_f32 v37, v37\n\t" \
    "v_exp_f32 v38, v38\n\t" \
    "v_exp_f32 v39, v39\n\t" \
    "v_cvt_pk_bf16_f32 v90, v36, v37\n\t" \
    "v_cvt_pk_bf16_f32 v91, v38, v39\n\t" \
    "v_mfma_f32_32x32x16_bf16 v[16:31], v[60:63], v[52:55], v[16:31]\n\t" \
    "ds_read_b128 v[72:75], v97 offset:34816\n\t" \
    "v_exp_f32 v40, v40\n\t" \
    "v_exp_f32 v41, v41\n\t" \
    "v_exp_f32 v42, v42\n\t" \
    "v_exp_f32 v43, v43\n\t" \
    "v_cvt_pk_bf16_f32 v92, v40, v41\n\t" \
    "v_cvt_pk_bf16_f32 v93, v42, v43\n\t" \
    "v_mfma_f32_32x32x16_bf16 v[0:15], v[80:83], v[88:91], v[0:15]\n\t" \
    "ds_read_b128 v[76:79], v97 offset:35840\n\t" \
    "v_exp_f32 v44, v44\n\t" \
    "v_exp_f32 v45, v45\n\t" \
    "v_exp_f32 v46, v46\n\t" \
    "v_exp_f32 v47, v47\n\t" \
    "v_cvt_pk_bf16_f32 v94, v44, v45\n\t" \
    "v_cvt_pk_bf16_f32 v95, v46, v47\n\t" \
    "v_exp_f32 v16, v16\n\t" \
    "v_exp_f32 v17, v17\n\t" \
    "v_mfma_f32_32x32x16_bf16 v[0:15], v[84:87], v[92:95], v[0:15]\n\t" \
    "ds_read_b128 v[56:59], v97 offset:40960\n\t" \
    "v_exp_f32 v18, v18\n\t" \
    "v_exp_f32 v19, v19\n\t" \
    "v_cvt_pk_bf16_f32 v88, v16, v17\n\t" \
    "v_cvt_pk_bf16_f32 v89, v18, v19\n\t" \
    "s_waitcnt lgkmcnt(2)\n\t" \
    "v_mfma_f32_32x32x16_bf16 v[32:47], v[64:67], v[48:51], 0\n\t" \
    "ds_read_b128 v[60:63], v97 offset:41984\n\t" \
    "v_exp_f32 v20, v20\n\t" \
    "v_exp_f32 v21, v21\n\t" \
    "v_exp_f32 v22, v22\n\t" \
    "v_exp_f32 v23, v23\n\t" \
    "v_cvt_pk_bf16_f32 v90, v20, v21\n\t" \
    "v_cvt_pk_bf16_f32 v91, v22, v23\n\t" \
    "v_mfma_f32_32x32x16_bf16 v[32:47], v[68:71], v[52:55], v[32:47]\n\t" \
    "ds_read_b128 v[80:83], v97 offset:38912\n\t" \
    "v_exp_f32 v24, v24\n\t" \
    "v_exp_f32 v25, v25\n\t" \
    "v_exp_f32 v26, v26\n\t" \
    "v_exp_f32 v27, v27\n\t" \
    "v_cvt_pk_bf16_f32 v92, v24, v25\n\t" \
    "v_cvt_pk_bf16_f32 v93, v26, v27\n\t" \
    "v_mfma_f32_32x32x16_bf16 v[0:15], v[72:75], v[88:91], v[0:15]\n\t" \
    "ds_read_b128 v[84:87], v97 offset:39936\n\t" \
    "v_exp_f32 v28, v28\n\t" \
    "v_exp_f32 v29, v29\n\t" \
    "v_exp_f32 v30, v30\n\t" \
    "v_exp_f32 v31, v31\n\t" \
    "v_cvt_pk_bf16_f32 v94, v28, v29\n\t" \
    "v_cvt_pk_bf16_f32 v95, v30, v31\n\t" \
    "v_exp_f32 v32, v32\n\t" \
    "v_exp_f32 v33, v33\n\t" \
    "v_mfma_f32_32x32x16_bf16 v[0:15], v[76:79], v[92:95], v[0:15]\n\t" \
    "ds_read_b128 v[64:67], v97 offset:45056\n\t" \
    "v_exp_f32 v34, v34\n\t" \
    "v_exp_f32 v35, v35\n\t" \
    "v_cvt_pk_bf16_f32 v88, v32, v33\n\t" \
    "v_cvt_pk_bf16_f32 v89, v34, v35\n\t" \
    "s_waitcnt lgkmcnt(2)\n\t" \
    "v_mfma_f32_32x32x16_bf16 v[16:31], v[56:59], v[48:51], 0\n\t" \
    "ds_read_b128 v[68:71], v97 offset:46080\n\t" \
    "v_exp_f32 v36, v36\n\t" \
    "v_exp_f32 v37, v37\n\t" \
    "v_exp_f32 v38, v38\n\t" \
    "v_exp_f32 v39, v39\n\t" \
    "v_cvt_pk_bf16_f32 v90, v36, v37\n\t" \
    "v_cvt_pk_bf16_f32 v91, v38, v39\n\t" \
    "v_mfma_f32_32x32x16_bf16 v[16:31], v[60:63], v[52:55], v[16:31]\n\t" \
    "ds_read_b128 v[72:75], v97 offset:43008\n\t" \
    "v_exp_f32 v40, v40\n\t" \
    "v_exp_f32 v41, v41\n\t" \
    "v_exp_f32 v42, v42\n\t" \
    "v_exp_f32 v43, v43\n\t" \
    "v_cvt_pk_bf16_f32 v92, v40, v41\n\t" \
    "v_cvt_pk_bf16_f32 v93, v42, v43\n\t" \
    "v_mfma_f32_32x32x16_bf16 v[0:15], v[80:83], v[88:91], v[0:15]\n\t" \
    "ds_read_b128 v[76:79], v97 offset:44032\n\t" \
    "v_exp_f32 v44, v44\n\t" \
    "v_exp_f32 v45, v45\n\t" \
    "v_exp_f32 v46, v46\n\t" \
    "v_exp_f32 v47, v47\n\t" \
    "v_cvt_pk_bf16_f32 v94, v44, v45\n\t" \
    "v_cvt_pk_bf16_f32 v95, v46, v47\n\t" \
    "s_waitcnt vmcnt(4)\n\t" \
    "s_barrier\n\t" \
    "s_add_u32 m0, s42, 65536\n\t" \
    "s_add_u32 s40, s40, 16384\n\t" \
    "s_addc_u32 s41, s41, 0\n\t" \
    "global_load_lds_dwordx4 v98, s[40:41] offset:0\n\t" \
    "global_load_lds_dwordx4 v98, s[40:41] offset:1024\n\t" \
    "global_load_lds_dwordx4 v98, s[40:41] offset:2048\n\t" \
    "global_load_lds_dwordx4 v98, s[40:41] offset:3072\n\t" \
    "v_exp_f32 v16, v16\n\t" \
    "v_exp_f32 v17, v17\n\t" \
    "v_mfma_f32_32x32x16_bf16 v[0:15], v[84:87], v[92:95], v[0:15]\n\t" \
    "ds_read_b128 v[56:59], v96 offset:0\n\t" \
    "v_exp_f32 v18, v18\n\t" \
    "v_exp_f32 v19, v19\n\t" \
    "v_cvt_pk_bf16_f32 v88, v16, v17\n\t" \
    "v_cvt_pk_bf16_f32 v89, v18, v19\n\t" \
    "s_waitcnt lgkmcnt(2)\n\t" \
    "v_mfma_f32_32x32x16_bf16 v[32:47], v[64:67], v[48:51], 0\n\t" \
    "ds_read_b128 v[60:63], v96 offset:1024\n\t" \
    "v_exp_f32 v20, v20\n\t" \
    "v_exp_f32 v21, v21\n\t" \
    "v_exp_f32 v22, v22\n\t" \
    "v_exp_f32 v23, v23\n\t" \
    "v_cvt_pk_bf16_f32 v90, v20, v21\n\t" \
    "v_cvt_pk_bf16_f32 v91, v22, v23\n\t" \
    "v_mfma_f32_32x32x16_bf16 v[32:47], v[68:71], v[52:55], v[32:47]\n\t" \
    "ds_read_b128 v[80:83], v97 offset:47104\n\t" \
    "v_exp_f32 v24, v24\n\t" \
    "v_exp_f32 v25, v25\n\t" \
    "v_exp_f32 v26, v26\n\t" \
    "v_exp_f32 v27, v27\n\t" \
    "v_cvt_pk_bf16_f32 v92, v24, v25\n\t" \
    "v_cvt_pk_bf16_f32 v93, v26, v27\n\t" \
    "v_mfma_f32_32x32x16_bf16 v[0:15], v[72:75], v[88:91], v[0:15]\n\t" \
    "ds_read_b128 v[84:87], v97 offset:48128\n\t" \
    "v_exp_f32 v28, v28\n\t" \
    "v_exp_f32 v29, v29\n\t" \
    "v_exp_f32 v30, v30\n\t" \
    "v_exp_f32 v31, v31\n\t" \
    "v_cvt_pk_bf16_f32 v94, v28, v29\n\t" \
    "v_cvt_pk_bf16_f32 v95, v30, v31\n\t" \
    "v_exp_f32 v32, v32\n\t" \
    "v_exp_f32 v33, v33\n\t" \
    "v_mfma_f32_32x32x16_bf16 v[0:15], v[76:79], v[92:95], v[0:15]\n\t" \
    "ds_read_b128 v[64:67], v96 offset:4096\n\t" \
    "v_exp_f32 v34, v34\n\t" \
    "v_exp_f32 v35, v35\n\t" \
    "v_cvt_pk_bf16_f32 v88, v32, v33\n\t" \
    "v_cvt_pk_bf16_f32 v89, v34, v35\n\t" \
    "s_waitcnt lgkmcnt(2)\n\t" \
    "v_mfma_f32_32x32x16_bf16 v[16:31], v[56:59], v[48:51], 0\n\t" \
    "ds_read_b128 v[68:71], v96 offset:5120\n\t" \
    "v_exp_f32 v36, v36\n\t" \
    "v_exp_f32 v37, v37\n\t" \
    "v_exp_f32 v38, v38\n\t" \
    "v_exp_f32 v39, v39\n\t" \
    "v_cvt_pk_bf16_f32 v90, v36, v37\n\t" \
    "v_cvt_pk_bf16_f32 v91, v38, v39\n\t" \
    "v_mfma_f32_32x32x16_bf16 v[16:31], v[60:63], v[52:55], v[16:31]\n\t" \
    "ds_read_b128 v[72:75], v96 offset:2048\n\t" \
    "v_exp_f32 v40, v40\n\t" \
    "v_exp_f32 v41, v41\n\t" \
    "v_exp_f32 v42, v42\n\t" \
    "v_exp_f32 v43, v43\n\t" \
    "v_cvt_pk_bf16_f32 v92, v40, v41\n\t" \
    "v_cvt_pk_bf16_f32 v93, v42, v43\n\t" \
    "v_mfma_f32_32x32x16_bf16 v[0:15], v[80:83], v[88:91], v[0:15]\n\t" \
    "ds_read_b128 v[76:79], v96 offset:3072\n\t" \
    "v_exp_f32 v44, v44\n\t" \
    "v_exp_f32 v45, v45\n\t" \
    "v_exp_f32 v46, v46\n\t" \
    "v_exp_f32 v47, v47\n\t" \
    "v_cvt_pk_bf16_f32 v94, v44, v45\n\t" \
    "v_cvt_pk_bf16_f32 v95, v46, v47\n\t" \
    "v_exp_f32 v16, v16\n\t" \
    "v_exp_f32 v17, v17\n\t" \
    "v_mfma_f32_32x32x16_bf16 v[0:15], v[84:87], v[92:95], v[0:15]\n\t" \
    "ds_read_b128 v[56:59], v96 offset:8192\n\t" \
    "v_exp_f32 v18, v18\n\t" \
    "v_exp_f32 v19, v19\n\t" \
    "v_cvt_pk_bf16_f32 v88, v16, v17\n\t" \
    "v_cvt_pk_bf16_f32 v89, v18, v19\n\t" \
    "s_waitcnt lgkmcnt(2)\n\t" \
    "v_mfma_f32_32x32x16_bf16 v[32:47], v[64:67], v[48:51], 0\n\t" \
    "ds_read_b128 v[60:63], v96 offset:9216\n\t" \
    "v_exp_f32 v20, v20\n\t" \
    "v_exp_f32 v21, v21\n\t" \
    "v_exp_f32 v22, v22\n\t" \
    "v_exp_f32 v23, v23\n\t" \
    "v_cvt_pk_bf16_f32 v90, v20, v21\n\t" \
    "v_cvt_pk_bf16_f32 v91, v22, v23\n\t" \
    "v_mfma_f32_32x32x16_bf16 v[32:47], v[68:71], v[52:55], v[32:47]\n\t" \
    "ds_read_b128 v[80:83], v96 offset:6144\n\t" \
    "v_exp_f32 v24, v24\n\t" \
    "v_exp_f32 v25, v25\n\t" \
    "v_exp_f32 v26, v26\n\t" \
    "v_exp_f32 v27, v27\n\t" \
    "v_cvt_pk_bf16_f32 v92, v24, v25\n\t" \
    "v_cvt_pk_bf16_f32 v93, v26, v27\n\t" \
    "v_mfma_f32_32x32x16_bf16 v[0:15], v[72:75], v[88:91], v[0:15]\n\t" \
    "ds_read_b128 v[84:87], v96 offset:7168\n\t" \
    "v_exp_f32 v28, v28\n\t" \
    "v_exp_f32 v29, v29\n\t" \
    "v_exp_f32 v30, v30\n\t" \
    "v_exp_f32 v31, v31\n\t" \
    "v_cvt_pk_bf16_f32 v94, v28, v29\n\t" \
    "v_cvt_pk_bf16_f32 v95, v30, v31\n\t" \
    "v_exp_f32 v32, v32\n\t" \
    "v_exp_f32 v33, v33\n\t" \
    "v_mfma_f32_32x32x16_bf16 v[0:15], v[76:79], v[92:95], v[0:15]\n\t" \
    "ds_read_b128 v[64:67], v96 offset:12288\n\t" \
    "v_exp_f32 v34, v34\n\t" \
    "v_exp_f32 v35, v35\n\t" \
    "v_cvt_pk_bf16_f32 v88, v32, v33\n\t" \
    "v_cvt_pk_bf16_f32 v89, v34, v35\n\t" \
    "s_waitcnt lgkmcnt(2)\n\t" \
    "v_mfma_f32_32x32x16_bf16 v[16:31], v[56:59], v[48:51], 0\n\t" \
    "ds_read_b128 v[68:71], v96 offset:13312\n\t" \
    "v_exp_f32 v36, v36\n\t" \
    "v_exp_f32 v37, v37\n\t" \
    "v_exp_f32 v38, v38\n\t" \
    "v_exp_f32 v39, v39\n\t" \
    "v_cvt_pk_bf16_f32 v90, v36, v37\n\t" \
    "v_cvt_pk_bf16_f32 v91, v38, v39\n\t" \
    "v_mfma_f32_32x32x16_bf16 v[16:31], v[60:63], v[52:55], v[16:31]\n\t" \
    "ds_read_b128 v[72:75], v96 offset:10240\n\t" \
    "v_exp_f32 v40, v40\n\t" \
    "v_exp_f32 v41, v41\n\t" \
    "v_exp_f32 v42, v42\n\t" \
    "v_exp_f32 v43, v43\n\t" \
    "v_cvt_pk_bf16_f32 v92, v40, v41\n\t" \
    "v_cvt_pk_bf16_f32 v93, v42, v43\n\t" \
    "v_mfma_f32_32x32x16_bf16 v[0:15], v[80:83], v[88:91], v[0:15]\n\t" \
    "ds_read_b128 v[76:79], v96 offset:11264\n\t" \
    "v_exp_f32 v44, v44\n\t" \
    "v_exp_f32 v45, v45\n\t" \
    "v_exp_f32 v46, v46\n\t" \
    "v_exp_f32 v47, v47\n\t" \
    "v_cvt_pk_bf16_f32 v94, v44, v45\n\t" \
    "v_cvt_pk_bf16_f32 v95, v46, v47\n\t" \
    "s_waitcnt vmcnt(4)\n\t" \
    "s_barrier\n\t" \
    "s_add_u32 m0, s42, 98304\n\t" \
    "s_add_u32 s40, s40, 16384\n\t" \
    "s_addc_u32 s41, s41, 0\n\t" \
    "global_load_lds_dwordx4 v98, s[40:41] offset:0\n\t" \
    "global_load_lds_dwordx4 v98, s[40:41] offset:1024\n\t" \
    "global_load_lds_dwordx4 v98, s[40:41] offset:2048\n\t" \
    "global_load_lds_dwordx4 v98, s[40:41] offset:3072\n\t" \
    "v_exp_f32 v16, v16\n\t" \
    "v_exp_f32 v17, v17\n\t" \
    "v_mfma_f32_32x32x16_bf16 v[0:15], v[84:87], v[92:95], v[0:15]\n\t" \
    "ds_read_b128 v[56:59], v96 offset:32768\n\t" \
    "v_exp_f32 v18, v18\n\t" \
    "v_exp_f32 v19, v19\n\t" \
    "v_cvt_pk_bf16_f32 v88, v16, v17\n\t" \
    "v_cvt_pk_bf16_f32 v89, v18, v19\n\t" \
    "s_waitcnt lgkmcnt(2)\n\t" \
    "v_mfma_f32_32x32x16_bf16 v[32:47], v[64:67], v[48:51], 0\n\t" \
    "ds_read_b128 v[60:63], v96 offset:33792\n\t" \
    "v_exp_f32 v20, v20\n\t" \
    "v_exp_f32 v21, v21\n\t" \
    "v_exp_f32 v22, v22\n\t" \
    "v_exp_f32 v23, v23\n\t" \
    "v_cvt_pk_bf16_f32 v90, v20, v21\n\t" \
    "v_cvt_pk_bf16_f32 v91, v22, v23\n\t" \
    "v_mfma_f32_32x32x16_bf16 v[32:47], v[68:71], v[52:55], v[32:47]\n\t" \
    "ds_read_b128 v[80:83], v96 offset:14336\n\t" \
    "v_exp_f32 v24, v24\n\t" \
    "v_exp_f32 v25, v25\n\t" \
    "v_exp_f32 v26, v26\n\t" \
    "v_exp_f32 v27, v27\n\t" \
    "v_cvt_pk_bf16_f32 v92, v24, v25\n\t" \
    "v_cvt_pk_bf16_f32 v93, v26, v27\n\t" \
    "v_mfma_f32_32x32x16_bf16 v[0:15], v[72:75], v[88:91], v[0:15]\n\t" \
    "ds_read_b128 v[84:87], v96 offset:15360\n\t" \
    "v_exp_f32 v28, v28\n\t" \
    "v_exp_f32 v29, v29\n\t" \
    "v_exp_f32 v30, v30\n\t" \
    "v_exp_f32 v31, v31\n\t" \
    "v_cvt_pk_bf16_f32 v94, v28, v29\n\t" \
    "v_cvt_pk_bf16_f32 v95, v30, v31\n\t" \
    "v_exp_f32 v32, v32\n\t" \
    "v_exp_f32 v33, v33\n\t" \
    "v_mfma_f32_32x32x16_bf16 v[0:15], v[76:79], v[92:95], v[0:15]\n\t" \
    "ds_read_b128 v[64:67], v96 offset:36864\n\t" \
    "v_exp_f32 v34, v34\n\t" \
    "v_exp_f32 v35, v35\n\t" \
    "v_cvt_pk_bf16_f32 v88, v32, v33\n\t" \
    "v_cvt_pk_bf16_f32 v89, v34, v35\n\t" \
    "s_waitcnt lgkmcnt(2)\n\t" \
    "v_mfma_f32_32x32x16_bf16 v[16:31], v[56:59], v[48:51], 0\n\t" \
    "ds_read_b128 v[68:71], v96 offset:37888\n\t" \
    "v_exp_f32 v36, v36\n\t" \
    "v_exp_f32 v37, v37\n\t" \
    "v_exp_f32 v38, v38\n\t" \
    "v_exp_f32 v39, v39\n\t" \
    "v_cvt_pk_bf16_f32 v90, v36, v37\n\t" \
    "v_cvt_pk_bf16_f32 v91, v38, v39\n\t" \
    "v_mfma_f32_32x32x16_bf16 v[16:31], v[60:63], v[52:55], v[16:31]\n\t" \
    "ds_read_b128 v[72:75], v96 offset:34816\n\t" \
    "v_exp_f32 v40, v40\n\t" \
    "v_exp_f32 v41, v41\n\t" \
    "v_exp_f32 v42, v42\n\t" \
    "v_exp_f32 v43, v43\n\t" \
    "v_cvt_pk_bf16_f32 v92, v40, v41\n\t" \
    "v_cvt_pk_bf16_f32 v93, v42, v43\n\t" \
    "v_mfma_f32_32x32x16_bf16 v[0:15], v[80:83], v[88:91], v[0:15]\n\t" \
    "ds_read_b128 v[76:79], v96 offset:35840\n\t" \
    "v_exp_f32 v44, v44\n\t" \
    "v_exp_f32 v45, v45\n\t" \
    "v_exp_f32 v46, v46\n\t" \
    "v_exp_f32 v47, v47\n\t" \
    "v_cvt_pk_bf16_f32 v94, v44, v45\n\t" \
    "v_cvt_pk_bf16_f32 v95, v46, v47\n\t" \
    "v_exp_f32 v16, v16\n\t" \
    "v_exp_f32 v17, v17\n\t" \
    "v_mfma_f32_32x32x16_bf16 v[0:15], v[84:87], v[92:95], v[0:15]\n\t" \
    "ds_read_b128 v[56:59], v96 offset:40960\n\t" \
    "v_exp_f32 v18, v18\n\t" \
    "v_exp_f32 v19, v19\n\t" \
    "v_cvt_pk_bf16_f32 v88, v16, v17\n\t" \
    "v_cvt_pk_bf16_f32 v89, v18, v19\n\t" \
    "s_waitcnt lgkmcnt(2)\n\t" \
    "v_mfma_f32_32x32x16_bf16 v[32:47], v[64:67], v[48:51], 0\n\t" \
    "ds_read_b128 v[60:63], v96 offset:41984\n\t" \
    "v_exp_f32 v20, v20\n\t" \
    "v_exp_f32 v21, v21\n\t" \
    "v_exp_f32 v22, v22\n\t" \
    "v_exp_f32 v23, v23\n\t" \
    "v_cvt_pk_bf16_f32 v90, v20, v21\n\t" \
    "v_cvt_pk_bf16_f32 v91, v22, v23\n\t" \
    "v_mfma_f32_32x32x16_bf16 v[32:47], v[68:71], v[52:55], v[32:47]\n\t" \
    "ds_read_b128 v[80:83], v96 offset:38912\n\t" \
    "v_exp_f32 v24, v24\n\t" \
    "v_exp_f32 v25, v25\n\t" \
    "v_exp_f32 v26, v26\n\t" \
    "v_exp_f32 v27, v27\n\t" \
    "v_cvt_pk_bf16_f32 v92, v24, v25\n\t" \
    "v_cvt_pk_bf16_f32 v93, v26, v27\n\t" \
    "v_mfma_f32_32x32x16_bf16 v[0:15], v[72:75], v[88:91], v[0:15]\n\t" \
    "ds_read_b128 v[84:87], v96 offset:39936\n\t" \
    "v_exp_f32 v28, v28\n\t" \
    "v_exp_f32 v29, v29\n\t" \
    "v_exp_f32 v30, v30\n\t" \
    "v_exp_f32 v31, v31\n\t" \
    "v_cvt_pk_bf16_f32 v94, v28, v29\n\t" \
    "v_cvt_pk_bf16_f32 v95, v30, v31\n\t" \
    "v_exp_f32 v32, v32\n\t" \
    "v_exp_f32 v33, v33\n\t" \
    "v_mfma_f32_32x32x16_bf16 v[0:15], v[76:79], v[92:95], v[0:15]\n\t" \
    "ds_read_b128 v[64:67], v96 offset:45056\n\t" \
    "v_exp_f32 v34, v34\n\t" \
    "v_exp_f32 v35, v35\n\t" \
    "v_cvt_pk_bf16_f32 v88, v32, v33\n\t" \
    "v_cvt_pk_bf16_f32 v89, v34, v35\n\t" \
    "s_waitcnt lgkmcnt(2)\n\t" \
    "v_mfma_f32_32x32x16_bf16 v[16:31], v[56:59], v[48:51], 0\n\t" \
    "ds_read_b128 v[68:71], v96 offset:46080\n\t" \
    "v_exp_f32 v36, v36\n\t" \
    "v_exp_f32 v37, v37\n\t" \
    "v_exp_f32 v38, v38\n\t" \
    "v_exp_f32 v39, v39\n\t" \
    "v_cvt_pk_bf16_f32 v90, v36, v37\n\t" \
    "v_cvt_pk_bf16_f32 v91, v38, v39\n\t" \
    "v_mfma_f32_32x32x16_bf16 v[16:31], v[60:63], v[52:55], v[16:31]\n\t" \
    "ds_read_b128 v[72:75], v96 offset:43008\n\t" \
    "v_exp_f32 v40, v40\n\t" \
    "v_exp_f32 v41, v41\n\t" \
    "v_exp_f32 v42, v42\n\t" \
    "v_exp_f32 v43, v43\n\t" \
    "v_cvt_pk_bf16_f32 v92, v40, v41\n\t" \
    "v_cvt_pk_bf16_f32 v93, v42, v43\n\t" \
    "v_mfma_f32_32x32x16_bf16 v[0:15], v[80:83], v[88:91], v[0:15]\n\t" \
    "ds_read_b128 v[76:79], v96 offset:44032\n\t" \
    "v_exp_f32 v44, v44\n\t" \
    "v_exp_f32 v45, v45\n\t" \
    "v_exp_f32 v46, v46\n\t" \
    "v_exp_f32 v47, v47\n\t" \
    "v_cvt_pk_bf16_f32 v94, v44, v45\n\t" \
    "v_cvt_pk_bf16_f32 v95, v46, v47\n\t" \
    "s_waitcnt vmcnt(4)\n\t" \
    "s_barrier\n\t" \
    "v_exp_f32 v16, v16\n\t" \
    "v_exp_f32 v17, v17\n\t" \
    "v_mfma_f32_32x32x16_bf16 v[0:15], v[84:87], v[92:95], v[0:15]\n\t" \
    "ds_read_b128 v[56:59], v97 offset:0\n\t" \
    "v_exp_f32 v18, v18\n\t" \
    "v_exp_f32 v19, v19\n\t" \
    "v_cvt_pk_bf16_f32 v88, v16, v17\n\t" \
    "v_cvt_pk_bf16_f32 v89, v18, v19\n\t" \
    "s_waitcnt lgkmcnt(2)\n\t" \
    "v_mfma_f32_32x32x16_bf16 v[32:47], v[64:67], v[48:51], 0\n\t" \
    "ds_read_b128 v[60:63], v97 offset:1024\n\t" \
    "v_exp_f32 v20, v20\n\t" \
    "v_exp_f32 v21, v21\n\t" \
    "v_exp_f32 v22, v22\n\t" \
    "v_exp_f32 v23, v23\n\t" \
    "v_cvt_pk_bf16_f32 v90, v20, v21\n\t" \
    "v_cvt_pk_bf16_f32 v91, v22, v23\n\t" \
    "v_mfma_f32_32x32x16_bf16 v[32:47], v[68:71], v[52:55], v[32:47]\n\t" \
    "ds_read_b128 v[80:83], v96 offset:47104\n\t" \
    "v_exp_f32 v24, v24\n\t" \
    "v_exp_f32 v25, v25\n\t" \
    "v_exp_f32 v26, v26\n\t" \
    "v_exp_f32 v27, v27\n\t" \
    "v_cvt_pk_bf16_f32 v92, v24, v25\n\t" \
    "v_cvt_pk_bf16_f32 v93, v26, v27\n\t" \
    "v_mfma_f32_32x32x16_bf16 v[0:15], v[72:75], v[88:91], v[0:15]\n\t" \
    "ds_read_b128 v[84:87], v96 offset:48128\n\t" \
    "v_exp_f32 v28, v28\n\t" \
    "v_exp_f32 v29, v29\n\t" \
    "v_exp_f32 v30, v30\n\t" \
    "v_exp_f32 v31, v31\n\t" \
    "v_cvt_pk_bf16_f32 v94, v28, v29\n\t" \
    "v_cvt_pk_bf16_f32 v95, v30, v31\n\t" \
    "v_exp_f32 v32, v32\n\t" \
    "v_exp_f32 v33, v33\n\t" \
    "v_mfma_f32_32x32x16_bf16 v[0:15], v[76:79], v[92:95], v[0:15]\n\t" \
    "ds_read_b128 v[64:67], v97 offset:4096\n\t" \
    "v_exp_f32 v34, v34\n\t" \
    "v_exp_f32 v35, v35\n\t" \
    "v_cvt_pk_bf16_f32 v88, v32, v33\n\t" \
    "v_cvt_pk_bf16_f32 v89, v34, v35\n\t" \
    "s_waitcnt lgkmcnt(2)\n\t" \
    "v_mfma_f32_32x32x16_bf16 v[16:31], v[56:59], v[48:51], 0\n\t" \
    "ds_read_b128 v[68:71], v97 offset:5120\n\t" \
    "v_exp_f32 v36, v36\n\t" \
    "v_exp_f32 v37, v37\n\t" \
    "v_exp_f32 v38, v38\n\t" \
    "v_exp_f32 v39, v39\n\t" \
    "v_cvt_pk_bf16_f32 v90, v36, v37\n\t" \
    "v_cvt_pk_bf16_f32 v91, v38, v39\n\t" \
    "v_mfma_f32_32x32x16_bf16 v[16:31], v[60:63], v[52:55], v[16:31]\n\t" \
    "ds_read_b128 v[72:75], v97 offset:2048\n\t" \
    "v_exp_f32 v40, v40\n\t" \
    "v_exp_f32 v41, v41\n\t" \
    "v_exp_f32 v42, v42\n\t" \
    "v_exp_f32 v43, v43\n\t" \
    "v_cvt_pk_bf16_f32 v92, v40, v41\n\t" \
    "v_cvt_pk_bf16_f32 v93, v42, v43\n\t" \
    "v_mfma_f32_32x32x16_bf16 v[0:15], v[80:83], v[88:91], v[0:15]\n\t" \
    "ds_read_b128 v[76:79], v97 offset:3072\n\t" \
    "v_exp_f32 v44, v44\n\t" \
    "v_exp_f32 v45, v45\n\t" \
    "v_exp_f32 v46, v46\n\t" \
    "v_exp_f32 v47, v47\n\t" \
    "v_cvt_pk_bf16_f32 v94, v44, v45\n\t" \
    "v_cvt_pk_bf16_f32 v95, v46, v47\n\t" \
    "v_exp_f32 v16, v16\n\t" \
    "v_exp_f32 v17, v17\n\t" \
    "v_mfma_f32_32x32x16_bf16 v[0:15], v[84:87], v[92:95], v[0:15]\n\t" \
    "ds_read_b128 v[56:59], v97 offset:8192\n\t" \
    "v_exp_f32 v18, v18\n\t" \
    "v_exp_f32 v19, v19\n\t" \
    "v_cvt_pk_bf16_f32 v88, v16, v17\n\t" \
    "v_cvt_pk_bf16_f32 v89, v18, v19\n\t" \
    "s_waitcnt lgkmcnt(2)\n\t" \
    "v_mfma_f32_32x32x16_bf16 v[32:47], v[64:67], v[48:51], 0\n\t" \
    "ds_read_b128 v[60:63], v97 offset:9216\n\t" \
    "v_exp_f32 v20, v20\n\t" \
    "v_exp_f32 v21, v21\n\t" \
    "v_exp_f32 v22, v22\n\t" \
    "v_exp_f32 v23, v23\n\t" \
    "v_cvt_pk_bf16_f32 v90, v20, v21\n\t" \
    "v_cvt_pk_bf16_f32 v91, v22, v23\n\t" \
    "v_mfma_f32_32x32x16_bf16 v[32:47], v[68:71], v[52:55], v[32:47]\n\t" \
    "ds_read_b128 v[80:83], v97 offset:6144\n\t" \
    "v_exp_f32 v24, v24\n\t" \
    "v_exp_f32 v25, v25\n\t" \
    "v_exp_f32 v26, v26\n\t" \
    "v_exp_f32 v27, v27\n\t" \
    "v_cvt_pk_bf16_f32 v92, v24, v25\n\t" \
    "v_cvt_pk_bf16_f32 v93, v26, v27\n\t" \
    "v_mfma_f32_32x32x16_bf16 v[0:15], v[72:75], v[88:91], v[0:15]\n\t" \
    "ds_read_b128 v[84:87], v97 offset:7168\n\t" \
    "v_exp_f32 v28, v28\n\t" \
    "v_exp_f32 v29, v29\n\t" \
    "v_exp_f32 v30, v30\n\t" \
    "v_exp_f32 v31, v31\n\t" \
    "v_cvt_pk_bf16_f32 v94, v28, v29\n\t" \
    "v_cvt_pk_bf16_f32 v95, v30, v31\n\t" \
    "v_exp_f32 v32, v32\n\t" \
    "v_exp_f32 v33, v33\n\t" \
    "v_mfma_f32_32x32x16_bf16 v[0:15], v[76:79], v[92:95], v[0:15]\n\t" \
    "ds_read_b128 v[64:67], v97 offset:12288\n\t" \
    "v_exp_f32 v34, v34\n\t" \
    "v_exp_f32 v35, v35\n\t" \
    "v_cvt_pk_bf16_f32 v88, v32, v33\n\t" \
    "v_cvt_pk_bf16_f32 v89, v34, v35\n\t" \
    "s_waitcnt lgkmcnt(2)\n\t" \
    "v_mfma_f32_32x32x16_bf16 v[16:31], v[56:59], v[48:51], 0\n\t" \
    "ds_read_b128 v[68:71], v97 offset:13312\n\t" \
    "v_exp_f32 v36, v36\n\t" \
    "v_exp_f32 v37, v37\n\t" \
    "v_exp_f32 v38, v38\n\t" \
    "v_exp_f32 v39, v39\n\t" \
    "v_cvt_pk_bf16_f32 v90, v36, v37\n\t" \
    "v_cvt_pk_bf16_f32 v91, v38, v39\n\t" \
    "v_mfma_f32_32x32x16_bf16 v[16:31], v[60:63], v[52:55], v[16:31]\n\t" \
    "ds_read_b128 v[72:75], v97 offset:10240\n\t" \
    "v_exp_f32 v40, v40\n\t" \
    "v_exp_f32 v41, v41\n\t" \
    "v_exp_f32 v42, v42\n\t" \
    "v_exp_f32 v43, v43\n\t" \
    "v_cvt_pk_bf16_f32 v92, v40, v41\n\t" \
    "v_cvt_pk_bf16_f32 v93, v42, v43\n\t" \
    "v_mfma_f32_32x32x16_bf16 v[0:15], v[80:83], v[88:91], v[0:15]\n\t" \
    "ds_read_b128 v[76:79], v97 offset:11264\n\t" \
    "v_exp_f32 v44, v44\n\t" \
    "v_exp_f32 v45, v45\n\t" \
    "v_exp_f32 v46, v46\n\t" \
    "v_exp_f32 v47, v47\n\t" \
    "v_cvt_pk_bf16_f32 v94, v44, v45\n\t" \
    "v_cvt_pk_bf16_f32 v95, v46, v47\n\t" \
    "s_waitcnt vmcnt(0)\n\t" \
    "s_barrier\n\t" \
    "v_exp_f32 v16, v16\n\t" \
    "v_exp_f32 v17, v17\n\t" \
    "v_mfma_f32_32x32x16_bf16 v[0:15], v[84:87], v[92:95], v[0:15]\n\t" \
    "ds_read_b128 v[56:59], v97 offset:32768\n\t" \
    "v_exp_f32 v18, v18\n\t" \
    "v_exp_f32 v19, v19\n\t" \
    "v_cvt_pk_bf16_f32 v88, v16, v17\n\t" \
    "v_cvt_pk_bf16_f32 v89, v18, v19\n\t" \
    "s_waitcnt lgkmcnt(2)\n\t" \
    "v_mfma_f32_32x32x16_bf16 v[32:47], v[64:67], v[48:51], 0\n\t" \
    "ds_read_b128 v[60:63], v97 offset:33792\n\t" \
    "v_exp_f32 v20, v20\n\t" \
    "v_exp_f32 v21, v21\n\t" \
    "v_exp_f32 v22, v22\n\t" \
    "v_exp_f32 v23, v23\n\t" \
    "v_cvt_pk_bf16_f32 v90, v20, v21\n\t" \
    "v_cvt_pk_bf16_f32 v91, v22, v23\n\t" \
    "v_mfma_f32_32x32x16_bf16 v[32:47], v[68:71], v[52:55], v[32:47]\n\t" \
    "ds_read_b128 v[80:83], v97 offset:14336\n\t" \
    "v_exp_f32 v24, v24\n\t" \
    "v_exp_f32 v25, v25\n\t" \
    "v_exp_f32 v26, v26\n\t" \
    "v_exp_f32 v27, v27\n\t" \
    "v_cvt_pk_bf16_f32 v92, v24, v25\n\t" \
    "v_cvt_pk_bf16_f32 v93, v26, v27\n\t" \
    "v_mfma_f32_32x32x16_bf16 v[0:15], v[72:75], v[88:91], v[0:15]\n\t" \
    "ds_read_b128 v[84:87], v97 offset:15360\n\t" \
    "v_exp_f32 v28, v28\n\t" \
    "v_exp_f32 v29, v29\n\t" \
    "v_exp_f32 v30, v30\n\t" \
    "v_exp_f32 v31, v31\n\t" \
    "v_cvt_pk_bf16_f32 v94, v28, v29\n\t" \
    "v_cvt_pk_bf16_f32 v95, v30, v31\n\t" \
    "v_exp_f32 v32, v32\n\t" \
    "v_exp_f32 v33, v33\n\t" \
    "v_mfma_f32_32x32x16_bf16 v[0:15], v[76:79], v[92:95], v[0:15]\n\t" \
    "ds_read_b128 v[64:67], v97 offset:36864\n\t" \
    "v_exp_f32 v34, v34\n\t" \
    "v_exp_f32 v35, v35\n\t" \
    "v_cvt_pk_bf16_f32 v88, v32, v33\n\t" \
    "v_cvt_pk_bf16_f32 v89, v34, v35\n\t" \
    "s_waitcnt lgkmcnt(2)\n\t" \
    "v_mfma_f32_32x32x16_bf16 v[16:31], v[56:59], v[48:51], 0\n\t" \
    "ds_read_b128 v[68:71], v97 offset:37888\n\t" \
    "v_exp_f32 v36, v36\n\t" \
    "v_exp_f32 v37, v37\n\t" \
    "v_exp_f32 v38, v38\n\t" \
    "v_exp_f32 v39, v39\n\t" \
    "v_cvt_pk_bf16_f32 v90, v36, v37\n\t" \
    "v_cvt_pk_bf16_f32 v91, v38, v39\n\t" \
    "v_mfma_f32_32x32x16_bf16 v[16:31], v[60:63], v[52:55], v[16:31]\n\t" \
    "ds_read_b128 v[72:75], v97 offset:34816\n\t" \
    "v_exp_f32 v40, v40\n\t" \
    "v_exp_f32 v41, v41\n\t" \
    "v_exp_f32 v42, v42\n\t" \
    "v_exp_f32 v43, v43\n\t" \
    "v_cvt_pk_bf16_f32 v92, v40, v41\n\t" \
    "v_cvt_pk_bf16_f32 v93, v42, v43\n\t" \
    "v_mfma_f32_32x32x16_bf16 v[0:15], v[80:83], v[88:91], v[0:15]\n\t" \
    "ds_read_b128 v[76:79], v97 offset:35840\n\t" \
    "v_exp_f32 v44, v44\n\t" \
    "v_exp_f32 v45, v45\n\t" \
    "v_exp_f32 v46, v46\n\t" \
    "v_exp_f32 v47, v47\n\t" \
    "v_cvt_pk_bf16_f32 v94, v44, v45\n\t" \
    "v_cvt_pk_bf16_f32 v95, v46, v47\n\t" \
    "v_exp_f32 v16, v16\n\t" \
    "v_exp_f32 v17, v17\n\t" \
    "v_mfma_f32_32x32x16_bf16 v[0:15], v[84:87], v[92:95], v[0:15]\n\t" \
    "ds_read_b128 v[56:59], v97 offset:40960\n\t" \
    "v_exp_f32 v18, v18\n\t" \
    "v_exp_f32 v19, v19\n\t" \
    "v_cvt_pk_bf16_f32 v88, v16, v17\n\t" \
    "v_cvt_pk_bf16_f32 v89, v18, v19\n\t" \
    "s_waitcnt lgkmcnt(2)\n\t" \
    "v_mfma_f32_32x32x16_bf16 v[32:47], v[64:67], v[48:51], 0\n\t" \
    "ds_read_b128 v[60:63], v97 offset:41984\n\t" \
    "v_exp_f32 v20, v20\n\t" \
    "v_exp_f32 v21, v21\n\t" \
    "v_exp_f32 v22, v22\n\t" \
    "v_exp_f32 v23, v23\n\t" \
    "v_cvt_pk_bf16_f32 v90, v20, v21\n\t" \
    "v_cvt_pk_bf16_f32 v91, v22, v23\n\t" \
    "v_mfma_f32_32x32x16_bf16 v[32:47], v[68:71], v[52:55], v[32:47]\n\t" \
    "ds_read_b128 v[80:83], v97 offset:38912\n\t" \
    "v_exp_f32 v24, v24\n\t" \
    "v_exp_f32 v25, v25\n\t" \
    "v_exp_f32 v26, v26\n\t" \
    "v_exp_f32 v27, v27\n\t" \
    "v_cvt_pk_bf16_f32 v92, v24, v25\n\t" \
    "v_cvt_pk_bf16_f32 v93, v26, v27\n\t" \
    "v_mfma_f32_32x32x16_bf16 v[0:15], v[72:75], v[88:91], v[0:15]\n\t" \
    "ds_read_b128 v[84:87], v97 offset:39936\n\t" \
    "v_exp_f32 v28, v28\n\t" \
    "v_exp_f32 v29, v29\n\t" \
    "v_exp_f32 v30, v30\n\t" \
    "v_exp_f32 v31, v31\n\t" \
    "v_cvt_pk_bf16_f32 v94, v28, v29\n\t" \
    "v_cvt_pk_bf16_f32 v95, v30, v31\n\t" \
    "v_exp_f32 v32, v32\n\t" \
    "v_exp_f32 v33, v33\n\t" \
    "v_mfma_f32_32x32x16_bf16 v[0:15], v[76:79], v[92:95], v[0:15]\n\t" \
    "ds_read_b128 v[64:67], v97 offset:45056\n\t" \
    "v_exp_f32 v34, v34\n\t" \
    "v_exp_f32 v35, v35\n\t" \
    "v_cvt_pk_bf16_f32 v88, v32, v33\n\t" \
    "v_cvt_pk_bf16_f32 v89, v34, v35\n\t" \
    "s_waitcnt lgkmcnt(2)\n\t" \
    "v_mfma_f32_32x32x16_bf16 v[16:31], v[56:59], v[48:51], 0\n\t" \
    "ds_read_b128 v[68:71], v97 offset:46080\n\t" \
    "v_exp_f32 v36, v36\n\t" \
    "v_exp_f32 v37, v37\n\t" \
    "v_exp_f32 v38, v38\n\t" \
    "v_exp_f32 v39, v39\n\t" \
    "v_cvt_pk_bf16_f32 v90, v36, v37\n\t" \
    "v_cvt_pk_bf16_f32 v91, v38, v39\n\t" \
    "v_mfma_f32_32x32x16_bf16 v[16:31], v[60:63], v[52:55], v[16:31]\n\t" \
    "ds_read_b128 v[72:75], v97 offset:43008\n\t" \
    "v_exp_f32 v40, v40\n\t" \
    "v_exp_f32 v41, v41\n\t" \
    "v_exp_f32 v42, v42\n\t" \
    "v_exp_f32 v43, v43\n\t" \
    "v_cvt_pk_bf16_f32 v92, v40, v41\n\t" \
    "v_cvt_pk_bf16_f32 v93, v42, v43\n\t" \
    "v_mfma_f32_32x32x16_bf16 v[0:15], v[80:83], v[88:91], v[0:15]\n\t" \
    "ds_read_b128 v[76:79], v97 offset:44032\n\t" \
    "v_exp_f32 v44, v44\n\t" \
    "v_exp_f32 v45, v45\n\t" \
    "v_exp_f32 v46, v46\n\t" \
    "v_exp_f32 v47, v47\n\t" \
    "v_cvt_pk_bf16_f32 v94, v44, v45\n\t" \
    "v_cvt_pk_bf16_f32 v95, v46, v47\n\t" \
    "v_exp_f32 v16, v16\n\t" \
    "v_exp_f32 v17, v17\n\t" \
    "v_mfma_f32_32x32x16_bf16 v[0:15], v[84:87], v[92:95], v[0:15]\n\t" \
    "v_exp_f32 v18, v18\n\t" \
    "v_exp_f32 v19, v19\n\t" \
    "v_cvt_pk_bf16_f32 v88, v16, v17\n\t" \
    "v_cvt_pk_bf16_f32 v89, v18, v19\n\t" \
    "s_waitcnt lgkmcnt(1)\n\t" \
    "v_mfma_f32_32x32x16_bf16 v[32:47], v[64:67], v[48:51], 0\n\t" \
    "v_exp_f32 v20, v20\n\t" \
    "v_exp_f32 v21, v21\n\t" \
    "v_exp_f32 v22, v22\n\t" \
    "v_exp_f32 v23, v23\n\t" \
    "v_cvt_pk_bf16_f32 v90, v20, v21\n\t" \
    "v_cvt_pk_bf16_f32 v91, v22, v23\n\t" \
    "v_mfma_f32_32x32x16_bf16 v[32:47], v[68:71], v[52:55], v[32:47]\n\t" \
    "ds_read_b128 v[80:83], v97 offset:47104\n\t" \
    "v_exp_f32 v24, v24\n\t" \
    "v_exp_f32 v25, v25\n\t" \
    "v_exp_f32 v26, v26\n\t" \
    "v_exp_f32 v27, v27\n\t" \
    "v_cvt_pk_bf16_f32 v92, v24, v25\n\t" \
    "v_cvt_pk_bf16_f32 v93, v26, v27\n\t" \
    "v_mfma_f32_32x32x16_bf16 v[0:15], v[72:75], v[88:91], v[0:15]\n\t" \
    "ds_read_b128 v[84:87], v97 offset:48128\n\t" \
    "v_exp_f32 v28, v28\n\t" \
    "v_exp_f32 v29, v29\n\t" \
    "v_exp_f32 v30, v30\n\t" \
    "v_exp_f32 v31, v31\n\t" \
    "v_cvt_pk_bf16_f32 v94, v28, v29\n\t" \
    "v_cvt_pk_bf16_f32 v95, v30, v31\n\t" \
    "v_exp_f32 v32, v32\n\t" \
    "v_exp_f32 v33, v33\n\t" \
    "v_mfma_f32_32x32x16_bf16 v[0:15], v[76:79], v[92:95], v[0:15]\n\t" \
    "v_exp_f32 v34, v34\n\t" \
    "v_exp_f32 v35, v35\n\t" \
    "v_cvt_pk_bf16_f32 v88, v32, v33\n\t" \
    "v_cvt_pk_bf16_f32 v89, v34, v35\n\t" \
    "s_waitcnt lgkmcnt(1)\n\t" \
    "v_exp_f32 v36, v36\n\t" \
    "v_exp_f32 v37, v37\n\t" \
    "v_exp_f32 v38, v38\n\t" \
    "v_exp_f32 v39, v39\n\t" \
    "v_cvt_pk_bf16_f32 v90, v36, v37\n\t" \
    "v_cvt_pk_bf16_f32 v91, v38, v39\n\t" \
    "v_exp_f32 v40, v40\n\t" \
    "v_exp_f32 v41, v41\n\t" \
    "v_exp_f32 v42, v42\n\t" \
    "v_exp_f32 v43, v43\n\t" \
    "v_cvt_pk_bf16_f32 v92, v40, v41\n\t" \
    "v_cvt_pk_bf16_f32 v93, v42, v43\n\t" \
    "v_mfma_f32_32x32x16_bf16 v[0:15], v[80:83], v[88:91], v[0:15]\n\t" \
    "v_exp_f32 v44, v44\n\t" \
    "v_exp_f32 v45, v45\n\t" \
    "v_exp_f32 v46, v46\n\t" \
    "v_exp_f32 v47, v47\n\t" \
    "v_cvt_pk_bf16_f32 v94, v44, v45\n\t" \
    "v_cvt_pk_bf16_f32 v95, v46, v47\n\t" \
    "s_waitcnt lgkmcnt(0)\n\t" \
    "s_nop 0\n\t" \
    "v_mfma_f32_32x32x16_bf16 v[0:15], v[84:87], v[92:95], v[0:15]\n\t" \
    "s_mov_b32 m0, s43\n\t" \
    "s_nop 7\n\t" \
    "s_nop 7\n\t"
#define ATTN_FAST_CLOBBERS "v16", "v17", "v18", "v19", "v20", "v21", "v22", "v23", "v24", "v25", "v26", "v27", "v28", "v29", "v30", "v31", "v32", "v33", "v34", "v35", "v36", "v37", "v38", "v39", "v40", "v41", "v42", "v43", "v44", "v45", "v46", "v47", "v56", "v57", "v58", "v59", "v60", "v61", "v62", "v63", "v64", "v65", "v66", "v67", "v68", "v69", "v70", "v71", "v72", "v73", "v74", "v75", "v76", "v77", "v78", "v79", "v80", "v81", "v82", "v83", "v84", "v85", "v86", "v87", "v88", "v89", "v90", "v91", "v92", "v93", "v94", "v95", "s43", "scc", "memory"

constexpr int A_SLOT = 28672;
constexpr int A_XS = 3 * A_SLOT;
constexpr int A_XROW = 464;
constexpr int L_Q = A_XS + 128 * A_XROW;
constexpr int L_CTL = L_Q + 8192;
constexpr int L_DUMMY = L_CTL + 256;
constexpr int L_TOTAL = L_DUMMY + 256;
constexpr int B_RED = 131072;
constexpr int C_RROW = 272;

DI float gelu_tanh(float h) {
    const float c0 = -2.0f * 0.7978845608028654f * 1.4426950408889634f, c1 = c0 * 0.044715f;
    const float z = h * __builtin_fmaf(h * h, c1, c0);
    const float e = __builtin_amdgcn_exp2f(z);
    return h * __builtin_amdgcn_rcpf(1.0f + e);
}

DI void phaseA(const char* __restrict__ x, const char* __restrict__ wqkv, const char* __restrict__ w1, const char* __restrict__ b1,
               char* __restrict__ ws, char* lds_generic, int Rb, int c, u64& tag) {
    LAS char* lds = (LAS char*)lds_generic;
    const unsigned lds0 = (unsigned)(uintptr_t)lds_generic;
    const int tid = threadIdx.x, l = tid & 63, w = rfl(tid >> 6) & 7, r = l & 31, h = l >> 5;
    const int tt = w & 3, half = w >> 2;
    STAMP(ws, 0);
    u32x2 e0; { const unsigned z = 0; asm volatile("global_load_dwordx2 %0, %1, %2 sc1" : "=v"(e0) : "v"(z), "s"(uptr(ws + WS_EPOCH)) : "memory"); }
    u32x4 xr[2][7];
    {
        const char* xb = x + (size_t)(Rb * 128 + 16 * w) * 896;
        const unsigned vo = (l >> 3) * 896 + (l & 7) * 16;
#pragma unroll
        for (int g = 0; g < 2; ++g)
#pragma unroll
            for (int i = 0; i < 7; ++i) xr[g][i] = gload16(xb + g * 8 * 896 + i * 128, vo);
    }
    {
        const unsigned vo = (l >> 2) * 2688 + ((l & 3) < 3 ? (l & 3) * 16 : 40);
#pragma unroll
        for (int i = 0; i < 11; ++i) {
            const int n = w + 8 * i;
            if (n < 84) { const int m = n / 28, kb = n % 28;
                glds16(wqkv + (size_t)kb * 16 * 2688 + m * 896 + c * 56, vo, lds0 + m * A_SLOT + kb * 1024); }
        }
    }
    asm volatile("s_waitcnt vmcnt(10)" : "+v"(e0), "+v"(xr[0][0]), "+v"(xr[0][1]), "+v"(xr[0][2]), "+v"(xr[0][3]), "+v"(xr[0][4]), "+v"(xr[0][5]), "+v"(xr[0][6]),
                 "+v"(xr[1][0]), "+v"(xr[1][1]), "+v"(xr[1][2]), "+v"(xr[1][3]), "+v"(xr[1][4]), "+v"(xr[1][5]), "+v"(xr[1][6]) :: "memory");
    tag = (((u64)rfl(e0[1]) << 32) | rfl(e0[0])) + 0x9E3779B97F4A7C15ull;
    STAMP(ws, 1);
#pragma unroll
    for (int g = 0; g < 2; ++g) {
        float sm = 0.f, sq = 0.f; unsigned ones_ = 0x3f803f80u; asm volatile("" : "+v"(ones_)); const bf16x2 one = asbf2(ones_);
#pragma unroll
        for (int i = 0; i < 7; ++i)
#pragma unroll
            for (int e = 0; e < 4; ++e) { const bf16x2 v = asbf2(xr[g][i][e]); sm = __builtin_amdgcn_fdot2_f32_bf16(v, one, sm, false); sq = __builtin_amdgcn_fdot2_f32_bf16(v, v, sq, false); }
        sm += dppf<0xB1>(sm); sq += dppf<0xB1>(sq); sm += dppf<0x4E>(sm); sq += dppf<0x4E>(sq); sm += dppf<0x141>(sm); sq += dppf<0x141>(sq);
        const float mean = sm * (1.0f / 448.0f); const float var = __builtin_fmaxf(sq * (1.0f / 448.0f) - mean * mean, 0.f);
        const float rstd = __builtin_amdgcn_rsqf(var + 1e-5f), nb = -mean * rstd;
#pragma unroll
        for (int i = 0; i < 7; ++i)
#pragma unroll
            for (int e = 0; e < 4; ++e) { const unsigned v = xr[g][i][e]; xr[g][i][e] = cvtpk(__builtin_fmaf(bflo(v), rstd, nb), __builtin_fmaf(bfhi(v), rstd, nb)); }
    }
    STAMP(ws, 2);
    bf16x8 xf[28];
    {
        LAS char* xs = lds + A_XS; const int sub = l & 7;
        const int wrow = 16 * w + (l >> 3);
        const LAS char* rd = xs + (32 * tt + r) * A_XROW + h * 16;
#pragma unroll
        for (int g = 0; g < 2; ++g) {
#pragma unroll
            for (int i = 0; i < 3; ++i) *(LAS u32x4*)(xs + (wrow + 8 * g) * A_XROW + (sub + 8 * i) * 16) = xr[g][i];
            if (sub < 4) *(LAS u32x4*)(xs + (wrow + 8 * g) * A_XROW + (sub + 24) * 16) = xr[g][3];
        }
        WAIT_LGKM0(); BARRIER();
        STAMP(ws, 15);
#pragma unroll
        for (int s = 0; s < 14; ++s) xf[s] = ldsfrag(rd + s * 32);
        WAIT_LGKM0(); BARRIER();
        STAMP(ws, 16);
#pragma unroll
        for (int g = 0; g < 2; ++g) {
            if (sub >= 4) *(LAS u32x4*)(xs + (wrow + 8 * g) * A_XROW + (sub - 4) * 16) = xr[g][3];
#pragma unroll
            for (int i = 4; i < 7; ++i) *(LAS u32x4*)(xs + (wrow + 8 * g) * A_XROW + (sub + 8 * i - 28) * 16) = xr[g][i];
        }
        WAIT_LGKM0(); BARRIER();
#pragma unroll
        for (int s = 0; s < 14; ++s) xf[14 + s] = ldsfrag(rd + s * 32);
        WAIT_LGKM0();
        STAMP(ws, 17);
        WAIT_VM(0);
        STAMP(ws, 18);
        BARRIER();
    }
    STAMP(ws, 3);
    const bool ffn = c < 14;
    const unsigned vo1 = (l >> 2) * 3584 + (l & 3) * 16;
    if (ffn) {
#pragma unroll
        for (int i = 0; i < 7; ++i) { const int n = w + 8 * i, t = n / 28, kb = n % 28;
            glds16(w1 + (size_t)kb * 16 * 3584 + (4 * c + t) * 64, vo1, lds0 + A_XS + t * A_SLOT + kb * 1024); }
    }
    const int troff = (8 * h + ((l & 15) >> 2)) * 64 + ((l >> 4) & 1) * 32 + (l & 3) * 8;
    const int tglob = Rb * 4 + tt;
    const unsigned vl = l * 16;
    if (half == 0) {
        f32x16 aq = {}, ak = {};
        const LAS char* pq = lds + troff; const LAS char* pk = lds + A_SLOT + troff;
#pragma unroll
        for (int s = 0; s < 28; ++s) {
            aq = MFMA(trfrag(pq + s * 1024, pq + s * 1024 + 256), xf[s], aq);
            ak = MFMA(trfrag(pk + s * 1024, pk + s * 1024 + 256), xf[s], ak);
        }
        const float qs = 0.18898223650461361f * 1.4426950408889634f;
        float nk = 0.f;
#pragma unroll
        for (int i = 0; i < 16; ++i) { const bool pad = (i >= 12) && (h == 0); aq[i] = pad ? 0.f : aq[i] * qs; const float kv = pad ? 0.f : ak[i]; nk = __builtin_fmaf(kv, kv, nk); ak[i] = kv; }
        if (h == 0) ak[12] = 1.0f;
        nk = row32_max(xhalf_sum(nk));
        char* kd = ws + WS_KV + (size_t)(c * 64 + tglob) * 4096;
        gstore16_sc1(kd, vl, pack8(ak, 0)); gstore16_sc1(kd + 1024, vl, pack8(ak, 1));
        if (l == 0) __hip_atomic_store((__attribute__((address_space(1))) unsigned*)(ws + WS_KN + (c * 64 + tglob) * 4), __float_as_uint(nk), __ATOMIC_RELAXED, __HIP_MEMORY_SCOPE_AGENT);
        *(LAS u32x4*)(lds + L_Q + tt * 2048 + vl) = pack8(aq, 0); *(LAS u32x4*)(lds + L_Q + tt * 2048 + 1024 + vl) = pack8(aq, 1);
    } else {
        f32x16 av = {};
        const LAS char* pv = lds + 2 * A_SLOT + troff;
#pragma unroll
        for (int s = 0; s < 28; ++s) av = MFMA(xf[s], trfrag(pv + s * 1024, pv + s * 1024 + 256), av);
        const bool pad = (r >= 24) && (r < 28); const float pv1 = (r == 24) ? 1.0f : 0.f;
#pragma unroll
        for (int i = 0; i < 16; ++i) av[i] = pad ? pv1 : av[i];
        char* vd = ws + WS_KV + (size_t)(c * 64 + tglob) * 4096 + 2048;
        gstore16_sc1(vd, vl, pack8(av, 0)); gstore16_sc1(vd + 1024, vl, pack8(av, 1));
    }
    STAMP(ws, 4);
    WAIT_LGKM0(); WAIT_VM(0); BARRIER();
    if (tid == 0) flag_store(ws, WS_FKV + c * 256 + Rb * 8, tag);
    STAMP(ws, 5);
    if (!ffn) return;
#pragma unroll
    for (int i = 0; i < 7; ++i) { const int n = w + 8 * i, t = n / 28, kb = n % 28;
        glds16(w1 + (size_t)kb * 16 * 3584 + (4 * c + 2 + t) * 64, vo1, lds0 + t * A_SLOT + kb * 1024); }
#pragma unroll
    for (int u = 0; u < 2; ++u) {
        const int ft = 4 * c + 2 * u + half;
        u32x2 bb[4];
#pragma unroll
        for (int g = 0; g < 4; ++g) bb[g] = *(const u32x2*)(b1 + (ft * 32 + 8 * g + 4 * h) * 2);
        if (u == 1) { WAIT_VM(0); BARRIER(); }
        f32x16 a = {};
        const LAS char* pw = lds + (u == 0 ? A_XS : 0) + half * A_SLOT + troff;
#pragma unroll
        for (int s = 0; s < 28; ++s) a = MFMA(trfrag(pw + s * 1024, pw + s * 1024 + 256), xf[s], a);
#pragma unroll
        for (int g = 0; g < 4; ++g) {
            a[4 * g + 0] = gelu_tanh(a[4 * g + 0] + bflo(bb[g][0])); a[4 * g + 1] = gelu_tanh(a[4 * g + 1] + bfhi(bb[g][0]));
            a[4 * g + 2] = gelu_tanh(a[4 * g + 2] + bflo(bb[g][1])); a[4 * g + 3] = gelu_tanh(a[4 * g + 3] + bfhi(bb[g][1]));
        }
        char* hd = ws + WS_H + ((size_t)tglob * 56 + ft) * 2048;
        gstore16_sc1(hd, vl, pack8(a, 0)); gstore16_sc1(hd + 1024, vl, pack8(a, 1));
        STAMP(ws, 6 + u);
    }
    WAIT_VM(0); BARRIER();
    if (tid == 0) flag_store(ws, WS_FH + Rb * 256 + c * 8, tag);
}

#define EX2(v) __builtin_amdgcn_exp2f(v)
DI void attn_safe(f32x16& o, const bf16x8 q0, const bf16x8 q1, const char* src, unsigned dst, unsigned vo, const LAS char* rb, int l) {
    float m = -1e30f;
    const f32x16 zero = {};
    WAIT_VM(0); BARRIER();
    for (int i = 0; i < 8; ++i) {
        const LAS char* sb = rb + (i & 3) * 32768;
#pragma unroll 1
        for (int t = 0; t < 4; ++t) {
            f32x16 sc = MFMA(ldsfrag(sb + t * 4096), q0, zero); sc = MFMA(ldsfrag(sb + t * 4096 + 1024), q1, sc);
            float mx = sc[0];
#pragma unroll
            for (int e = 1; e < 16; ++e) mx = __builtin_fmaxf(mx, sc[e]);
            mx = xhalf_max(mx);
            const float mn = __builtin_fmaxf(m, mx), al = EX2(m - mn); m = mn;
#pragma unroll
            for (int e = 0; e < 16; ++e) { o[e] *= al; sc[e] = EX2(sc[e] - mn); }
            o = MFMA(ldsfrag(sb + t * 4096 + 2048), asbf8(pack8(sc, 0)), o); o = MFMA(ldsfrag(sb + t * 4096 + 3072), asbf8(pack8(sc, 1)), o);
        }
        WAIT_LGKM0(); WAIT_VM(0); BARRIER();
        if (i + 4 < 8) {
#pragma unroll
            for (int e = 0; e < 4; ++e) glds16(src + (size_t)(i + 4) * 16384 + e * 1024, vo, dst + (i & 3) * 32768 + e * 1024);
        }
    }
    o[13] = (l < 32) ? m : o[13];
}

DI void phaseB(char* __restrict__ ws, char* lds_generic, int hd, int qb, u64 tag) {
    LAS char* lds = (LAS char*)lds_generic;
    const unsigned lds0 = (unsigned)(uintptr_t)lds_generic;
    const int tid = threadIdx.x, l = tid & 63, w = rfl(tid >> 6) & 7, h = l >> 5;
    const int qt = w & 3, kh = w >> 2, tglob = qb * 4 + qt;
    STAMP(ws, 10);
    if (w == 0) {
        flag_wait(ws, WS_FKV + hd * 256, 16, tag, l);
        __builtin_amdgcn_fence(__ATOMIC_ACQUIRE, "agent");
        WAIT_VM(0);
    }
    if (tid == 64) *(LAS unsigned*)(lds + L_CTL) = 0u;
    WAIT_LGKM0(); BARRIER();
    STAMP(ws, 11);
    const unsigned vo = l * 16;
    const char* src = ws + WS_KV + (size_t)hd * 262144 + (size_t)kh * 131072 + qt * 4096;
    const unsigned dst = lds0 + kh * 16384 + qt * 4096;
    float kn = *(const float*)(ws + WS_KN + (hd * 64 + l) * 4);
    u32x4 q0 = *(const LAS u32x4*)(lds + L_Q + qt * 2048 + vo), q1 = *(const LAS u32x4*)(lds + L_Q + qt * 2048 + 1024 + vo);
    kn = xhalf_max(row32_max(kn));
    float qn = 0.f;
#pragma unroll
    for (int e = 0; e < 4; ++e) { qn = __builtin_amdgcn_fdot2_f32_bf16(asbf2(q0[e]), asbf2(q0[e]), qn, false); qn = __builtin_amdgcn_fdot2_f32_bf16(asbf2(q1[e]), asbf2(q1[e]), qn, false); }
    qn = xhalf_sum(qn);
    const float bound = __builtin_sqrtf(qn * kn) * 1.02f + 1e-3f;
    if (__any(!(bound <= 48.0f)) && l == 0) *(LAS unsigned*)(lds + L_CTL) = 1u;
#pragma unroll
    for (int i = 0; i < 4; ++i)
#pragma unroll
        for (int e = 0; e < 4; ++e) glds16(src + (size_t)i * 16384 + e * 1024, vo, dst + i * 32768 + e * 1024);
    WAIT_LGKM0(); BARRIER();
    const bool safe = rfl(*(const LAS unsigned*)(lds + L_CTL)) != 0u;
    f32x16 o;
    STAMP(ws, 12); STAMPC(ws, 28);
    if (!safe) {
        if (h == 0) q1[2] = cvtpk(-bound, 0.f);
        const unsigned ad0 = lds0 + kh * 16384 + vo, ad1 = ad0 + 65536;
        const char* s3 = (const char*)uptr(src + 3 * 16384);
        const unsigned dsts = rfl(dst);
        asm volatile(ATTN_FAST_ASM
                     : "={v[0:15]}"(o), "+{s[40:41]}"(s3)
                     : "{v[48:51]}"(q0), "{v[52:55]}"(q1), "{v96}"(ad0), "{v97}"(ad1), "{v98}"(vo), "{s42}"(dsts)
                     : ATTN_FAST_CLOBBERS);
    } else {
        o = (f32x16){};
        attn_safe(o, asbf8(q0), asbf8(q1), src, dst, vo, lds + kh * 16384 + vo, l);
    }
    STAMP(ws, 13); STAMPC(ws, 29);
    LAS char* red = lds + B_RED + qt * 4096 + vo;
    if (kh == 1) {
#pragma unroll
        for (int g = 0; g < 4; ++g) *(LAS f32x4*)(red + g * 1024) = (f32x4){o[4 * g], o[4 * g + 1], o[4 * g + 2], o[4 * g + 3]};
    }
    WAIT_LGKM0(); BARRIER();
    if (kh == 0) {
        f32x16 p;
#pragma unroll
        for (int g = 0; g < 4; ++g) { const f32x4 t = *(const LAS f32x4*)(red + g * 1024); p[4 * g] = t[0]; p[4 * g + 1] = t[1]; p[4 * g + 2] = t[2]; p[4 * g + 3] = t[3]; }
        if (safe) {
            const float ma = swap32_lo(o[13]), mb = swap32_lo(p[13]);
            const float mn = __builtin_fmaxf(ma, mb), fa = EX2(ma - mn), fb = EX2(mb - mn);
#pragma unroll
            for (int e = 0; e < 16; ++e) o[e] = o[e] * fa + p[e] * fb;
        } else {
#pragma unroll
            for (int e = 0; e < 16; ++e) o[e] += p[e];
        }
        const float inv = 1.0f / swap32_lo(o[12]);
#pragma unroll
        for (int e = 0; e < 16; ++e) o[e] = (e >= 12 && h == 0) ? 0.f : o[e] * inv;
        char* od = ws + WS_O + ((size_t)tglob * 16 + hd) * 2048;
        gstore16_sc1(od, vo, pack8(o, 0)); gstore16_sc1(od + 1024, vo, pack8(o, 1));
    }
    WAIT_VM(0); BARRIER();
    if (tid == 0) flag_store(ws, WS_FO + qb * 256 + hd * 8, tag);
    STAMP(ws, 14);
}

DI void phaseC(const char* __restrict__ x, const char* __restrict__ wo, const char* __restrict__ w2, const char* __restrict__ b2,
               char* __restrict__ ws, char* __restrict__ out, char* lds_generic, int rb, int ng, u64 tag, bool bump) {
    LAS char* lds = (LAS char*)lds_generic;
    const unsigned lds0 = (unsigned)(uintptr_t)lds_generic;
    const int tid = threadIdx.x, l = tid & 63, w = rfl(tid >> 6) & 7, h = l >> 5;
    const int tt = w & 1, kq = w >> 1, tglob = rb * 2 + tt;
    STAMP(ws, 20);
    if (w == 0) flag_wait(ws, WS_FH + (rb >> 1) * 256, 14, tag, l);
    BARRIER();
    STAMP(ws, 21);
    const unsigned vo = (l >> 3) * 896 + (((l & 7) ^ (((l >> 4) & 1) << 2)) * 16);
    const unsigned vl = l * 16;
    const char* w2src = w2 + ng * 128 + (size_t)(448 * kq + 32 * tt) * 896;
    const unsigned wdst = lds0 + kq * 8192 + tt * 4096;
    const char* hsrc = ws + WS_H + ((size_t)tglob * 56 + 14 * kq) * 2048;
    const char* osrc = ws + WS_O + ((size_t)tglob * 16 + 4 * kq) * 2048;
    u32x4 f[9][4];
    auto issue = [&](int st) {
        if (st < 7) {
#pragma unroll
            for (int e = 0; e < 4; ++e) glds16(w2src + (size_t)(st * 64 + e * 8) * 896, vo, wdst + (st & 3) * 32768 + e * 1024);
#pragma unroll
            for (int e = 0; e < 4; ++e) f[st][e] = gload16_sc1(hsrc + st * 4096 + e * 1024, vl);
        } else {
            const int hd = 4 * kq + 2 * (st - 7) + tt; const int r0 = hd < 15 ? 28 * hd : 416;
#pragma unroll
            for (int e = 0; e < 4; ++e) glds16(wo + ng * 128 + (size_t)(r0 + e * 8) * 896, vo, wdst + (st & 3) * 32768 + e * 1024);
#pragma unroll
            for (int e = 0; e < 4; ++e) f[st][e] = gload16_sc1(osrc + (st - 7) * 4096 + e * 1024, vl);
        }
    };
    issue(0); issue(1); issue(2); issue(3);
    const int q = (l & 15) >> 2, p = l & 3, cb = (l >> 4) & 1;
    const int tr0 = (4 * h + q) * 128 + (((2 * cb + (p >> 1)) ^ ((q >> 1) << 2)) * 16) + (p & 1) * 8, tr1 = tr0 ^ 64;
    f32x16 a0 = {}, a1 = {};
#define PINF(st) asm volatile("" : "+v"(f[st][0]), "+v"(f[st][1]), "+v"(f[st][2]), "+v"(f[st][3]))
#pragma unroll
    for (int st = 0; st < 9; ++st) {
        if (st == 0) WAIT_VM(24); else if (st <= 6) WAIT_VM(16); else if (st == 7) WAIT_VM(8); else WAIT_VM(0);
        PINF(st);
        if (st == 4 && w == 0) flag_wait(ws, WS_FO + (rb >> 1) * 256, 16, tag, l);
        WAIT_LGKM0(); BARRIER();
        if (st >= 1 && st + 3 < 9) issue(st + 3);
        const LAS char* wb = lds + (st & 3) * 32768 + kq * 8192;
        if (st < 7) {
#pragma unroll
            for (int u = 0; u < 2; ++u)
#pragma unroll
                for (int s = 0; s < 2; ++s) {
                    const LAS char* wp = wb + u * 4096 + s * 2048;
                    a0 = MFMA(trfrag(wp + tr0, wp + tr0 + 1024), asbf8(f[st][2 * u + s]), a0);
                    a1 = MFMA(trfrag(wp + tr1, wp + tr1 + 1024), asbf8(f[st][2 * u + s]), a1);
                }
        } else {
#pragma unroll
            for (int u = 0; u < 2; ++u) {
                const int hd = 4 * kq + 2 * (st - 7) + u;
                const LAS char* wp = wb + u * 4096 + (hd == 15 ? 512 : 0);
                a0 = MFMA(trfrag(wp + tr0, wp + tr0 + 1024), asbf8(f[st][2 * u]), a0);
                a1 = MFMA(trfrag(wp + tr1, wp + tr1 + 1024), asbf8(f[st][2 * u]), a1);
                a0 = MFMA(trfrag(wp + tr0 + 2048, wp + tr0 + 2048 + 512), asbf8(f[st][2 * u + 1]), a0);
                a1 = MFMA(trfrag(wp + tr1 + 2048, wp + tr1 + 2048 + 512), asbf8(f[st][2 * u + 1]), a1);
            }
        }
    }
    STAMP(ws, 22);
    const int tok = tid >> 3, ch = tid & 7;
    const size_t goff = (size_t)(rb * 64 + tok) * 896 + ng * 128 + ch * 16;
    const u32x4 xv = *(const u32x4*)(x + goff), bv = *(const u32x4*)(b2 + ng * 128 + ch * 16);
    WAIT_LGKM0(); BARRIER();
    {
        LAS char* rw = lds + (kq * 64 + tt * 32 + (l & 31)) * C_RROW + h * 16;
#pragma unroll
        for (int g = 0; g < 4; ++g) {
            *(LAS f32x4*)(rw + g * 32) = (f32x4){a0[4 * g], a0[4 * g + 1], a0[4 * g + 2], a0[4 * g + 3]};
            *(LAS f32x4*)(rw + 128 + g * 32) = (f32x4){a1[4 * g], a1[4 * g + 1], a1[4 * g + 2], a1[4 * g + 3]};
        }
    }
    WAIT_LGKM0(); BARRIER();
    f32x4 s0 = {}, s1 = {};
#pragma unroll
    for (int k = 0; k < 4; ++k) { const LAS char* rr = lds + (k * 64 + tok) * C_RROW + ch * 32; s0 += *(const LAS f32x4*)rr; s1 += *(const LAS f32x4*)(rr + 16); }
    u32x4 ov;
    ov[0] = cvtpk(s0[0] + bflo(xv[0]) + bflo(bv[0]), s0[1] + bfhi(xv[0]) + bfhi(bv[0]));
    ov[1] = cvtpk(s0[2] + bflo(xv[1]) + bflo(bv[1]), s0[3] + bfhi(xv[1]) + bfhi(bv[1]));
    ov[2] = cvtpk(s1[0] + bflo(xv[2]) + bflo(bv[2]), s1[1] + bfhi(xv[2]) + bfhi(bv[2]));
    ov[3] = cvtpk(s1[2] + bflo(xv[3]) + bflo(bv[3]), s1[3] + bfhi(xv[3]) + bfhi(bv[3]));
    *(u32x4*)(out + goff) = ov;
    if (bump && tid == 0) flag_store(ws, WS_EPOCH, tag);
    STAMP(ws, 23);
}

__global__ void __launch_bounds__(512, 2) fused_block(const char* x, const char* wqkv, const char* wo, const char* w1, const char* b1,
                                                      const char* w2, const char* b2, char* out, char* ws) {
    extern __shared__ __attribute__((aligned(16))) char lds[];
    const int b = blockIdx.x, xcd = b & 7, i = b >> 3;
    const int Rb = i >> 1, c = 2 * xcd + (i & 1);
    u64 tag;
    { uint64_t pc; asm volatile("s_getpc_b64 %0" : "=s"(pc)); pc &= ~127ull;
      if (threadIdx.x >= 448) { const unsigned ll = threadIdx.x & 63; const unsigned o2 = (unsigned)(((blockIdx.x >> 3) * 11 + (ll < 11 ? ll : 0)) * 128); touch4((const void*)pc, o2, (unsigned)(uintptr_t)lds + L_DUMMY); } }
    phaseA(x, wqkv, w1, b1, ws, lds, Rb, c, tag);
    phaseB(ws, lds, c, Rb, tag);
    if (i < 28) phaseC(x, wo, w2, b2, ws, out, lds, 4 * xcd + i / 7, i % 7, tag, b == 0);
}

extern "C" void kernel_launch(void* const* d_in, const int* in_sizes, int n_in, void* d_out, int out_size, void* d_ws, size_t ws_size, hipStream_t stream) {
    const char* x = (const char*)d_in[0]; const char* wqkv = (const char*)d_in[1]; const char* wo = (const char*)d_in[2];
    const char* w1 = (const char*)d_in[4]; const char* b1 = (const char*)d_in[5]; const char* w2 = (const char*)d_in[6]; const char* b2 = (const char*)d_in[7];
    (void)hipFuncSetAttribute((const void*)fused_block, hipFuncAttributeMaxDynamicSharedMemorySize, L_TOTAL);
    hipLaunchKernelGGL(fused_block, dim3(256), dim3(512), L_TOTAL, stream, x, wqkv, wo, w1, b1, w2, b2, (char*)d_out, (char*)d_ws);
}
```

```cpp
#include <hip/hip_runtime.h>
#include <hip/hip_bf16.h>
#include <stdint.h>

typedef __bf16 bf16x2 __attribute__((ext_vector_type(2)));
typedef __bf16 bf16x8 __attribute__((ext_vector_type(8)));
typedef float f32x2 __attribute__((ext_vector_type(2)));
typedef float f32x4 __attribute__((ext_vector_type(4)));
typedef float f32x16 __attribute__((ext_vector_type(16)));
typedef short s16x4 __attribute__((ext_vector_type(4)));
typedef unsigned u32x2 __attribute__((ext_vector_type(2)));
typedef unsigned u32x4 __attribute__((ext_vector_type(4)));
typedef __attribute__((address_space(1))) unsigned long long gu64;
#define LAS __attribute__((address_space(3)))
#define DI __device__ __forceinline__
#define MFMA(a, b, c) __builtin_amdgcn_mfma_f32_32x32x16_bf16((a), (b), (c), 0, 0, 0)

constexpr size_t WS_EPOCH = 0;
constexpr size_t WS_FKV = 256;
constexpr size_t WS_FH = WS_FKV + 16 * 256;
constexpr size_t WS_FO = WS_FH + 16 * 256;
constexpr size_t WS_KN = 16384;
constexpr size_t WS_KV = 32768;
constexpr size_t WS_H = WS_KV + 16 * 64 * 4096;
constexpr size_t WS_O = WS_H + (size_t)64 * 56 * 2048;
constexpr size_t WS_END = WS_O + 64 * 16 * 2048;
#define STAMPC(wsp, k) do {} while (0)
#define STAMP(wsp, k) do {} while (0)

DI unsigned cvtpk(float lo, float hi) { f32x2 v = {lo, hi}; bf16x2 b = __builtin_convertvector(v, bf16x2); return __builtin_bit_cast(unsigned, b); }
DI bf16x2 asbf2(unsigned v) { return __builtin_bit_cast(bf16x2, v); }
DI bf16x8 asbf8(u32x4 v) { return __builtin_bit_cast(bf16x8, v); }
DI float bflo(unsigned v) { return __uint_as_float(v << 16); }
DI float bfhi(unsigned v) { return __uint_as_float(v & 0xffff0000u); }
DI unsigned rfl(unsigned x) { return __builtin_amdgcn_readfirstlane(x); }
DI const void* uptr(const void* p) { const uint64_t v = (uint64_t)p; return (const void*)(((uint64_t)rfl((unsigned)(v >> 32)) << 32) | rfl((unsigned)v)); }
DI void glds16(const void* sbase_, unsigned voff, unsigned lds_dst_) {
    unsigned sv; const void* sbase = uptr(sbase_); const unsigned lds_dst = rfl(lds_dst_);
    asm volatile("s_mov_b32 %0, m0\n\ts_mov_b32 m0, %3\n\ts_nop 4\n\tglobal_load_lds_dwordx4 %1, %2\n\ts_mov_b32 m0, %0"
                 : "=&s"(sv) : "v"(voff), "s"(sbase), "s"(lds_dst) : "memory");
}
DI u32x4 gload16(const void* sbase, unsigned voff) { u32x4 r; asm volatile("global_load_dwordx4 %0, %1, %2" : "=v"(r) : "v"(voff), "s"(uptr(sbase)) : "memory"); return r; }
DI u32x4 gload16_sc1(const void* sbase, unsigned voff) { u32x4 r; asm volatile("s_nop 4\n\tglobal_load_dwordx4 %0, %1, %2 sc1" : "=v"(r) : "v"(voff), "s"(uptr(sbase)) : "memory"); return r; }
DI void gstore16_sc1(void* sbase, unsigned voff, u32x4 v) { asm volatile("s_nop 4\n\tglobal_store_dwordx4 %0, %1, %2 sc1\n\ts_nop 1" :: "v"(voff), "v"(v), "s"(uptr(sbase)) : "memory"); }
DI s16x4 trread(const LAS char* p) { return __builtin_bit_cast(s16x4, __builtin_amdgcn_ds_read_tr16_b64_v4i16((LAS s16x4*)p)); }
DI bf16x8 trfrag(const LAS char* p0, const LAS char* p1) {
    s16x4 a = trread(p0), b = trread(p1);
    return __builtin_bit_cast(bf16x8, __builtin_shufflevector(a, b, 0, 1, 2, 3, 4, 5, 6, 7));
}
DI bf16x8 ldsfrag(const LAS char* p) { return *(const LAS bf16x8*)p; }
template <int CTRL> DI float dppf(float v) { return __int_as_float(__builtin_amdgcn_update_dpp(0, __float_as_int(v), CTRL, 0xf, 0xf, true)); }
DI u32x4 pack8(const f32x16& a, int s) {
    u32x4 p = {cvtpk(a[8 * s], a[8 * s + 1]), cvtpk(a[8 * s + 2], a[8 * s + 3]), cvtpk(a[8 * s + 4], a[8 * s + 5]), cvtpk(a[8 * s + 6], a[8 * s + 7])};
    return p;
}
DI float swap32_lo(float v) { auto sw = __builtin_amdgcn_permlane32_swap(__float_as_uint(v), __float_as_uint(v), false, false); return __uint_as_float(sw[0]); }
DI float xhalf_sum(float v) { auto sw = __builtin_amdgcn_permlane32_swap(__float_as_uint(v), __float_as_uint(v), false, false); return __uint_as_float(sw[0]) + __uint_as_float(sw[1]); }
DI float xhalf_max(float v) { auto sw = __builtin_amdgcn_permlane32_swap(__float_as_uint(v), __float_as_uint(v), false, false); return __builtin_fmaxf(__uint_as_float(sw[0]), __uint_as_float(sw[1])); }
DI float row32_max(float v) {
    v = __builtin_fmaxf(v, dppf<0xB1>(v)); v = __builtin_fmaxf(v, dppf<0x4E>(v)); v = __builtin_fmaxf(v, dppf<0x141>(v)); v = __builtin_fmaxf(v, dppf<0x140>(v));
    auto sw = __builtin_amdgcn_permlane16_swap(__float_as_uint(v), __float_as_uint(v), false, false); return __builtin_fmaxf(__uint_as_float(sw[0]), __uint_as_float(sw[1]));
}
#define BARRIER() asm volatile("s_barrier" ::: "memory")
#define WAIT_VM(n) asm volatile("s_waitcnt vmcnt(" #n ")" ::: "memory")
#define WAIT_LGKM0() asm volatile("s_waitcnt lgkmcnt(0)" ::: "memory")

typedef unsigned long long u64;
DI void flag_store(char* ws, size_t off, u64 tag) { __hip_atomic_store((gu64*)(ws + off), tag, __ATOMIC_RELAXED, __HIP_MEMORY_SCOPE_AGENT); }
DI void flag_wait(const char* ws, size_t off, int n, u64 tag, int l) {
    gu64* p = (gu64*)(ws + off) + (l < n ? l : 0);
    for (unsigned spins = 0; spins < (1u << 20); ++spins) {
        const u64 v = __hip_atomic_load(p, __ATOMIC_RELAXED, __HIP_MEMORY_SCOPE_AGENT);
        if (__all(v == tag)) break;
        __builtin_amdgcn_s_sleep(2);
    }
}
#define ATTN_FAST_ASM \
    "s_mov_b32 s43, m0\n\t" \
    "s_waitcnt vmcnt(12)\n\t" \
    "s_barrier\n\t" \
    "ds_read_b128 v[56:59], v96 offset:0\n\t" \
    "ds_read_b128 v[60:63], v96 offset:1024\n\t" \
    "ds_read_b128 v[64:67], v96 offset:4096\n\t" \
    "ds_read_b128 v[68:71], v96 offset:5120\n\t" \
    "ds_read_b128 v[72:75], v96 offset:2048\n\t" \
    "ds_read_b128 v[76:79], v96 offset:3072\n\t" \
    "s_waitcnt lgkmcnt(4)\n\t" \
    "v_mfma_f32_32x32x16_bf16 v[16:31], v[56:59], v[48:51], 0\n\t" \
    "v_mfma_f32_32x32x16_bf16 v[16:31], v[60:63], v[52:55], v[16:31]\n\t" \
    "s_nop 7\n\t" \
    "s_nop 7\n\t" \
    "v_exp_f32 v16, v16\n\t" \
    "v_exp_f32 v17, v17\n\t" \
    "ds_read_b128 v[56:59], v96 offset:8192\n\t" \
    "v_exp_f32 v18, v18\n\t" \
    "v_exp_f32 v19, v19\n\t" \
    "v_cvt_pk_bf16_f32 v88, v16, v17\n\t" \
    "v_cvt_pk_bf16_f32 v89, v18, v19\n\t" \
    "s_waitcnt lgkmcnt(2)\n\t" \
    "v_mfma_f32_32x32x16_bf16 v[32:47], v[64:67], v[48:51], 0\n\t" \
    "ds_read_b128 v[60:63], v96 offset:9216\n\t" \
    "v_exp_f32 v20, v20\n\t" \
    "v_exp_f32 v21, v21\n\t" \
    "v_exp_f32 v22, v22\n\t" \
    "v_exp_f32 v23, v23\n\t" \
    "v_cvt_pk_bf16_f32 v90, v20, v21\n\t" \
    "v_cvt_pk_bf16_f32 v91, v22, v23\n\t" \
    "v_mfma_f32_32x32x16_bf16 v[32:47], v[68:71], v[52:55], v[32:47]\n\t" \
    "ds_read_b128 v[80:83], v96 offset:6144\n\t" \
    "v_exp_f32 v24, v24\n\t" \
    "v_exp_f32 v25, v25\n\t" \
    "v_exp_f32 v26, v26\n\t" \
    "v_exp_f32 v27, v27\n\t" \
    "v_cvt_pk_bf16_f32 v92, v24, v25\n\t" \
    "v_cvt_pk_bf16_f32 v93, v26, v27\n\t" \
    "v_mfma_f32_32x32x16_bf16 v[0:15], v[72:75], v[88:91], 0\n\t" \
    "ds_read_b128 v[84:87], v96 offset:7168\n\t" \
    "v_exp_f32 v28, v28\n\t" \
    "v_exp_f32 v29, v29\n\t" \
    "v_exp_f32 v30, v30\n\t" \
    "v_exp_f32 v31, v31\n\t" \
    "v_cvt_pk_bf16_f32 v94, v28, v29\n\t" \
    "v_cvt_pk_bf16_f32 v95, v30, v31\n\t" \
    "v_exp_f32 v32, v32\n\t" \
    "v_exp_f32 v33, v33\n\t" \
    "v_mfma_f32_32x32x16_bf16 v[0:15], v[76:79], v[92:95], v[0:15]\n\t" \
    "ds_read_b128 v[64:67], v96 offset:12288\n\t" \
    "v_exp_f32 v34, v34\n\t" \
    "v_exp_f32 v35, v35\n\t" \
    "v_cvt_pk_bf16_f32 v88, v32, v33\n\t" \
    "v_cvt_pk_bf16_f32 v89, v34, v35\n\t" \
    "s_waitcnt lgkmcnt(2)\n\t" \
    "v_mfma_f32_32x32x16_bf16 v[16:31], v[56:59], v[48:51], 0\n\t" \
    "ds_read_b128 v[68:71], v96 offset:13312\n\t" \
    "v_exp_f32 v36, v36\n\t" \
    "v_exp_f32 v37, v37\n\t" \
    "v_exp_f32 v38, v38\n\t" \
    "v_exp_f32 v39, v39\n\t" \
    "v_cvt_pk_bf16_f32 v90, v36, v37\n\t" \
    "v_cvt_pk_bf16_f32 v91, v38, v39\n\t" \
    "v_mfma_f32_32x32x16_bf16 v[16:31], v[60:63], v[52:55], v[16:31]\n\t" \
    "ds_read_b128 v[72:75], v96 offset:10240\n\t" \
    "v_exp_f32 v40, v40\n\t" \
    "v_exp_f32 v41, v41\n\t" \
    "v_exp_f32 v42, v42\n\t" \
    "v_exp_f32 v43, v43\n\t" \
    "v_cvt_pk_bf16_f32 v92, v40, v41\n\t" \
    "v_cvt_pk_bf16_f32 v93, v42, v43\n\t" \
    "v_mfma_f32_32x32x16_bf16 v[0:15], v[80:83], v[88:91], v[0:15]\n\t" \
    "ds_read_b128 v[76:79], v96 offset:11264\n\t" \
    "v_exp_f32 v44, v44\n\t" \
    "v_exp_f32 v45, v45\n\t" \
    "v_exp_f32 v46, v46\n\t" \
    "v_exp_f32 v47, v47\n\t" \
    "v_cvt_pk_bf16_f32 v94, v44, v45\n\t" \
    "v_cvt_pk_bf16_f32 v95, v46, v47\n\t" \
    "s_waitcnt vmcnt(8)\n\t" \
    "s_barrier\n\t" \
    "v_exp_f32 v16, v16\n\t" \
    "v_exp_f32 v17, v17\n\t" \
    "v_mfma_f32_32x32x16_bf16 v[0:15], v[84:87], v[92:95], v[0:15]\n\t" \
    "ds_read_b128 v[56:59], v96 offset:32768\n\t" \
    "v_exp_f32 v18, v18\n\t" \
    "v_exp_f32 v19, v19\n\t" \
    "v_cvt_pk_bf16_f32 v88, v16, v17\n\t" \
    "v_cvt_pk_bf16_f32 v89, v18, v19\n\t" \
    "s_waitcnt lgkmcnt(2)\n\t" \
    "v_mfma_f32_32x32x16_bf16 v[32:47], v[64:67], v[48:51], 0\n\t" \
    "ds_read_b128 v[60:63], v96 offset:33792\n\t" \
    "v_exp_f32 v20, v20\n\t" \
    "v_exp_f32 v21, v21\n\t" \
    "v_exp_f32 v22, v22\n\t" \
    "v_exp_f32 v23, v23\n\t" \
    "v_cvt_pk_bf16_f32 v90, v20, v21\n\t" \
    "v_cvt_pk_bf16_f32 v91, v22, v23\n\t" \
    "v_mfma_f32_32x32x16_bf16 v[32:47], v[68:71], v[52:55], v[32:47]\n\t" \
    "ds_read_b128 v[80:83], v96 offset:14336\n\t" \
    "v_exp_f32 v24, v24\n\t" \
    "v_exp_f32 v25, v25\n\t" \
    "v_exp_f32 v26, v26\n\t" \
    "v_exp_f32 v27, v27\n\t" \
    "v_cvt_pk_bf16_f32 v92, v24, v25\n\t" \
    "v_cvt_pk_bf16_f32 v93, v26, v27\n\t" \
    "v_mfma_f32_32x32x16_bf16 v[0:15], v[72:75], v[88:91], v[0:15]\n\t" \
    "ds_read_b128 v[84:87], v96 offset:15360\n\t" \
    "v_exp_f32 v28, v28\n\t" \
    "v_exp_f32 v29, v29\n\t" \
    "v_exp_f32 v30, v30\n\t" \
    "v_exp_f32 v31, v31\n\t" \
    "v_cvt_pk_bf16_f32 v94, v28, v29\n\t" \
    "v_cvt_pk_bf16_f32 v95, v30, v31\n\t" \
    "v_exp_f32 v32, v32\n\t" \
    "v_exp_f32 v33, v33\n\t" \
    "v_mfma_f32_32x32x16_bf16 v[0:15], v[76:79], v[92:95], v[0:15]\n\t" \
    "ds_read_b128 v[64:67], v96 offset:36864\n\t" \
    "v_exp_f32 v34, v34\n\t" \
    "v_exp_f32 v35, v35\n\t" \
    "v_cvt_pk_bf16_f32 v88, v32, v33\n\t" \
    "v_cvt_pk_bf16_f32 v89, v34, v35\n\t" \
    "s_waitcnt lgkmcnt(2)\n\t" \
    "v_mfma_f32_32x32x16_bf16 v[16:31], v[56:59], v[48:51], 0\n\t" \
    "ds_read_b128 v[68:71], v96 offset:37888\n\t" \
    "v_exp_f32 v36, v36\n\t" \
    "v_exp_f32 v37, v37\n\t" \
    "v_exp_f32 v38, v38\n\t" \
    "v_exp_f32 v39, v39\n\t" \
    "v_cvt_pk_bf16_f32 v90, v36, v37\n\t" \
    "v_cvt_pk_bf16_f32 v91, v38, v39\n\t" \
    "v_mfma_f32_32x32x16_bf16 v[16:31], v[60:63], v[52:55], v[16:31]\n\t" \
    "ds_read_b128 v[72:75], v96 offset:34816\n\t" \
    "v_exp_f32 v40, v40\n\t" \
    "v_exp_f32 v41, v41\n\t" \
    "v_exp_f32 v42, v42\n\t" \
    "v_exp_f32 v43, v43\n\t" \
    "v_cvt_pk_bf16_f32 v92, v40, v41\n\t" \
    "v_cvt_pk_bf16_f32 v93, v42, v43\n\t" \
    "v_mfma_f32_32x32x16_bf16 v[0:15], v[80:83], v[88:91], v[0:15]\n\t" \
    "ds_read_b128 v[76:79], v96 offset:35840\n\t" \
    "v_exp_f32 v44, v44\n\t" \
    "v_exp_f32 v45, v45\n\t" \
    "v_exp_f32 v46, v46\n\t" \
    "v_exp_f32 v47, v47\n\t" \
    "v_cvt_pk_bf16_f32 v94, v44, v45\n\t" \
    "v_cvt_pk_bf16_f32 v95, v46, v47\n\t" \
    "v_exp_f32 v16, v16\n\t" \
    "v_exp_f32 v17, v17\n\t" \
    "v_mfma_f32_32x32x16_bf16 v[0:15], v[84:87], v[92:95], v[0:15]\n\t" \
    "ds_read_b128 v[56:59], v96 offset:40960\n\t" \
    "v_exp_f32 v18, v18\n\t" \
    "v_exp_f32 v19, v19\n\t" \
    "v_cvt_pk_bf16_f32 v88, v16, v17\n\t" \
    "v_cvt_pk_bf16_f32 v89, v18, v19\n\t" \
    "s_waitcnt lgkmcnt(2)\n\t" \
    "v_mfma_f32_32x32x16_bf16 v[32:47], v[64:67], v[48:51], 0\n\t" \
    "ds_read_b128 v[60:63], v96 offset:41984\n\t" \
    "v_exp_f32 v20, v20\n\t" \
    "v_exp_f32 v21, v21\n\t" \
    "v_exp_f32 v22, v22\n\t" \
    "v_exp_f32 v23, v23\n\t" \
    "v_cvt_pk_bf16_f32 v90, v20, v21\n\t" \
    "v_cvt_pk_bf16_f32 v91, v22, v23\n\t" \
    "v_mfma_f32_32x32x16_bf16 v[32:47], v[68:71], v[52:55], v[32:47]\n\t" \
    "ds_read_b128 v[80:83], v96 offset:38912\n\t" \
    "v_exp_f32 v24, v24\n\t" \
    "v_exp_f32 v25, v25\n\t" \
    "v_exp_f32 v26, v26\n\t" \
    "v_exp_f32 v27, v27\n\t" \
    "v_cvt_pk_bf16_f32 v92, v24, v25\n\t" \
    "v_cvt_pk_bf16_f32 v93, v26, v27\n\t" \
    "v_mfma_f32_32x32x16_bf16 v[0:15], v[72:75], v[88:91], v[0:15]\n\t" \
    "ds_read_b128 v[84:87], v96 offset:39936\n\t" \
    "v_exp_f32 v28, v28\n\t" \
    "v_exp_f32 v29, v29\n\t" \
    "v_exp_f32 v30, v30\n\t" \
    "v_exp_f32 v31, v31\n\t" \
    "v_cvt_pk_bf16_f32 v94, v28, v29\n\t" \
    "v_cvt_pk_bf16_f32 v95, v30, v31\n\t" \
    "v_exp_f32 v32, v32\n\t" \
    "v_exp_f32 v33, v33\n\t" \
    "v_mfma_f32_32x32x16_bf16 v[0:15], v[76:79], v[92:95], v[0:15]\n\t" \
    "ds_read_b128 v[64:67], v96 offset:45056\n\t" \
    "v_exp_f32 v34, v34\n\t" \
    "v_exp_f32 v35, v35\n\t" \
    "v_cvt_pk_bf16_f32 v88, v32, v33\n\t" \
    "v_cvt_pk_bf16_f32 v89, v34, v35\n\t" \
    "s_waitcnt lgkmcnt(2)\n\t" \
    "v_mfma_f32_32x32x16_bf16 v[16:31], v[56:59], v[48:51], 0\n\t" \
    "ds_read_b128 v[68:71], v96 offset:46080\n\t" \
    "v_exp_f32 v36, v36\n\t" \
    "v_exp_f32 v37, v37\n\t" \
    "v_exp_f32 v38, v38\n\t" \
    "v_exp_f32 v39, v39\n\t" \
    "v_cvt_pk_bf16_f32 v90, v36, v37\n\t" \
    "v_cvt_pk_bf16_f32 v91, v38, v39\n\t" \
    "v_mfma_f32_32x32x16_bf16 v[16:31], v[60:63], v[52:55], v[16:31]\n\t" \
    "ds_read_b128 v[72:75], v96 offset:43008\n\t" \
    "v_exp_f32 v40, v40\n\t" \
    "v_exp_f32 v41, v41\n\t" \
    "v_exp_f32 v42, v42\n\t" \
    "v_exp_f32 v43, v43\n\t" \
    "v_cvt_pk_bf16_f32 v92, v40, v41\n\t" \
    "v_cvt_pk_bf16_f32 v93, v42, v43\n\t" \
    "v_mfma_f32_32x32x16_bf16 v[0:15], v[80:83], v[88:91], v[0:15]\n\t" \
    "ds_read_b128 v[76:79], v96 offset:44032\n\t" \
    "v_exp_f32 v44, v44\n\t" \
    "v_exp_f32 v45, v45\n\t" \
    "v_exp_f32 v46, v46\n\t" \
    "v_exp_f32 v47, v47\n\t" \
    "v_cvt_pk_bf16_f32 v94, v44, v45\n\t" \
    "v_cvt_pk_bf16_f32 v95, v46, v47\n\t" \
    "s_waitcnt vmcnt(4)\n\t" \
    "s_barrier\n\t" \
    "s_mov_b32 m0, s42\n\t" \
    "s_add_u32 s40, s40, 16384\n\t" \
    "s_addc_u32 s41, s41, 0\n\t" \
    "global_load_lds_dwordx4 v98, s[40:41] offset:0\n\t" \
    "global_load_lds_dwordx4 v98, s[40:41] offset:1024\n\t" \
    "global_load_lds_dwordx4 v98, s[40:41] offset:2048\n\t" \
    "global_load_lds_dwordx4 v98, s[40:41] offset:3072\n\t" \
    "v_exp_f32 v16, v16\n\t" \
    "v_exp_f32 v17, v17\n\t" \
    "v_mfma_f32_32x32x16_bf16 v[0:15], v[84:87], v[92:95], v[0:15]\n\t" \
    "ds_read_b128 v[56:59], v97 offset:0\n\t" \
    "v_exp_f32 v18, v18\n\t" \
    "v_exp_f32 v19, v19\n\t" \
    "v_cvt_pk_bf16_f32 v88, v16, v17\n\t" \
    "v_cvt_pk_bf16_f32 v89, v18, v19\n\t" \
    "s_waitcnt lgkmcnt(2)\n\t" \
    "v_mfma_f32_32x32x16_bf16 v[32:47], v[64:67], v[48:51], 0\n\t" \
    "ds_read_b128 v[60:63], v97 offset:1024\n\t" \
    "v_exp_f32 v20, v20\n\t" \
    "v_exp_f32 v21, v21\n\t" \
    "v_exp_f32 v22, v22\n\t" \
    "v_exp_f32 v23, v23\n\t" \
    "v_cvt_pk_bf16_f32 v90, v20, v21\n\t" \
    "v_cvt_pk_bf16_f32 v91, v22, v23\n\t" \
    "v_mfma_f32_32x32x16_bf16 v[32:47], v[68:71], v[52:55], v[32:47]\n\t" \
    "ds_read_b128 v[80:83], v96 offset:47104\n\t" \
    "v_exp_f32 v24, v24\n\t" \
    "v_exp_f32 v25, v25\n\t" \
    "v_exp_f32 v26, v26\n\t" \
    "v_exp_f32 v27, v27\n\t" \
    "v_cvt_pk_bf16_f32 v92, v24, v25\n\t" \
    "v_cvt_pk_bf16_f32 v93, v26, v27\n\t" \
    "v_mfma_f32_32x32x16_bf16 v[0:15], v[72:75], v[88:91], v[0:15]\n\t" \
    "ds_read_b128 v[84:87], v96 offset:48128\n\t" \
    "v_exp_f32 v28, v28\n\t" \
    "v_exp_f32 v29, v29\n\t" \
    "v_exp_f32 v30, v30\n\t" \
    "v_exp_f32 v31, v31\n\t" \
    "v_cvt_pk_bf16_f32 v94, v28, v29\n\t" \
    "v_cvt_pk_bf16_f32 v95, v30, v31\n\t" \
    "v_exp_f32 v32, v32\n\t" \
    "v_exp_f32 v33, v33\n\t" \
    "v_mfma_f32_32x32x16_bf16 v[0:15], v[76:79], v[92:95], v[0:15]\n\t" \
    "ds_read_b128 v[64:67], v97 offset:4096\n\t" \
    "v_exp_f32 v34, v34\n\t" \
    "v_exp_f32 v35, v35\n\t" \
    "v_cvt_pk_bf16_f32 v88, v32, v33\n\t" \
    "v_cvt_pk_bf16_f32 v89, v34, v35\n\t" \
    "s_waitcnt lgkmcnt(2)\n\t" \
    "v_mfma_f32_32x32x16_bf16 v[16:31], v[56:59], v[48:51], 0\n\t" \
    "ds_read_b128 v[68:71], v97 offset:5120\n\t" \
    "v_exp_f32 v36, v36\n\t" \
    "v_exp_f32 v37, v37\n\t" \
    "v_exp_f32 v38, v38\n\t" \
    "v_exp_f32 v39, v39\n\t" \
    "v_cvt_pk_bf16_f32 v90, v36, v37\n\t" \
    "v_cvt_pk_bf16_f32 v91, v38, v39\n\t" \
    "v_mfma_f32_32x32x16_bf16 v[16:31], v[60:63], v[52:55], v[16:31]\n\t" \
    "ds_read_b128 v[72:75], v97 offset:2048\n\t" \
    "v_exp_f32 v40, v40\n\t" \
    "v_exp_f32 v41, v41\n\t" \
    "v_exp_f32 v42, v42\n\t" \
    "v_exp_f32 v43, v43\n\t" \
    "v_cvt_pk_bf16_f32 v92, v40, v41\n\t" \
    "v_cvt_pk_bf16_f32 v93, v42, v43\n\t" \
    "v_mfma_f32_32x32x16_bf16 v[0:15], v[80:83], v[88:91], v[0:15]\n\t" \
    "ds_read_b128 v[76:79], v97 offset:3072\n\t" \
    "v_exp_f32 v44, v44\n\t" \
    "v_exp_f32 v45, v45\n\t" \
    "v_exp_f32 v46, v46\n\t" \
    "v_exp_f32 v47, v47\n\t" \
    "v_cvt_pk_bf16_f32 v94, v44, v45\n\t" \
    "v_cvt_pk_bf16_f32 v95, v46, v47\n\t" \
    "v_exp_f32 v16, v16\n\t" \
    "v_exp_f32 v17, v17\n\t" \
    "v_mfma_f32_32x32x16_bf16 v[0:15], v[84:87], v[92:95], v[0:15]\n\t" \
    "ds_read_b128 v[56:59], v97 offset:8192\n\t" \
    "v_exp_f32 v18, v18\n\t" \
    "v_exp_f32 v19, v19\n\t" \
    "v_cvt_pk_bf16_f32 v88, v16, v17\n\t" \
    "v_cvt_pk_bf16_f32 v89, v18, v19\n\t" \
    "s_waitcnt lgkmcnt(2)\n\t" \
    "v_mfma_f32_32x32x16_bf16 v[32:47], v[64:67], v[48:51], 0\n\t" \
    "ds_read_b128 v[60:63], v97 offset:9216\n\t" \
    "v_exp_f32 v20, v20\n\t" \
    "v_exp_f32 v21, v21\n\t" \
    "v_exp_f32 v22, v22\n\t" \
    "v_exp_f32 v23, v23\n\t" \
    "v_cvt_pk_bf16_f32 v90, v20, v21\n\t" \
    "v_cvt_pk_bf16_f32 v91, v22, v23\n\t" \
    "v_mfma_f32_32x32x16_bf16 v[32:47], v[68:71], v[52:55], v[32:47]\n\t" \
    "ds_read_b128 v[80:83], v97 offset:6144\n\t" \
    "v_exp_f32 v24, v24\n\t" \
    "v_exp_f32 v25, v25\n\t" \
    "v_exp_f32 v26, v26\n\t" \
    "v_exp_f32 v27, v27\n\t" \
    "v_cvt_pk_bf16_f32 v92, v24, v25\n\t" \
    "v_cvt_pk_bf16_f32 v93, v26, v27\n\t" \
    "v_mfma_f32_32x32x16_bf16 v[0:15], v[72:75], v[88:91], v[0:15]\n\t" \
    "ds_read_b128 v[84:87], v97 offset:7168\n\t" \
    "v_exp_f32 v28, v28\n\t" \
    "v_exp_f32 v29, v29\n\t" \
    "v_exp_f32 v30, v30\n\t" \
    "v_exp_f32 v31, v31\n\t" \
    "v_cvt_pk_bf16_f32 v94, v28, v29\n\t" \
    "v_cvt_pk_bf16_f32 v95, v30, v31\n\t" \
    "v_exp_f32 v32, v32\n\t" \
    "v_exp_f32 v33, v33\n\t" \
    "v_mfma_f32_32x32x16_bf16 v[0:15], v[76:79], v[92:95], v[0:15]\n\t" \
    "ds_read_b128 v[64:67], v97 offset:12288\n\t" \
    "v_exp_f32 v34, v34\n\t" \
    "v_exp_f32 v35, v35\n\t" \
    "v_cvt_pk_bf16_f32 v88, v32, v33\n\t" \
    "v_cvt_pk_bf16_f32 v89, v34, v35\n\t" \
    "s_waitcnt lgkmcnt(2)\n\t" \
    "v_mfma_f32_32x32x16_bf16 v[16:31], v[56:59], v[48:51], 0\n\t" \
    "ds_read_b128 v[68:71], v97 offset:13312\n\t" \
    "v_exp_f32 v36, v36\n\t" \
    "v_exp_f32 v37, v37\n\t" \
    "v_exp_f32 v38, v38\n\t" \
    "v_exp_f32 v39, v39\n\t" \
    "v_cvt_pk_bf16_f32 v90, v36, v37\n\t" \
    "v_cvt_pk_bf16_f32 v91, v38, v39\n\t" \
    "v_mfma_f32_32x32x16_bf16 v[16:31], v[60:63], v[52:55], v[16:31]\n\t" \
    "ds_read_b128 v[72:75], v97 offset:10240\n\t" \
    "v_exp_f32 v40, v40\n\t" \
    "v_exp_f32 v41, v41\n\t" \
    "v_exp_f32 v42, v42\n\t" \
    "v_exp_f32 v43, v43\n\t" \
    "v_cvt_pk_bf16_f32 v92, v40, v41\n\t" \
    "v_cvt_pk_bf16_f32 v93, v42, v43\n\t" \
    "v_mfma_f32_32x32x16_bf16 v[0:15], v[80:83], v[88:91], v[0:15]\n\t" \
    "ds_read_b128 v[76:79], v97 offset:11264\n\t" \
    "v_exp_f32 v44, v44\n\t" \
    "v_exp_f32 v45, v45\n\t" \
    "v_exp_f32 v46, v46\n\t" \
    "v_exp_f32 v47, v47\n\t" \
    "v_cvt_pk_bf16_f32 v94, v44, v45\n\t" \
    "v_cvt_pk_bf16_f32 v95, v46, v47\n\t" \
    "s_waitcnt vmcnt(4)\n\t" \
    "s_barrier\n\t" \
    "s_add_u32 m0, s42, 32768\n\t" \
    "s_add_u32 s40, s40, 16384\n\t" \
    "s_addc_u32 s41, s41, 0\n\t" \
    "global_load_lds_dwordx4 v98, s[40:41] offset:0\n\t" \
    "global_load_lds_dwordx4 v98, s[40:41] offset:1024\n\t" \
    "global_load_lds_dwordx4 v98, s[40:41] offset:2048\n\t" \
    "global_load_lds_dwordx4 v98, s[40:41] offset:3072\n\t" \
    "v_exp_f32 v16, v16\n\t" \
    "v_exp_f32 v17, v17\n\t" \
    "v_mfma_f32_32x32x16_bf16 v[0:15], v[84:87], v[92:95], v[0:15]\n\t" \
    "ds_read_b128 v[56:59], v97 offset:32768\n\t" \
    "v_exp_f32 v18, v18\n\t" \
    "v_exp_f32 v19, v19\n\t" \
    "v_cvt_pk_bf16_f32 v88, v16, v17\n\t" \
    "v_cvt_pk_bf16_f32 v89, v18, v19\n\t" \
    "s_waitcnt lgkmcnt(2)\n\t" \
    "v_mfma_f32_32x32x16_bf16 v[32:47], v[64:67], v[48:51], 0\n\t" \
    "ds_read_b128 v[60:63], v97 offset:33792\n\t" \
    "v_exp_f32 v20, v20\n\t" \
    "v_exp_f32 v21, v21\n\t" \
    "v_exp_f32 v22, v22\n\t" \
    "v_exp_f32 v23, v23\n\t" \
    "v_cvt_pk_bf16_f32 v90, v20, v21\n\t" \
    "v_cvt_pk_bf16_f32 v91, v22, v23\n\t" \
    "v_mfma_f32_32x32x16_bf16 v[32:47], v[68:71], v[52:55], v[32:47]\n\t" \
    "ds_read_b128 v[80:83], v97 offset:14336\n\t" \
    "v_exp_f32 v24, v24\n\t" \
    "v_exp_f32 v25, v25\n\t" \
    "v_exp_f32 v26, v26\n\t" \
    "v_exp_f32 v27, v27\n\t" \
    "v_cvt_pk_bf16_f32 v92, v24, v25\n\t" \
    "v_cvt_pk_bf16_f32 v93, v26, v27\n\t" \
    "v_mfma_f32_32x32x16_bf16 v[0:15], v[72:75], v[88:91], v[0:15]\n\t" \
    "ds_read_b128 v[84:87], v97 offset:15360\n\t" \
    "v_exp_f32 v28, v28\n\t" \
    "v_exp_f32 v29, v29\n\t" \
    "v_exp_f32 v30, v30\n\t" \
    "v_exp_f32 v31, v31\n\t" \
    "v_cvt_pk_bf16_f32 v94, v28, v29\n\t" \
    "v_cvt_pk_bf16_f32 v95, v30, v31\n\t" \
    "v_exp_f32 v32, v32\n\t" \
    "v_exp_f32 v33, v33\n\t" \
    "v_mfma_f32_32x32x16_bf16 v[0:15], v[76:79], v[92:95], v[0:15]\n\t" \
    "ds_read_b128 v[64:67], v97 offset:36864\n\t" \
    "v_exp_f32 v34, v34\n\t" \
    "v_exp_f32 v35, v35\n\t" \
    "v_cvt_pk_bf16_f32 v88, v32, v33\n\t" \
    "v_cvt_pk_bf16_f32 v89, v34, v35\n\t" \
    "s_waitcnt lgkmcnt(2)\n\t" \
    "v_mfma_f32_32x32x16_bf16 v[16:31], v[56:59], v[48:51], 0\n\t" \
    "ds_read_b128 v[68:71], v97 offset:37888\n\t" \
    "v_exp_f32 v36, v36\n\t" \
    "v_exp_f32 v37, v37\n\t" \
    "v_exp_f32 v38, v38\n\t" \
    "v_exp_f32 v39, v39\n\t" \
    "v_cvt_pk_bf16_f32 v90, v36, v37\n\t" \
    "v_cvt_pk_bf16_f32 v91, v38, v39\n\t" \
    "v_mfma_f32_32x32x16_bf16 v[16:31], v[60:63], v[52:55], v[16:31]\n\t" \
    "ds_read_b128 v[72:75], v97 offset:34816\n\t" \
    "v_exp_f32 v40, v40\n\t" \
    "v_exp_f32 v41, v41\n\t" \
    "v_exp_f32 v42, v42\n\t" \
    "v_exp_f32 v43, v43\n\t" \
    "v_cvt_pk_bf16_f32 v92, v40, v41\n\t" \
    "v_cvt_pk_bf16_f32 v93, v42, v43\n\t" \
    "v_mfma_f32_32x32x16_bf16 v[0:15], v[80:83], v[88:91], v[0:15]\n\t" \
    "ds_read_b128 v[76:79], v97 offset:35840\n\t" \
    "v_exp_f32 v44, v44\n\t" \
    "v_exp_f32 v45, v45\n\t" \
    "v_exp_f32 v46, v46\n\t" \
    "v_exp_f32 v47, v47\n\t" \
    "v_cvt_pk_bf16_f32 v94, v44, v45\n\t" \
    "v_cvt_pk_bf16_f32 v95, v46, v47\n\t" \
    "v_exp_f32 v16, v16\n\t" \
    "v_exp_f32 v17, v17\n\t" \
    "v_mfma_f32_32x32x16_bf16 v[0:15], v[84:87], v[92:95], v[0:15]\n\t" \
    "ds_read_b128 v[56:59], v97 offset:40960\n\t" \
    "v_exp_f32 v18, v18\n\t" \
    "v_exp_f32 v19, v19\n\t" \
    "v_cvt_pk_bf16_f32 v88, v16, v17\n\t" \
    "v_cvt_pk_bf16_f32 v89, v18, v19\n\t" \
    "s_waitcnt lgkmcnt(2)\n\t" \
    "v_mfma_f32_32x32x16_bf16 v[32:47], v[64:67], v[48:51], 0\n\t" \
    "ds_read_b128 v[60:63], v97 offset:41984\n\t" \
    "v_exp_f32 v20, v20\n\t" \
    "v_exp_f32 v21, v21\n\t" \
    "v_exp_f32 v22, v22\n\t" \
    "v_exp_f32 v23, v23\n\t" \
    "v_cvt_pk_bf16_f32 v90, v20, v21\n\t" \
    "v_cvt_pk_bf16_f32 v91, v22, v23\n\t" \
    "v_mfma_f32_32x32x16_bf16 v[32:47], v[68:71], v[52:55], v[32:47]\n\t" \
    "ds_read_b128 v[80:83], v97 offset:38912\n\t" \
    "v_exp_f32 v24, v24\n\t" \
    "v_exp_f32 v25, v25\n\t" \
    "v_exp_f32 v26, v26\n\t" \
    "v_exp_f32 v27, v27\n\t" \
    "v_cvt_pk_bf16_f32 v92, v24, v25\n\t" \
    "v_cvt_pk_bf16_f32 v93, v26, v27\n\t" \
    "v_mfma_f32_32x32x16_bf16 v[0:15], v[72:75], v[88:91], v[0:15]\n\t" \
    "ds_read_b128 v[84:87], v97 offset:39936\n\t" \
    "v_exp_f32 v28, v28\n\t" \
    "v_exp_f32 v29, v29\n\t" \
    "v_exp_f32 v30, v30\n\t" \
    "v_exp_f32 v31, v31\n\t" \
    "v_cvt_pk_bf16_f32 v94, v28, v29\n\t" \
    "v_cvt_pk_bf16_f32 v95, v30, v31\n\t" \
    "v_exp_f32 v32, v32\n\t" \
    "v_exp_f32 v33, v33\n\t" \
    "v_mfma_f32_32x32x16_bf16 v[0:15], v[76:79], v[92:95], v[0:15]\n\t" \
    "ds_read_b128 v[64:67], v97 offset:45056\n\t" \
    "v_exp_f32 v34, v34\n\t" \
    "v_exp_f32 v35, v35\n\t" \
    "v_cvt_pk_bf16_f32 v88, v32, v33\n\t" \
    "v_cvt_pk_bf16_f32 v89, v34, v35\n\t" \
    "s_waitcnt lgkmcnt(2)\n\t" \
    "v_mfma_f32_32x32x16_bf16 v[16:31], v[56:59], v[48:51], 0\n\t" \
    "ds_read_b128 v[68:71], v97 offset:46080\n\t" \
    "v_exp_f32 v36, v36\n\t" \
    "v_exp_f32 v37, v37\n\t" \
    "v_exp_f32 v38, v38\n\t" \
    "v_exp_f32 v39, v39\n\t" \
    "v_cvt_pk_bf16_f32 v90, v36, v37\n\t" \
    "v_cvt_pk_bf16_f32 v91, v38, v39\n\t" \
    "v_mfma_f32_32x32x16_bf16 v[16:31], v[60:63], v[52:55], v[16:31]\n\t" \
    "ds_read_b128 v[72:75], v97 offset:43008\n\t" \
    "v_exp_f32 v40, v40\n\t" \
    "v_exp_f32 v41, v41\n\t" \
    "v_exp_f32 v42, v42\n\t" \
    "v_exp_f32 v43, v43\n\t" \
    "v_cvt_pk_bf16_f32 v92, v40, v41\n\t" \
    "v_cvt_pk_bf16_f32 v93, v42, v43\n\t" \
    "v_mfma_f32_32x32x16_bf16 v[0:15], v[80:83], v[88:91], v[0:15]\n\t" \
    "ds_read_b128 v[76:79], v97 offset:44032\n\t" \
    "v_exp_f32 v44, v44\n\t" \
    "v_exp_f32 v45, v45\n\t" \
    "v_exp_f32 v46, v46\n\t" \
    "v_exp_f32 v47, v47\n\t" \
    "v_cvt_pk_bf16_f32 v94, v44, v45\n\t" \
    "v_cvt_pk_bf16_f32 v95, v46, v47\n\t" \
    "s_waitcnt vmcnt(4)\n\t" \
    "s_barrier\n\t" \
    "s_add_u32 m0, s42, 65536\n\t" \
    "s_add_u32 s40, s40, 16384\n\t" \
    "s_addc_u32 s41, s41, 0\n\t" \
    "global_load_lds_dwordx4 v98, s[40:41] offset:0\n\t" \
    "global_load_lds_dwordx4 v98, s[40:41] offset:1024\n\t" \
    "global_load_lds_dwordx4 v98, s[40:41] offset:2048\n\t" \
    "global_load_lds_dwordx4 v98, s[40:41] offset:3072\n\t" \
    "v_exp_f32 v16, v16\n\t" \
    "v_exp_f32 v17, v17\n\t" \
    "v_mfma_f32_32x32x16_bf16 v[0:15], v[84:87], v[92:95], v[0:15]\n\t" \
    "ds_read_b128 v[56:59], v96 offset:0\n\t" \
    "v_exp_f32 v18, v18\n\t" \
    "v_exp_f32 v19, v19\n\t" \
    "v_cvt_pk_bf16_f32 v88, v16, v17\n\t" \
    "v_cvt_pk_bf16_f32 v89, v18, v19\n\t" \
    "s_waitcnt lgkmcnt(2)\n\t" \
    "v_mfma_f32_32x32x16_bf16 v[32:47], v[64:67], v[48:51], 0\n\t" \
    "ds_read_b128 v[60:63], v96 offset:1024\n\t" \
    "v_exp_f32 v20, v20\n\t" \
    "v_exp_f32 v21, v21\n\t" \
    "v_exp_f32 v22, v22\n\t" \
    "v_exp_f32 v23, v23\n\t" \
    "v_cvt_pk_bf16_f32 v90, v20, v21\n\t" \
    "v_cvt_pk_bf16_f32 v91, v22, v23\n\t" \
    "v_mfma_f32_32x32x16_bf16 v[32:47], v[68:71], v[52:55], v[32:47]\n\t" \
    "ds_read_b128 v[80:83], v97 offset:47104\n\t" \
    "v_exp_f32 v24, v24\n\t" \
    "v_exp_f32 v25, v25\n\t" \
    "v_exp_f32 v26, v26\n\t" \
    "v_exp_f32 v27, v27\n\t" \
    "v_cvt_pk_bf16_f32 v92, v24, v25\n\t" \
    "v_cvt_pk_bf16_f32 v93, v26, v27\n\t" \
    "v_mfma_f32_32x32x16_bf16 v[0:15], v[72:75], v[88:91], v[0:15]\n\t" \
    "ds_read_b128 v[84:87], v97 offset:48128\n\t" \
    "v_exp_f32 v28, v28\n\t" \
    "v_exp_f32 v29, v29\n\t" \
    "v_exp_f32 v30, v30\n\t" \
    "v_exp_f32 v31, v31\n\t" \
    "v_cvt_pk_bf16_f32 v94, v28, v29\n\t" \
    "v_cvt_pk_bf16_f32 v95, v30, v31\n\t" \
    "v_exp_f32 v32, v32\n\t" \
    "v_exp_f32 v33, v33\n\t" \
    "v_mfma_f32_32x32x16_bf16 v[0:15], v[76:79], v[92:95], v[0:15]\n\t" \
    "ds_read_b128 v[64:67], v96 offset:4096\n\t" \
    "v_exp_f32 v34, v34\n\t" \
    "v_exp_f32 v35, v35\n\t" \
    "v_cvt_pk_bf16_f32 v88, v32, v33\n\t" \
    "v_cvt_pk_bf16_f32 v89, v34, v35\n\t" \
    "s_waitcnt lgkmcnt(2)\n\t" \
    "v_mfma_f32_32x32x16_bf16 v[16:31], v[56:59], v[48:51], 0\n\t" \
    "ds_read_b128 v[68:71], v96 offset:5120\n\t" \
    "v_exp_f32 v36, v36\n\t" \
    "v_exp_f32 v37, v37\n\t" \
    "v_exp_f32 v38, v38\n\t" \
    "v_exp_f32 v39, v39\n\t" \
    "v_cvt_pk_bf16_f32 v90, v36, v37\n\t" \
    "v_cvt_pk_bf16_f32 v91, v38, v39\n\t" \
    "v_mfma_f32_32x32x16_bf16 v[16:31], v[60:63], v[52:55], v[16:31]\n\t" \
    "ds_read_b128 v[72:75], v96 offset:2048\n\t" \
    "v_exp_f32 v40, v40\n\t" \
    "v_exp_f32 v41, v41\n\t" \
    "v_exp_f32 v42, v42\n\t" \
    "v_exp_f32 v43, v43\n\t" \
    "v_cvt_pk_bf16_f32 v92, v40, v41\n\t" \
    "v_cvt_pk_bf16_f32 v93, v42, v43\n\t" \
    "v_mfma_f32_32x32x16_bf16 v[0:15], v[80:83], v[88:91], v[0:15]\n\t" \
    "ds_read_b128 v[76:79], v96 offset:3072\n\t" \
    "v_exp_f32 v44, v44\n\t" \
    "v_exp_f32 v45, v45\n\t" \
    "v_exp_f32 v46, v46\n\t" \
    "v_exp_f32 v47, v47\n\t" \
    "v_cvt_pk_bf16_f32 v94, v44, v45\n\t" \
    "v_cvt_pk_bf16_f32 v95, v46, v47\n\t" \
    "v_exp_f32 v16, v16\n\t" \
    "v_exp_f32 v17, v17\n\t" \
    "v_mfma_f32_32x32x16_bf16 v[0:15], v[84:87], v[92:95], v[0:15]\n\t" \
    "ds_read_b128 v[56:59], v96 offset:8192\n\t" \
    "v_exp_f32 v18, v18\n\t" \
    "v_exp_f32 v19, v19\n\t" \
    "v_cvt_pk_bf16_f32 v88, v16, v17\n\t" \
    "v_cvt_pk_bf16_f32 v89, v18, v19\n\t" \
    "s_waitcnt lgkmcnt(2)\n\t" \
    "v_mfma_f32_32x32x16_bf16 v[32:47], v[64:67], v[48:51], 0\n\t" \
    "ds_read_b128 v[60:63], v96 offset:9216\n\t" \
    "v_exp_f32 v20, v20\n\t" \
    "v_exp_f32 v21, v21\n\t" \
    "v_exp_f32 v22, v22\n\t" \
    "v_exp_f32 v23, v23\n\t" \
    "v_cvt_pk_bf16_f32 v90, v20, v21\n\t" \
    "v_cvt_pk_bf16_f32 v91, v22, v23\n\t" \
    "v_mfma_f32_32x32x16_bf16 v[32:47], v[68:71], v[52:55], v[32:47]\n\t" \
    "ds_read_b128 v[80:83], v96 offset:6144\n\t" \
    "v_exp_f32 v24, v24\n\t" \
    "v_exp_f32 v25, v25\n\t" \
    "v_exp_f32 v26, v26\n\t" \
    "v_exp_f32 v27, v27\n\t" \
    "v_cvt_pk_bf16_f32 v92, v24, v25\n\t" \
    "v_cvt_pk_bf16_f32 v93, v26, v27\n\t" \
    "v_mfma_f32_32x32x16_bf16 v[0:15], v[72:75], v[88:91], v[0:15]\n\t" \
    "ds_read_b128 v[84:87], v96 offset:7168\n\t" \
    "v_exp_f32 v28, v28\n\t" \
    "v_exp_f32 v29, v29\n\t" \
    "v_exp_f32 v30, v30\n\t" \
    "v_exp_f32 v31, v31\n\t" \
    "v_cvt_pk_bf16_f32 v94, v28, v29\n\t" \
    "v_cvt_pk_bf16_f32 v95, v30, v31\n\t" \
    "v_exp_f32 v32, v32\n\t" \
    "v_exp_f32 v33, v33\n\t" \
    "v_mfma_f32_32x32x16_bf16 v[0:15], v[76:79], v[92:95], v[0:15]\n\t" \
    "ds_read_b128 v[64:67], v96 offset:12288\n\t" \
    "v_exp_f32 v34, v34\n\t" \
    "v_exp_f32 v35, v35\n\t" \
    "v_cvt_pk_bf16_f32 v88, v32, v33\n\t" \
    "v_cvt_pk_bf16_f32 v89, v34, v35\n\t" \
    "s_waitcnt lgkmcnt(2)\n\t" \
    "v_mfma_f32_32x32x16_bf16 v[16:31], v[56:59], v[48:51], 0\n\t" \
    "ds_read_b128 v[68:71], v96 offset:13312\n\t" \
    "v_exp_f32 v36, v36\n\t" \
    "v_exp_f32 v37, v37\n\t" \
    "v_exp_f32 v38, v38\n\t" \
    "v_exp_f32 v39, v39\n\t" \
    "v_cvt_pk_bf16_f32 v90, v36, v37\n\t" \
    "v_cvt_pk_bf16_f32 v91, v38, v39\n\t" \
    "v_mfma_f32_32x32x16_bf16 v[16:31], v[60:63], v[52:55], v[16:31]\n\t" \
    "ds_read_b128 v[72:75], v96 offset:10240\n\t" \
    "v_exp_f32 v40, v40\n\t" \
    "v_exp_f32 v41, v41\n\t" \
    "v_exp_f32 v42, v42\n\t" \
    "v_exp_f32 v43, v43\n\t" \
    "v_cvt_pk_bf16_f32 v92, v40, v41\n\t" \
    "v_cvt_pk_bf16_f32 v93, v42, v43\n\t" \
    "v_mfma_f32_32x32x16_bf16 v[0:15], v[80:83], v[88:91], v[0:15]\n\t" \
    "ds_read_b128 v[76:79], v96 offset:11264\n\t" \
    "v_exp_f32 v44, v44\n\t" \
    "v_exp_f32 v45, v45\n\t" \
    "v_exp_f32 v46, v46\n\t" \
    "v_exp_f32 v47, v47\n\t" \
    "v_cvt_pk_bf16_f32 v94, v44, v45\n\t" \
    "v_cvt_pk_bf16_f32 v95, v46, v47\n\t" \
    "s_waitcnt vmcnt(4)\n\t" \
    "s_barrier\n\t" \
    "s_add_u32 m0, s42, 98304\n\t" \
    "s_add_u32 s40, s40, 16384\n\t" \
    "s_addc_u32 s41, s41, 0\n\t" \
    "global_load_lds_dwordx4 v98, s[40:41] offset:0\n\t" \
    "global_load_lds_dwordx4 v98, s[40:41] offset:1024\n\t" \
    "global_load_lds_dwordx4 v98, s[40:41] offset:2048\n\t" \
    "global_load_lds_dwordx4 v98, s[40:41] offset:3072\n\t" \
    "v_exp_f32 v16, v16\n\t" \
    "v_exp_f32 v17, v17\n\t" \
    "v_mfma_f32_32x32x16_bf16 v[0:15], v[84:87], v[92:95], v[0:15]\n\t" \
    "ds_read_b128 v[56:59], v96 offset:32768\n\t" \
    "v_exp_f32 v18, v18\n\t" \
    "v_exp_f32 v19, v19\n\t" \
    "v_cvt_pk_bf16_f32 v88, v16, v17\n\t" \
    "v_cvt_pk_bf16_f32 v89, v18, v19\n\t" \
    "s_waitcnt lgkmcnt(2)\n\t" \
    "v_mfma_f32_32x32x16_bf16 v[32:47], v[64:67], v[48:51], 0\n\t" \
    "ds_read_b128 v[60:63], v96 offset:33792\n\t" \
    "v_exp_f32 v20, v20\n\t" \
    "v_exp_f32 v21, v21\n\t" \
    "v_exp_f32 v22, v22\n\t" \
    "v_exp_f32 v23, v23\n\t" \
    "v_cvt_pk_bf16_f32 v90, v20, v21\n\t" \
    "v_cvt_pk_bf16_f32 v91, v22, v23\n\t" \
    "v_mfma_f32_32x32x16_bf16 v[32:47], v[68:71], v[52:55], v[32:47]\n\t" \
    "ds_read_b128 v[80:83], v96 offset:14336\n\t" \
    "v_exp_f32 v24, v24\n\t" \
    "v_exp_f32 v25, v25\n\t" \
    "v_exp_f32 v26, v26\n\t" \
    "v_exp_f32 v27, v27\n\t" \
    "v_cvt_pk_bf16_f32 v92, v24, v25\n\t" \
    "v_cvt_pk_bf16_f32 v93, v26, v27\n\t" \
    "v_mfma_f32_32x32x16_bf16 v[0:15], v[72:75], v[88:91], v[0:15]\n\t" \
    "ds_read_b128 v[84:87], v96 offset:15360\n\t" \
    "v_exp_f32 v28, v28\n\t" \
    "v_exp_f32 v29, v29\n\t" \
    "v_exp_f32 v30, v30\n\t" \
    "v_exp_f32 v31, v31\n\t" \
    "v_cvt_pk_bf16_f32 v94, v28, v29\n\t" \
    "v_cvt_pk_bf16_f32 v95, v30, v31\n\t" \
    "v_exp_f32 v32, v32\n\t" \
    "v_exp_f32 v33, v33\n\t" \
    "v_mfma_f32_32x32x16_bf16 v[0:15], v[76:79], v[92:95], v[0:15]\n\t" \
    "ds_read_b128 v[64:67], v96 offset:36864\n\t" \
    "v_exp_f32 v34, v34\n\t" \
    "v_exp_f32 v35, v35\n\t" \
    "v_cvt_pk_bf16_f32 v88, v32, v33\n\t" \
    "v_cvt_pk_bf16_f32 v89, v34, v35\n\t" \
    "s_waitcnt lgkmcnt(2)\n\t" \
    "v_mfma_f32_32x32x16_bf16 v[16:31], v[56:59], v[48:51], 0\n\t" \
    "ds_read_b128 v[68:71], v96 offset:37888\n\t" \
    "v_exp_f32 v36, v36\n\t" \
    "v_exp_f32 v37, v37\n\t" \
    "v_exp_f32 v38, v38\n\t" \
    "v_exp_f32 v39, v39\n\t" \
    "v_cvt_pk_bf16_f32 v90, v36, v37\n\t" \
    "v_cvt_pk_bf16_f32 v91, v38, v39\n\t" \
    "v_mfma_f32_32x32x16_bf16 v[16:31], v[60:63], v[52:55], v[16:31]\n\t" \
    "ds_read_b128 v[72:75], v96 offset:34816\n\t" \
    "v_exp_f32 v40, v40\n\t" \
    "v_exp_f32 v41, v41\n\t" \
    "v_exp_f32 v42, v42\n\t" \
    "v_exp_f32 v43, v43\n\t" \
    "v_cvt_pk_bf16_f32 v92, v40, v41\n\t" \
    "v_cvt_pk_bf16_f32 v93, v42, v43\n\t" \
    "v_mfma_f32_32x32x16_bf16 v[0:15], v[80:83], v[88:91], v[0:15]\n\t" \
    "ds_read_b128 v[76:79], v96 offset:35840\n\t" \
    "v_exp_f32 v44, v44\n\t" \
    "v_exp_f32 v45, v45\n\t" \
    "v_exp_f32 v46, v46\n\t" \
    "v_exp_f32 v47, v47\n\t" \
    "v_cvt_pk_bf16_f32 v94, v44, v45\n\t" \
    "v_cvt_pk_bf16_f32 v95, v46, v47\n\t" \
    "v_exp_f32 v16, v16\n\t" \
    "v_exp_f32 v17, v17\n\t" \
    "v_mfma_f32_32x32x16_bf16 v[0:15], v[84:87], v[92:95], v[0:15]\n\t" \
    "ds_read_b128 v[56:59], v96 offset:40960\n\t" \
    "v_exp_f32 v18, v18\n\t" \
    "v_exp_f32 v19, v19\n\t" \
    "v_cvt_pk_bf16_f32 v88, v16, v17\n\t" \
    "v_cvt_pk_bf16_f32 v89, v18, v19\n\t" \
    "s_waitcnt lgkmcnt(2)\n\t" \
    "v_mfma_f32_32x32x16_bf16 v[32:47], v[64:67], v[48:51], 0\n\t" \
    "ds_read_b128 v[60:63], v96 offset:41984\n\t" \
    "v_exp_f32 v20, v20\n\t" \
    "v_exp_f32 v21, v21\n\t" \
    "v_exp_f32 v22, v22\n\t" \
    "v_exp_f32 v23, v23\n\t" \
    "v_cvt_pk_bf16_f32 v90, v20, v21\n\t" \
    "v_cvt_pk_bf16_f32 v91, v22, v23\n\t" \
    "v_mfma_f32_32x32x16_bf16 v[32:47], v[68:71], v[52:55], v[32:47]\n\t" \
    "ds_read_b128 v[80:83], v96 offset:38912\n\t" \
    "v_exp_f32 v24, v24\n\t" \
    "v_exp_f32 v25, v25\n\t" \
    "v_exp_f32 v26, v26\n\t" \
    "v_exp_f32 v27, v27\n\t" \
    "v_cvt_pk_bf16_f32 v92, v24, v25\n\t" \
    "v_cvt_pk_bf16_f32 v93, v26, v27\n\t" \
    "v_mfma_f32_32x32x16_bf16 v[0:15], v[72:75], v[88:91], v[0:15]\n\t" \
    "ds_read_b128 v[84:87], v96 offset:39936\n\t" \
    "v_exp_f32 v28, v28\n\t" \
    "v_exp_f32 v29, v29\n\t" \
    "v_exp_f32 v30, v30\n\t" \
    "v_exp_f32 v31, v31\n\t" \
    "v_cvt_pk_bf16_f32 v94, v28, v29\n\t" \
    "v_cvt_pk_bf16_f32 v95, v30, v31\n\t" \
    "v_exp_f32 v32, v32\n\t" \
    "v_exp_f32 v33, v33\n\t" \
    "v_mfma_f32_32x32x16_bf16 v[0:15], v[76:79], v[92:95], v[0:15]\n\t" \
    "ds_read_b128 v[64:67], v96 offset:45056\n\t" \
    "v_exp_f32 v34, v34\n\t" \
    "v_exp_f32 v35, v35\n\t" \
    "v_cvt_pk_bf16_f32 v88, v32, v33\n\t" \
    "v_cvt_pk_bf16_f32 v89, v34, v35\n\t" \
    "s_waitcnt lgkmcnt(2)\n\t" \
    "v_mfma_f32_32x32x16_bf16 v[16:31], v[56:59], v[48:51], 0\n\t" \
    "ds_read_b128 v[68:71], v96 offset:46080\n\t" \
    "v_exp_f32 v36, v36\n\t" \
    "v_exp_f32 v37, v37\n\t" \
    "v_exp_f32 v38, v38\n\t" \
    "v_exp_f32 v39, v39\n\t" \
    "v_cvt_pk_bf16_f32 v90, v36, v37\n\t" \
    "v_cvt_pk_bf16_f32 v91, v38, v39\n\t" \
    "v_mfma_f32_32x32x16_bf16 v[16:31], v[60:63], v[52:55], v[16:31]\n\t" \
    "ds_read_b128 v[72:75], v96 offset:43008\n\t" \
    "v_exp_f32 v40, v40\n\t" \
    "v_exp_f32 v41, v41\n\t" \
    "v_exp_f32 v42, v42\n\t" \
    "v_exp_f32 v43, v43\n\t" \
    "v_cvt_pk_bf16_f32 v92, v40, v41\n\t" \
    "v_cvt_pk_bf16_f32 v93, v42, v43\n\t" \
    "v_mfma_f32_32x32x16_bf16 v[0:15], v[80:83], v[88:91], v[0:15]\n\t" \
    "ds_read_b128 v[76:79], v96 offset:44032\n\t" \
    "v_exp_f32 v44, v44\n\t" \
    "v_exp_f32 v45, v45\n\t" \
    "v_exp_f32 v46, v46\n\t" \
    "v_exp_f32 v47, v47\n\t" \
    "v_cvt_pk_bf16_f32 v94, v44, v45\n\t" \
    "v_cvt_pk_bf16_f32 v95, v46, v47\n\t" \
    "s_waitcnt vmcnt(4)\n\t" \
    "s_barrier\n\t" \
    "v_exp_f32 v16, v16\n\t" \
    "v_exp_f32 v17, v17\n\t" \
    "v_mfma_f32_32x32x16_bf16 v[0:15], v[84:87], v[92:95], v[0:15]\n\t" \
    "ds_read_b128 v[56:59], v97 offset:0\n\t" \
    "v_exp_f32 v18, v18\n\t" \
    "v_exp_f32 v19, v19\n\t" \
    "v_cvt_pk_bf16_f32 v88, v16, v17\n\t" \
    "v_cvt_pk_bf16_f32 v89, v18, v19\n\t" \
    "s_waitcnt lgkmcnt(2)\n\t" \
    "v_mfma_f32_32x32x16_bf16 v[32:47], v[64:67], v[48:51], 0\n\t" \
    "ds_read_b128 v[60:63], v97 offset:1024\n\t" \
    "v_exp_f32 v20, v20\n\t" \
    "v_exp_f32 v21, v21\n\t" \
    "v_exp_f32 v22, v22\n\t" \
    "v_exp_f32 v23, v23\n\t" \
    "v_cvt_pk_bf16_f32 v90, v20, v21\n\t" \
    "v_cvt_pk_bf16_f32 v91, v22, v23\n\t" \
    "v_mfma_f32_32x32x16_bf16 v[32:47], v[68:71], v[52:55], v[32:47]\n\t" \
    "ds_read_b128 v[80:83], v96 offset:47104\n\t" \
    "v_exp_f32 v24, v24\n\t" \
    "v_exp_f32 v25, v25\n\t" \
    "v_exp_f32 v26, v26\n\t" \
    "v_exp_f32 v27, v27\n\t" \
    "v_cvt_pk_bf16_f32 v92, v24, v25\n\t" \
    "v_cvt_pk_bf16_f32 v93, v26, v27\n\t" \
    "v_mfma_f32_32x32x16_bf16 v[0:15], v[72:75], v[88:91], v[0:15]\n\t" \
    "ds_read_b128 v[84:87], v96 offset:48128\n\t" \
    "v_exp_f32 v28, v28\n\t" \
    "v_exp_f32 v29, v29\n\t" \
    "v_exp_f32 v30, v30\n\t" \
    "v_exp_f32 v31, v31\n\t" \
    "v_cvt_pk_bf16_f32 v94, v28, v29\n\t" \
    "v_cvt_pk_bf16_f32 v95, v30, v31\n\t" \
    "v_exp_f32 v32, v32\n\t" \
    "v_exp_f32 v33, v33\n\t" \
    "v_mfma_f32_32x32x16_bf16 v[0:15], v[76:79], v[92:95], v[0:15]\n\t" \
    "ds_read_b128 v[64:67], v97 offset:4096\n\t" \
    "v_exp_f32 v34, v34\n\t" \
    "v_exp_f32 v35, v35\n\t" \
    "v_cvt_pk_bf16_f32 v88, v32, v33\n\t" \
    "v_cvt_pk_bf16_f32 v89, v34, v35\n\t" \
    "s_waitcnt lgkmcnt(2)\n\t" \
    "v_mfma_f32_32x32x16_bf16 v[16:31], v[56:59], v[48:51], 0\n\t" \
    "ds_read_b128 v[68:71], v97 offset:5120\n\t" \
    "v_exp_f32 v36, v36\n\t" \
    "v_exp_f32 v37, v37\n\t" \
    "v_exp_f32 v38, v38\n\t" \
    "v_exp_f32 v39, v39\n\t" \
    "v_cvt_pk_bf16_f32 v90, v36, v37\n\t" \
    "v_cvt_pk_bf16_f32 v91, v38, v39\n\t" \
    "v_mfma_f32_32x32x16_bf16 v[16:31], v[60:63], v[52:55], v[16:31]\n\t" \
    "ds_read_b128 v[72:75], v97 offset:2048\n\t" \
    "v_exp_f32 v40, v40\n\t" \
    "v_exp_f32 v41, v41\n\t" \
    "v_exp_f32 v42, v42\n\t" \
    "v_exp_f32 v43, v43\n\t" \
    "v_cvt_pk_bf16_f32 v92, v40, v41\n\t" \
    "v_cvt_pk_bf16_f32 v93, v42, v43\n\t" \
    "v_mfma_f32_32x32x16_bf16 v[0:15], v[80:83], v[88:91], v[0:15]\n\t" \
    "ds_read_b128 v[76:79], v97 offset:3072\n\t" \
    "v_exp_f32 v44, v44\n\t" \
    "v_exp_f32 v45, v45\n\t" \
    "v_exp_f32 v46, v46\n\t" \
    "v_exp_f32 v47, v47\n\t" \
    "v_cvt_pk_bf16_f32 v94, v44, v45\n\t" \
    "v_cvt_pk_bf16_f32 v95, v46, v47\n\t" \
    "v_exp_f32 v16, v16\n\t" \
    "v_exp_f32 v17, v17\n\t" \
    "v_mfma_f32_32x32x16_bf16 v[0:15], v[84:87], v[92:95], v[0:15]\n\t" \
    "ds_read_b128 v[56:59], v97 offset:8192\n\t" \
    "v_exp_f32 v18, v18\n\t" \
    "v_exp_f32 v19, v19\n\t" \
    "v_cvt_pk_bf16_f32 v88, v16, v17\n\t" \
    "v_cvt_pk_bf16_f32 v89, v18, v19\n\t" \
    "s_waitcnt lgkmcnt(2)\n\t" \
    "v_mfma_f32_32x32x16_bf16 v[32:47], v[64:67], v[48:51], 0\n\t" \
    "ds_read_b128 v[60:63], v97 offset:9216\n\t" \
    "v_exp_f32 v20, v20\n\t" \
    "v_exp_f32 v21, v21\n\t" \
    "v_exp_f32 v22, v22\n\t" \
    "v_exp_f32 v23, v23\n\t" \
    "v_cvt_pk_bf16_f32 v90, v20, v21\n\t" \
    "v_cvt_pk_bf16_f32 v91, v22, v23\n\t" \
    "v_mfma_f32_32x32x16_bf16 v[32:47], v[68:71], v[52:55], v[32:47]\n\t" \
    "ds_read_b128 v[80:83], v97 offset:6144\n\t" \
    "v_exp_f32 v24, v24\n\t" \
    "v_exp_f32 v25, v25\n\t" \
    "v_exp_f32 v26, v26\n\t" \
    "v_exp_f32 v27, v27\n\t" \
    "v_cvt_pk_bf16_f32 v92, v24, v25\n\t" \
    "v_cvt_pk_bf16_f32 v93, v26, v27\n\t" \
    "v_mfma_f32_32x32x16_bf16 v[0:15], v[72:75], v[88:91], v[0:15]\n\t" \
    "ds_read_b128 v[84:87], v97 offset:7168\n\t" \
    "v_exp_f32 v28, v28\n\t" \
    "v_exp_f32 v29, v29\n\t" \
    "v_exp_f32 v30, v30\n\t" \
    "v_exp_f32 v31, v31\n\t" \
    "v_cvt_pk_bf16_f32 v94, v28, v29\n\t" \
    "v_cvt_pk_bf16_f32 v95, v30, v31\n\t" \
    "v_exp_f32 v32, v32\n\t" \
    "v_exp_f32 v33, v33\n\t" \
    "v_mfma_f32_32x32x16_bf16 v[0:15], v[76:79], v[92:95], v[0:15]\n\t" \
    "ds_read_b128 v[64:67], v97 offset:12288\n\t" \
    "v_exp_f32 v34, v34\n\t" \
    "v_exp_f32 v35, v35\n\t" \
    "v_cvt_pk_bf16_f32 v88, v32, v33\n\t" \
    "v_cvt_pk_bf16_f32 v89, v34, v35\n\t" \
    "s_waitcnt lgkmcnt(2)\n\t" \
    "v_mfma_f32_32x32x16_bf16 v[16:31], v[56:59], v[48:51], 0\n\t" \
    "ds_read_b128 v[68:71], v97 offset:13312\n\t" \
    "v_exp_f32 v36, v36\n\t" \
    "v_exp_f32 v37, v37\n\t" \
    "v_exp_f32 v38, v38\n\t" \
    "v_exp_f32 v39, v39\n\t" \
    "v_cvt_pk_bf16_f32 v90, v36, v37\n\t" \
    "v_cvt_pk_bf16_f32 v91, v38, v39\n\t" \
    "v_mfma_f32_32x32x16_bf16 v[16:31], v[60:63], v[52:55], v[16:31]\n\t" \
    "ds_read_b128 v[72:75], v97 offset:10240\n\t" \
    "v_exp_f32 v40, v40\n\t" \
    "v_exp_f32 v41, v41\n\t" \
    "v_exp_f32 v42, v42\n\t" \
    "v_exp_f32 v43, v43\n\t" \
    "v_cvt_pk_bf16_f32 v92, v40, v41\n\t" \
    "v_cvt_pk_bf16_f32 v93, v42, v43\n\t" \
    "v_mfma_f32_32x32x16_bf16 v[0:15], v[80:83], v[88:91], v[0:15]\n\t" \
    "ds_read_b128 v[76:79], v97 offset:11264\n\t" \
    "v_exp_f32 v44, v44\n\t" \
    "v_exp_f32 v45, v45\n\t" \
    "v_exp_f32 v46, v46\n\t" \
    "v_exp_f32 v47, v47\n\t" \
    "v_cvt_pk_bf16_f32 v94, v44, v45\n\t" \
    "v_cvt_pk_bf16_f32 v95, v46, v47\n\t" \
    "s_waitcnt vmcnt(0)\n\t" \
    "s_barrier\n\t" \
    "v_exp_f32 v16, v16\n\t" \
    "v_exp_f32 v17, v17\n\t" \
    "v_mfma_f32_32x32x16_bf16 v[0:15], v[84:87], v[92:95], v[0:15]\n\t" \
    "ds_read_b128 v[56:59], v97 offset:32768\n\t" \
    "v_exp_f32 v18, v18\n\t" \
    "v_exp_f32 v19, v19\n\t" \
    "v_cvt_pk_bf16_f32 v88, v16, v17\n\t" \
    "v_cvt_pk_bf16_f32 v89, v18, v19\n\t" \
    "s_waitcnt lgkmcnt(2)\n\t" \
    "v_mfma_f32_32x32x16_bf16 v[32:47], v[64:67], v[48:51], 0\n\t" \
    "ds_read_b128 v[60:63], v97 offset:33792\n\t" \
    "v_exp_f32 v20, v20\n\t" \
    "v_exp_f32 v21, v21\n\t" \
    "v_exp_f32 v22, v22\n\t" \
    "v_exp_f32 v23, v23\n\t" \
    "v_cvt_pk_bf16_f32 v90, v20, v21\n\t" \
    "v_cvt_pk_bf16_f32 v91, v22, v23\n\t" \
    "v_mfma_f32_32x32x16_bf16 v[32:47], v[68:71], v[52:55], v[32:47]\n\t" \
    "ds_read_b128 v[80:83], v97 offset:14336\n\t" \
    "v_exp_f32 v24, v24\n\t" \
    "v_exp_f32 v25, v25\n\t" \
    "v_exp_f32 v26, v26\n\t" \
    "v_exp_f32 v27, v27\n\t" \
    "v_cvt_pk_bf16_f32 v92, v24, v25\n\t" \
    "v_cvt_pk_bf16_f32 v93, v26, v27\n\t" \
    "v_mfma_f32_32x32x16_bf16 v[0:15], v[72:75], v[88:91], v[0:15]\n\t" \
    "ds_read_b128 v[84:87], v97 offset:15360\n\t" \
    "v_exp_f32 v28, v28\n\t" \
    "v_exp_f32 v29, v29\n\t" \
    "v_exp_f32 v30, v30\n\t" \
    "v_exp_f32 v31, v31\n\t" \
    "v_cvt_pk_bf16_f32 v94, v28, v29\n\t" \
    "v_cvt_pk_bf16_f32 v95, v30, v31\n\t" \
    "v_exp_f32 v32, v32\n\t" \
    "v_exp_f32 v33, v33\n\t" \
    "v_mfma_f32_32x32x16_bf16 v[0:15], v[76:79], v[92:95], v[0:15]\n\t" \
    "ds_read_b128 v[64:67], v97 offset:36864\n\t" \
    "v_exp_f32 v34, v34\n\t" \
    "v_exp_f32 v35, v35\n\t" \
    "v_cvt_pk_bf16_f32 v88, v32, v33\n\t" \
    "v_cvt_pk_bf16_f32 v89, v34, v35\n\t" \
    "s_waitcnt lgkmcnt(2)\n\t" \
    "v_mfma_f32_32x32x16_bf16 v[16:31], v[56:59], v[48:51], 0\n\t" \
    "ds_read_b128 v[68:71], v97 offset:37888\n\t" \
    "v_exp_f32 v36, v36\n\t" \
    "v_exp_f32 v37, v37\n\t" \
    "v_exp_f32 v38, v38\n\t" \
    "v_exp_f32 v39, v39\n\t" \
    "v_cvt_pk_bf16_f32 v90, v36, v37\n\t" \
    "v_cvt_pk_bf16_f32 v91, v38, v39\n\t" \
    "v_mfma_f32_32x32x16_bf16 v[16:31], v[60:63], v[52:55], v[16:31]\n\t" \
    "ds_read_b128 v[72:75], v97 offset:34816\n\t" \
    "v_exp_f32 v40, v40\n\t" \
    "v_exp_f32 v41, v41\n\t" \
    "v_exp_f32 v42, v42\n\t" \
    "v_exp_f32 v43, v43\n\t" \
    "v_cvt_pk_bf16_f32 v92, v40, v41\n\t" \
    "v_cvt_pk_bf16_f32 v93, v42, v43\n\t" \
    "v_mfma_f32_32x32x16_bf16 v[0:15], v[80:83], v[88:91], v[0:15]\n\t" \
    "ds_read_b128 v[76:79], v97 offset:35840\n\t" \
    "v_exp_f32 v44, v44\n\t" \
    "v_exp_f32 v45, v45\n\t" \
    "v_exp_f32 v46, v46\n\t" \
    "v_exp_f32 v47, v47\n\t" \
    "v_cvt_pk_bf16_f32 v94, v44, v45\n\t" \
    "v_cvt_pk_bf16_f32 v95, v46, v47\n\t" \
    "v_exp_f32 v16, v16\n\t" \
    "v_exp_f32 v17, v17\n\t" \
    "v_mfma_f32_32x32x16_bf16 v[0:15], v[84:87], v[92:95], v[0:15]\n\t" \
    "ds_read_b128 v[56:59], v97 offset:40960\n\t" \
    "v_exp_f32 v18, v18\n\t" \
    "v_exp_f32 v19, v19\n\t" \
    "v_cvt_pk_bf16_f32 v88, v16, v17\n\t" \
    "v_cvt_pk_bf16_f32 v89, v18, v19\n\t" \
    "s_waitcnt lgkmcnt(2)\n\t" \
    "v_mfma_f32_32x32x16_bf16 v[32:47], v[64:67], v[48:51], 0\n\t" \
    "ds_read_b128 v[60:63], v97 offset:41984\n\t" \
    "v_exp_f32 v20, v20\n\t" \
    "v_exp_f32 v21, v21\n\t" \
    "v_exp_f32 v22, v22\n\t" \
    "v_exp_f32 v23, v23\n\t" \
    "v_cvt_pk_bf16_f32 v90, v20, v21\n\t" \
    "v_cvt_pk_bf16_f32 v91, v22, v23\n\t" \
    "v_mfma_f32_32x32x16_bf16 v[32:47], v[68:71], v[52:55], v[32:47]\n\t" \
    "ds_read_b128 v[80:83], v97 offset:38912\n\t" \
    "v_exp_f32 v24, v24\n\t" \
    "v_exp_f32 v25, v25\n\t" \
    "v_exp_f32 v26, v26\n\t" \
    "v_exp_f32 v27, v27\n\t" \
    "v_cvt_pk_bf16_f32 v92, v24, v25\n\t" \
    "v_cvt_pk_bf16_f32 v93, v26, v27\n\t" \
    "v_mfma_f32_32x32x16_bf16 v[0:15], v[72:75], v[88:91], v[0:15]\n\t" \
    "ds_read_b128 v[84:87], v97 offset:39936\n\t" \
    "v_exp_f32 v28, v28\n\t" \
    "v_exp_f32 v29, v29\n\t" \
    "v_exp_f32 v30, v30\n\t" \
    "v_exp_f32 v31, v31\n\t" \
    "v_cvt_pk_bf16_f32 v94, v28, v29\n\t" \
    "v_cvt_pk_bf16_f32 v95, v30, v31\n\t" \
    "v_exp_f32 v32, v32\n\t" \
    "v_exp_f32 v33, v33\n\t" \
    "v_mfma_f32_32x32x16_bf16 v[0:15], v[76:79], v[92:95], v[0:15]\n\t" \
    "ds_read_b128 v[64:67], v97 offset:45056\n\t" \
    "v_exp_f32 v34, v34\n\t" \
    "v_exp_f32 v35, v35\n\t" \
    "v_cvt_pk_bf16_f32 v88, v32, v33\n\t" \
    "v_cvt_pk_bf16_f32 v89, v34, v35\n\t" \
    "s_waitcnt lgkmcnt(2)\n\t" \
    "v_mfma_f32_32x32x16_bf16 v[16:31], v[56:59], v[48:51], 0\n\t" \
    "ds_read_b128 v[68:71], v97 offset:46080\n\t" \
    "v_exp_f32 v36, v36\n\t" \
    "v_exp_f32 v37, v37\n\t" \
    "v_exp_f32 v38, v38\n\t" \
    "v_exp_f32 v39, v39\n\t" \
    "v_cvt_pk_bf16_f32 v90, v36, v37\n\t" \
    "v_cvt_pk_bf16_f32 v91, v38, v39\n\t" \
    "v_mfma_f32_32x32x16_bf16 v[16:31], v[60:63], v[52:55], v[16:31]\n\t" \
    "ds_read_b128 v[72:75], v97 offset:43008\n\t" \
    "v_exp_f32 v40, v40\n\t" \
    "v_exp_f32 v41, v41\n\t" \
    "v_exp_f32 v42, v42\n\t" \
    "v_exp_f32 v43, v43\n\t" \
    "v_cvt_pk_bf16_f32 v92, v40, v41\n\t" \
    "v_cvt_pk_bf16_f32 v93, v42, v43\n\t" \
    "v_mfma_f32_32x32x16_bf16 v[0:15], v[80:83], v[88:91], v[0:15]\n\t" \
    "ds_read_b128 v[76:79], v97 offset:44032\n\t" \
    "v_exp_f32 v44, v44\n\t" \
    "v_exp_f32 v45, v45\n\t" \
    "v_exp_f32 v46, v46\n\t" \
    "v_exp_f32 v47, v47\n\t" \
    "v_cvt_pk_bf16_f32 v94, v44, v45\n\t" \
    "v_cvt_pk_bf16_f32 v95, v46, v47\n\t" \
    "v_exp_f32 v16, v16\n\t" \
    "v_exp_f32 v17, v17\n\t" \
    "v_mfma_f32_32x32x16_bf16 v[0:15], v[84:87], v[92:95], v[0:15]\n\t" \
    "v_exp_f32 v18, v18\n\t" \
    "v_exp_f32 v19, v19\n\t" \
    "v_cvt_pk_bf16_f32 v88, v16, v17\n\t" \
    "v_cvt_pk_bf16_f32 v89, v18, v19\n\t" \
    "s_waitcnt lgkmcnt(1)\n\t" \
    "v_mfma_f32_32x32x16_bf16 v[32:47], v[64:67], v[48:51], 0\n\t" \
    "v_exp_f32 v20, v20\n\t" \
    "v_exp_f32 v21, v21\n\t" \
    "v_exp_f32 v22, v22\n\t" \
    "v_exp_f32 v23, v23\n\t" \
    "v_cvt_pk_bf16_f32 v90, v20, v21\n\t" \
    "v_cvt_pk_bf16_f32 v91, v22, v23\n\t" \
    "v_mfma_f32_32x32x16_bf16 v[32:47], v[68:71], v[52:55], v[32:47]\n\t" \
    "ds_read_b128 v[80:83], v97 offset:47104\n\t" \
    "v_exp_f32 v24, v24\n\t" \
    "v_exp_f32 v25, v25\n\t" \
    "v_exp_f32 v26, v26\n\t" \
    "v_exp_f32 v27, v27\n\t" \
    "v_cvt_pk_bf16_f32 v92, v24, v25\n\t" \
    "v_cvt_pk_bf16_f32 v93, v26, v27\n\t" \
    "v_mfma_f32_32x32x16_bf16 v[0:15], v[72:75], v[88:91], v[0:15]\n\t" \
    "ds_read_b128 v[84:87], v97 offset:48128\n\t" \
    "v_exp_f32 v28, v28\n\t" \
    "v_exp_f32 v29, v29\n\t" \
    "v_exp_f32 v30, v30\n\t" \
    "v_exp_f32 v31, v31\n\t" \
    "v_cvt_pk_bf16_f32 v94, v28, v29\n\t" \
    "v_cvt_pk_bf16_f32 v95, v30, v31\n\t" \
    "v_exp_f32 v32, v32\n\t" \
    "v_exp_f32 v33, v33\n\t" \
    "v_mfma_f32_32x32x16_bf16 v[0:15], v[76:79], v[92:95], v[0:15]\n\t" \
    "v_exp_f32 v34, v34\n\t" \
    "v_exp_f32 v35, v35\n\t" \
    "v_cvt_pk_bf16_f32 v88, v32, v33\n\t" \
    "v_cvt_pk_bf16_f32 v89, v34, v35\n\t" \
    "s_waitcnt lgkmcnt(1)\n\t" \
    "v_exp_f32 v36, v36\n\t" \
    "v_exp_f32 v37, v37\n\t" \
    "v_exp_f32 v38, v38\n\t" \
    "v_exp_f32 v39, v39\n\t" \
    "v_cvt_pk_bf16_f32 v90, v36, v37\n\t" \
    "v_cvt_pk_bf16_f32 v91, v38, v39\n\t" \
    "v_exp_f32 v40, v40\n\t" \
    "v_exp_f32 v41, v41\n\t" \
    "v_exp_f32 v42, v42\n\t" \
    "v_exp_f32 v43, v43\n\t" \
    "v_cvt_pk_bf16_f32 v92, v40, v41\n\t" \
    "v_cvt_pk_bf16_f32 v93, v42, v43\n\t" \
    "v_mfma_f32_32x32x16_bf16 v[0:15], v[80:83], v[88:91], v[0:15]\n\t" \
    "v_exp_f32 v44, v44\n\t" \
    "v_exp_f32 v45, v45\n\t" \
    "v_exp_f32 v46, v46\n\t" \
    "v_exp_f32 v47, v47\n\t" \
    "v_cvt_pk_bf16_f32 v94, v44, v45\n\t" \
    "v_cvt_pk_bf16_f32 v95, v46, v47\n\t" \
    "s_waitcnt lgkmcnt(0)\n\t" \
    "s_nop 0\n\t" \
    "v_mfma_f32_32x32x16_bf16 v[0:15], v[84:87], v[92:95], v[0:15]\n\t" \
    "s_mov_b32 m0, s43\n\t" \
    "s_nop 7\n\t" \
    "s_nop 7\n\t"
#define ATTN_FAST_CLOBBERS "v16", "v17", "v18", "v19", "v20", "v21", "v22", "v23", "v24", "v25", "v26", "v27", "v28", "v29", "v30", "v31", "v32", "v33", "v34", "v35", "v36", "v37", "v38", "v39", "v40", "v41", "v42", "v43", "v44", "v45", "v46", "v47", "v56", "v57", "v58", "v59", "v60", "v61", "v62", "v63", "v64", "v65", "v66", "v67", "v68", "v69", "v70", "v71", "v72", "v73", "v74", "v75", "v76", "v77", "v78", "v79", "v80", "v81", "v82", "v83", "v84", "v85", "v86", "v87", "v88", "v89", "v90", "v91", "v92", "v93", "v94", "v95", "s43", "scc", "memory"

constexpr int A_SLOT = 28672;
constexpr int A_XS = 3 * A_SLOT;
constexpr int A_XROW = 464;
constexpr int L_Q = A_XS + 128 * A_XROW;
constexpr int L_CTL = L_Q + 8192;
constexpr int L_TOTAL = L_CTL + 256;
constexpr int B_RED = 131072;
constexpr int C_RROW = 272;

DI float gelu_tanh(float h) {
    const float c0 = -2.0f * 0.7978845608028654f * 1.4426950408889634f, c1 = c0 * 0.044715f;
    const float z = h * __builtin_fmaf(h * h, c1, c0);
    const float e = __builtin_amdgcn_exp2f(z);
    return h * __builtin_amdgcn_rcpf(1.0f + e);
}

DI void phaseA(const char* __restrict__ x, const char* __restrict__ wqkv, const char* __restrict__ w1, const char* __restrict__ b1,
               char* __restrict__ ws, char* lds_generic, int Rb, int c, u64& tag) {
    LAS char* lds = (LAS char*)lds_generic;
    const unsigned lds0 = (unsigned)(uintptr_t)lds_generic;
    const int tid = threadIdx.x, l = tid & 63, w = rfl(tid >> 6) & 7, r = l & 31, h = l >> 5;
    const int tt = w & 3, half = w >> 2;
    STAMP(ws, 0);
    u32x2 e0; { const unsigned z = 0; asm volatile("global_load_dwordx2 %0, %1, %2 sc1" : "=v"(e0) : "v"(z), "s"(uptr(ws + WS_EPOCH)) : "memory"); }
    u32x4 xr[2][7];
    {
        const char* xb = x + (size_t)(Rb * 128 + 16 * w) * 896;
        const unsigned vo = (l >> 3) * 896 + (l & 7) * 16;
#pragma unroll
        for (int g = 0; g < 2; ++g)
#pragma unroll
            for (int i = 0; i < 7; ++i) xr[g][i] = gload16(xb + g * 8 * 896 + i * 128, vo);
    }
    {
        const unsigned vo = (l >> 2) * 2688 + ((l & 3) < 3 ? (l & 3) * 16 : 40);
#pragma unroll
        for (int i = 0; i < 11; ++i) {
            const int n = w + 8 * i;
            if (n < 84) { const int m = n / 28, kb = n % 28;
                glds16(wqkv + (size_t)kb * 16 * 2688 + m * 896 + c * 56, vo, lds0 + m * A_SLOT + kb * 1024); }
        }
    }
    asm volatile("s_waitcnt vmcnt(10)" : "+v"(e0), "+v"(xr[0][0]), "+v"(xr[0][1]), "+v"(xr[0][2]), "+v"(xr[0][3]), "+v"(xr[0][4]), "+v"(xr[0][5]), "+v"(xr[0][6]),
                 "+v"(xr[1][0]), "+v"(xr[1][1]), "+v"(xr[1][2]), "+v"(xr[1][3]), "+v"(xr[1][4]), "+v"(xr[1][5]), "+v"(xr[1][6]) :: "memory");
    tag = (((u64)rfl(e0[1]) << 32) | rfl(e0[0])) + 0x9E3779B97F4A7C15ull;
    STAMP(ws, 1);
#pragma unroll
    for (int g = 0; g < 2; ++g) {
        float sm = 0.f, sq = 0.f; unsigned ones_ = 0x3f803f80u; asm volatile("" : "+v"(ones_)); const bf16x2 one = asbf2(ones_);
#pragma unroll
        for (int i = 0; i < 7; ++i)
#pragma unroll
            for (int e = 0; e < 4; ++e) { const bf16x2 v = asbf2(xr[g][i][e]); sm = __builtin_amdgcn_fdot2_f32_bf16(v, one, sm, false); sq = __builtin_amdgcn_fdot2_f32_bf16(v, v, sq, false); }
        sm += dppf<0xB1>(sm); sq += dppf<0xB1>(sq); sm += dppf<0x4E>(sm); sq += dppf<0x4E>(sq); sm += dppf<0x141>(sm); sq += dppf<0x141>(sq);
        const float mean = sm * (1.0f / 448.0f); const float var = __builtin_fmaxf(sq * (1.0f / 448.0f) - mean * mean, 0.f);
        const float rstd = __builtin_amdgcn_rsqf(var + 1e-5f), nb = -mean * rstd;
#pragma unroll
        for (int i = 0; i < 7; ++i)
#pragma unroll
            for (int e = 0; e < 4; ++e) { const unsigned v = xr[g][i][e]; xr[g][i][e] = cvtpk(__builtin_fmaf(bflo(v), rstd, nb), __builtin_fmaf(bfhi(v), rstd, nb)); }
    }
    STAMP(ws, 2);
    bf16x8 xf[28];
    {
        LAS char* xs = lds + A_XS; const int sub = l & 7;
        const int wrow = 16 * w + (l >> 3);
        const LAS char* rd = xs + (32 * tt + r) * A_XROW + h * 16;
#pragma unroll
        for (int g = 0; g < 2; ++g) {
#pragma unroll
            for (int i = 0; i < 3; ++i) *(LAS u32x4*)(xs + (wrow + 8 * g) * A_XROW + (sub + 8 * i) * 16) = xr[g][i];
            if (sub < 4) *(LAS u32x4*)(xs + (wrow + 8 * g) * A_XROW + (sub + 24) * 16) = xr[g][3];
        }
        WAIT_LGKM0(); BARRIER();
        STAMP(ws, 15);
#pragma unroll
        for (int s = 0; s < 14; ++s) xf[s] = ldsfrag(rd + s * 32);
        WAIT_LGKM0(); BARRIER();
        STAMP(ws, 16);
#pragma unroll
        for (int g = 0; g < 2; ++g) {
            if (sub >= 4) *(LAS u32x4*)(xs + (wrow + 8 * g) * A_XROW + (sub - 4) * 16) = xr[g][3];
#pragma unroll
            for (int i = 4; i < 7; ++i) *(LAS u32x4*)(xs + (wrow + 8 * g) * A_XROW + (sub + 8 * i - 28) * 16) = xr[g][i];
        }
        WAIT_LGKM0(); BARRIER();
#pragma unroll
        for (int s = 0; s < 14; ++s) xf[14 + s] = ldsfrag(rd + s * 32);
        WAIT_LGKM0();
        STAMP(ws, 17);
        WAIT_VM(0);
        STAMP(ws, 18);
        BARRIER();
    }
    STAMP(ws, 3);
    const bool ffn = c < 14;
    const unsigned vo1 = (l >> 2) * 3584 + (l & 3) * 16;
    if (ffn) {
#pragma unroll
        for (int i = 0; i < 7; ++i) { const int n = w + 8 * i, t = n / 28, kb = n % 28;
            glds16(w1 + (size_t)kb * 16 * 3584 + (4 * c + t) * 64, vo1, lds0 + A_XS + t * A_SLOT + kb * 1024); }
    }
    const int troff = (8 * h + ((l & 15) >> 2)) * 64 + ((l >> 4) & 1) * 32 + (l & 3) * 8;
    const int tglob = Rb * 4 + tt;
    const unsigned vl = l * 16;
    if (half == 0) {
        f32x16 aq = {}, ak = {};
        const LAS char* pq = lds + troff; const LAS char* pk = lds + A_SLOT + troff;
#pragma unroll
        for (int s = 0; s < 28; ++s) {
            aq = MFMA(trfrag(pq + s * 1024, pq + s * 1024 + 256), xf[s], aq);
            ak = MFMA(trfrag(pk + s * 1024, pk + s * 1024 + 256), xf[s], ak);
        }
        const float qs = 0.18898223650461361f * 1.4426950408889634f;
        float nk = 0.f;
#pragma unroll
        for (int i = 0; i < 16; ++i) { const bool pad = (i >= 12) && (h == 0); aq[i] = pad ? 0.f : aq[i] * qs; const float kv = pad ? 0.f : ak[i]; nk = __builtin_fmaf(kv, kv, nk); ak[i] = kv; }
        if (h == 0) ak[12] = 1.0f;
        nk = row32_max(xhalf_sum(nk));
        char* kd = ws + WS_KV + (size_t)(c * 64 + tglob) * 4096;
        gstore16_sc1(kd, vl, pack8(ak, 0)); gstore16_sc1(kd + 1024, vl, pack8(ak, 1));
        if (l == 0) __hip_atomic_store((__attribute__((address_space(1))) unsigned*)(ws + WS_KN + (c * 64 + tglob) * 4), __float_as_uint(nk), __ATOMIC_RELAXED, __HIP_MEMORY_SCOPE_AGENT);
        *(LAS u32x4*)(lds + L_Q + tt * 2048 + vl) = pack8(aq, 0); *(LAS u32x4*)(lds + L_Q + tt * 2048 + 1024 + vl) = pack8(aq, 1);
    } else {
        f32x16 av = {};
        const LAS char* pv = lds + 2 * A_SLOT + troff;
#pragma unroll
        for (int s = 0; s < 28; ++s) av = MFMA(xf[s], trfrag(pv + s * 1024, pv + s * 1024 + 256), av);
        const bool pad = (r >= 24) && (r < 28); const float pv1 = (r == 24) ? 1.0f : 0.f;
#pragma unroll
        for (int i = 0; i < 16; ++i) av[i] = pad ? pv1 : av[i];
        char* vd = ws + WS_KV + (size_t)(c * 64 + tglob) * 4096 + 2048;
        gstore16_sc1(vd, vl, pack8(av, 0)); gstore16_sc1(vd + 1024, vl, pack8(av, 1));
    }
    STAMP(ws, 4);
    WAIT_LGKM0(); WAIT_VM(0); BARRIER();
    if (tid == 0) flag_store(ws, WS_FKV + c * 256 + Rb * 8, tag);
    STAMP(ws, 5);
    if (!ffn) return;
#pragma unroll
    for (int i = 0; i < 7; ++i) { const int n = w + 8 * i, t = n / 28, kb = n % 28;
        glds16(w1 + (size_t)kb * 16 * 3584 + (4 * c + 2 + t) * 64, vo1, lds0 + t * A_SLOT + kb * 1024); }
#pragma unroll
    for (int u = 0; u < 2; ++u) {
        const int ft = 4 * c + 2 * u + half;
        u32x2 bb[4];
#pragma unroll
        for (int g = 0; g < 4; ++g) bb[g] = *(const u32x2*)(b1 + (ft * 32 + 8 * g + 4 * h) * 2);
        if (u == 1) { WAIT_VM(0); BARRIER(); }
        f32x16 a = {};
        const LAS char* pw = lds + (u == 0 ? A_XS : 0) + half * A_SLOT + troff;
#pragma unroll
        for (int s = 0; s < 28; ++s) a = MFMA(trfrag(pw + s * 1024, pw + s * 1024 + 256), xf[s], a);
#pragma unroll
        for (int g = 0; g < 4; ++g) {
            a[4 * g + 0] = gelu_tanh(a[4 * g + 0] + bflo(bb[g][0])); a[4 * g + 1] = gelu_tanh(a[4 * g + 1] + bfhi(bb[g][0]));
            a[4 * g + 2] = gelu_tanh(a[4 * g + 2] + bflo(bb[g][1])); a[4 * g + 3] = gelu_tanh(a[4 * g + 3] + bfhi(bb[g][1]));
        }
        char* hd = ws + WS_H + ((size_t)tglob * 56 + ft) * 2048;
        gstore16_sc1(hd, vl, pack8(a, 0)); gstore16_sc1(hd + 1024, vl, pack8(a, 1));
        STAMP(ws, 6 + u);
    }
    WAIT_VM(0); BARRIER();
    if (tid == 0) flag_store(ws, WS_FH + Rb * 256 + c * 8, tag);
}

#define EX2(v) __builtin_amdgcn_exp2f(v)
DI void attn_safe(f32x16& o, const bf16x8 q0, const bf16x8 q1, const char* src, unsigned dst, unsigned vo, const LAS char* rb, int l) {
    float m = -1e30f;
    const f32x16 zero = {};
    WAIT_VM(0); BARRIER();
    for (int i = 0; i < 8; ++i) {
        const LAS char* sb = rb + (i & 3) * 32768;
#pragma unroll 1
        for (int t = 0; t < 4; ++t) {
            f32x16 sc = MFMA(ldsfrag(sb + t * 4096), q0, zero); sc = MFMA(ldsfrag(sb + t * 4096 + 1024), q1, sc);
            float mx = sc[0];
#pragma unroll
            for (int e = 1; e < 16; ++e) mx = __builtin_fmaxf(mx, sc[e]);
            mx = xhalf_max(mx);
            const float mn = __builtin_fmaxf(m, mx), al = EX2(m - mn); m = mn;
#pragma unroll
            for (int e = 0; e < 16; ++e) { o[e] *= al; sc[e] = EX2(sc[e] - mn); }
            o = MFMA(ldsfrag(sb + t * 4096 + 2048), asbf8(pack8(sc, 0)), o); o = MFMA(ldsfrag(sb + t * 4096 + 3072), asbf8(pack8(sc, 1)), o);
        }
        WAIT_LGKM0(); WAIT_VM(0); BARRIER();
        if (i + 4 < 8) {
#pragma unroll
            for (int e = 0; e < 4; ++e) glds16(src + (size_t)(i + 4) * 16384 + e * 1024, vo, dst + (i & 3) * 32768 + e * 1024);
        }
    }
    o[13] = (l < 32) ? m : o[13];
}

DI void phaseB(char* __restrict__ ws, char* lds_generic, int hd, int qb, u64 tag) {
    LAS char* lds = (LAS char*)lds_generic;
    const unsigned lds0 = (unsigned)(uintptr_t)lds_generic;
    const int tid = threadIdx.x, l = tid & 63, w = rfl(tid >> 6) & 7, h = l >> 5;
    const int qt = w & 3, kh = w >> 2, tglob = qb * 4 + qt;
    STAMP(ws, 10);
    if (w == 0) {
        flag_wait(ws, WS_FKV + hd * 256, 16, tag, l);
        __builtin_amdgcn_fence(__ATOMIC_ACQUIRE, "agent");
        WAIT_VM(0);
    }
    if (tid == 64) *(LAS unsigned*)(lds + L_CTL) = 0u;
    WAIT_LGKM0(); BARRIER();
    STAMP(ws, 11);
    const unsigned vo = l * 16;
    const char* src = ws + WS_KV + (size_t)hd * 262144 + (size_t)kh * 131072 + qt * 4096;
    const unsigned dst = lds0 + kh * 16384 + qt * 4096;
    float kn = *(const float*)(ws + WS_KN + (hd * 64 + l) * 4);
    u32x4 q0 = *(const LAS u32x4*)(lds + L_Q + qt * 2048 + vo), q1 = *(const LAS u32x4*)(lds + L_Q + qt * 2048 + 1024 + vo);
    kn = xhalf_max(row32_max(kn));
    float qn = 0.f;
#pragma unroll
    for (int e = 0; e < 4; ++e) { qn = __builtin_amdgcn_fdot2_f32_bf16(asbf2(q0[e]), asbf2(q0[e]), qn, false); qn = __builtin_amdgcn_fdot2_f32_bf16(asbf2(q1[e]), asbf2(q1[e]), qn, false); }
    qn = xhalf_sum(qn);
    const float bound = __builtin_sqrtf(qn * kn) * 1.02f + 1e-3f;
    if (__any(!(bound <= 48.0f)) && l == 0) *(LAS unsigned*)(lds + L_CTL) = 1u;
#pragma unroll
    for (int i = 0; i < 4; ++i)
#pragma unroll
        for (int e = 0; e < 4; ++e) glds16(src + (size_t)i * 16384 + e * 1024, vo, dst + i * 32768 + e * 1024);
    WAIT_LGKM0(); BARRIER();
    const bool safe = rfl(*(const LAS unsigned*)(lds + L_CTL)) != 0u;
    f32x16 o;
    STAMP(ws, 12); STAMPC(ws, 28);
    if (!safe) {
        if (h == 0) q1[2] = cvtpk(-bound, 0.f);
        const unsigned ad0 = lds0 + kh * 16384 + vo, ad1 = ad0 + 65536;
        const char* s3 = (const char*)uptr(src + 3 * 16384);
        const unsigned dsts = rfl(dst);
        asm volatile(ATTN_FAST_ASM
                     : "={v[0:15]}"(o), "+{s[40:41]}"(s3)
                     : "{v[48:51]}"(q0), "{v[52:55]}"(q1), "{v96}"(ad0), "{v97}"(ad1), "{v98}"(vo), "{s42}"(dsts)
                     : ATTN_FAST_CLOBBERS);
    } else {
        o = (f32x16){};
        attn_safe(o, asbf8(q0), asbf8(q1), src, dst, vo, lds + kh * 16384 + vo, l);
    }
    STAMP(ws, 13); STAMPC(ws, 29);
    LAS char* red = lds + B_RED + qt * 4096 + vo;
    if (kh == 1) {
#pragma unroll
        for (int g = 0; g < 4; ++g) *(LAS f32x4*)(red + g * 1024) = (f32x4){o[4 * g], o[4 * g + 1], o[4 * g + 2], o[4 * g + 3]};
    }
    WAIT_LGKM0(); BARRIER();
    if (kh == 0) {
        f32x16 p;
#pragma unroll
        for (int g = 0; g < 4; ++g) { const f32x4 t = *(const LAS f32x4*)(red + g * 1024); p[4 * g] = t[0]; p[4 * g + 1] = t[1]; p[4 * g + 2] = t[2]; p[4 * g + 3] = t[3]; }
        if (safe) {
            const float ma = swap32_lo(o[13]), mb = swap32_lo(p[13]);
            const float mn = __builtin_fmaxf(ma, mb), fa = EX2(ma - mn), fb = EX2(mb - mn);
#pragma unroll
            for (int e = 0; e < 16; ++e) o[e] = o[e] * fa + p[e] * fb;
        } else {
#pragma unroll
            for (int e = 0; e < 16; ++e) o[e] += p[e];
        }
        const float inv = 1.0f / swap32_lo(o[12]);
#pragma unroll
        for (int e = 0; e < 16; ++e) o[e] = (e >= 12 && h == 0) ? 0.f : o[e] * inv;
        char* od = ws + WS_O + ((size_t)tglob * 16 + hd) * 2048;
        gstore16_sc1(od, vo, pack8(o, 0)); gstore16_sc1(od + 1024, vo, pack8(o, 1));
    }
    WAIT_VM(0); BARRIER();
    if (tid == 0) flag_store(ws, WS_FO + qb * 256 + hd * 8, tag);
    STAMP(ws, 14);
}

DI void phaseC(const char* __restrict__ x, const char* __restrict__ wo, const char* __restrict__ w2, const char* __restrict__ b2,
               char* __restrict__ ws, char* __restrict__ out, char* lds_generic, int rb, int ng, u64 tag, bool bump) {
    LAS char* lds = (LAS char*)lds_generic;
    const unsigned lds0 = (unsigned)(uintptr_t)lds_generic;
    const int tid = threadIdx.x, l = tid & 63, w = rfl(tid >> 6) & 7, h = l >> 5;
    const int tt = w & 1, kq = w >> 1, tglob = rb * 2 + tt;
    STAMP(ws, 20);
    if (w == 0) flag_wait(ws, WS_FH + (rb >> 1) * 256, 14, tag, l);
    BARRIER();
    STAMP(ws, 21);
    const unsigned vo = (l >> 3) * 896 + (((l & 7) ^ (((l >> 4) & 1) << 2)) * 16);
    const unsigned vl = l * 16;
    const char* w2src = w2 + ng * 128 + (size_t)(448 * kq + 32 * tt) * 896;
    const unsigned wdst = lds0 + kq * 8192 + tt * 4096;
    const char* hsrc = ws + WS_H + ((size_t)tglob * 56 + 14 * kq) * 2048;
    const char* osrc = ws + WS_O + ((size_t)tglob * 16 + 4 * kq) * 2048;
    u32x4 f[9][4];
    auto issue = [&](int st) {
        if (st < 7) {
#pragma unroll
            for (int e = 0; e < 4; ++e) glds16(w2src + (size_t)(st * 64 + e * 8) * 896, vo, wdst + (st & 3) * 32768 + e * 1024);
#pragma unroll
            for (int e = 0; e < 4; ++e) f[st][e] = gload16_sc1(hsrc + st * 4096 + e * 1024, vl);
        } else {
            const int hd = 4 * kq + 2 * (st - 7) + tt; const int r0 = hd < 15 ? 28 * hd : 416;
#pragma unroll
            for (int e = 0; e < 4; ++e) glds16(wo + ng * 128 + (size_t)(r0 + e * 8) * 896, vo, wdst + (st & 3) * 32768 + e * 1024);
#pragma unroll
            for (int e = 0; e < 4; ++e) f[st][e] = gload16_sc1(osrc + (st - 7) * 4096 + e * 1024, vl);
        }
    };
    issue(0); issue(1); issue(2); issue(3);
    const int q = (l & 15) >> 2, p = l & 3, cb = (l >> 4) & 1;
    const int tr0 = (4 * h + q) * 128 + (((2 * cb + (p >> 1)) ^ ((q >> 1) << 2)) * 16) + (p & 1) * 8, tr1 = tr0 ^ 64;
    f32x16 a0 = {}, a1 = {};
#define PINF(st) asm volatile("" : "+v"(f[st][0]), "+v"(f[st][1]), "+v"(f[st][2]), "+v"(f[st][3]))
#pragma unroll
    for (int st = 0; st < 9; ++st) {
        if (st == 0) WAIT_VM(24); else if (st <= 6) WAIT_VM(16); else if (st == 7) WAIT_VM(8); else WAIT_VM(0);
        PINF(st);
        if (st == 4 && w == 0) flag_wait(ws, WS_FO + (rb >> 1) * 256, 16, tag, l);
        WAIT_LGKM0(); BARRIER();
        if (st >= 1 && st + 3 < 9) issue(st + 3);
        const LAS char* wb = lds + (st & 3) * 32768 + kq * 8192;
        if (st < 7) {
#pragma unroll
            for (int u = 0; u < 2; ++u)
#pragma unroll
                for (int s = 0; s < 2; ++s) {
                    const LAS char* wp = wb + u * 4096 + s * 2048;
                    a0 = MFMA(trfrag(wp + tr0, wp + tr0 + 1024), asbf8(f[st][2 * u + s]), a0);
                    a1 = MFMA(trfrag(wp + tr1, wp + tr1 + 1024), asbf8(f[st][2 * u + s]), a1);
                }
        } else {
#pragma unroll
            for (int u = 0; u < 2; ++u) {
                const int hd = 4 * kq + 2 * (st - 7) + u;
                const LAS char* wp = wb + u * 4096 + (hd == 15 ? 512 : 0);
                a0 = MFMA(trfrag(wp + tr0, wp + tr0 + 1024), asbf8(f[st][2 * u]), a0);
                a1 = MFMA(trfrag(wp + tr1, wp + tr1 + 1024), asbf8(f[st][2 * u]), a1);
                a0 = MFMA(trfrag(wp + tr0 + 2048, wp + tr0 + 2048 + 512), asbf8(f[st][2 * u + 1]), a0);
                a1 = MFMA(trfrag(wp + tr1 + 2048, wp + tr1 + 2048 + 512), asbf8(f[st][2 * u + 1]), a1);
            }
        }
    }
    STAMP(ws, 22);
    const int tok = tid >> 3, ch = tid & 7;
    const size_t goff = (size_t)(rb * 64 + tok) * 896 + ng * 128 + ch * 16;
    const u32x4 xv = *(const u32x4*)(x + goff), bv = *(const u32x4*)(b2 + ng * 128 + ch * 16);
    WAIT_LGKM0(); BARRIER();
    {
        LAS char* rw = lds + (kq * 64 + tt * 32 + (l & 31)) * C_RROW + h * 16;
#pragma unroll
        for (int g = 0; g < 4; ++g) {
            *(LAS f32x4*)(rw + g * 32) = (f32x4){a0[4 * g], a0[4 * g + 1], a0[4 * g + 2], a0[4 * g + 3]};
            *(LAS f32x4*)(rw + 128 + g * 32) = (f32x4){a1[4 * g], a1[4 * g + 1], a1[4 * g + 2], a1[4 * g + 3]};
        }
    }
    WAIT_LGKM0(); BARRIER();
    f32x4 s0 = {}, s1 = {};
#pragma unroll
    for (int k = 0; k < 4; ++k) { const LAS char* rr = lds + (k * 64 + tok) * C_RROW + ch * 32; s0 += *(const LAS f32x4*)rr; s1 += *(const LAS f32x4*)(rr + 16); }
    u32x4 ov;
    ov[0] = cvtpk(s0[0] + bflo(xv[0]) + bflo(bv[0]), s0[1] + bfhi(xv[0]) + bfhi(bv[0]));
    ov[1] = cvtpk(s0[2] + bflo(xv[1]) + bflo(bv[1]), s0[3] + bfhi(xv[1]) + bfhi(bv[1]));
    ov[2] = cvtpk(s1[0] + bflo(xv[2]) + bflo(bv[2]), s1[1] + bfhi(xv[2]) + bfhi(bv[2]));
    ov[3] = cvtpk(s1[2] + bflo(xv[3]) + bflo(bv[3]), s1[3] + bfhi(xv[3]) + bfhi(bv[3]));
    *(u32x4*)(out + goff) = ov;
    if (bump && tid == 0) flag_store(ws, WS_EPOCH, tag);
    STAMP(ws, 23);
}

__global__ void __launch_bounds__(512, 2) fused_block(const char* x, const char* wqkv, const char* wo, const char* w1, const char* b1,
                                                      const char* w2, const char* b2, char* out, char* ws) {
    extern __shared__ __attribute__((aligned(16))) char lds[];
    const int b = blockIdx.x, xcd = b & 7, i = b >> 3;
    const int Rb = i >> 1, c = 2 * xcd + (i & 1);
    u64 tag;
    const unsigned long long t_entry = __builtin_amdgcn_s_memrealtime();
    phaseA(x, wqkv, w1, b1, ws, lds, Rb, c, tag);
    phaseB(ws, lds, c, Rb, tag);
    if (i < 28) phaseC(x, wo, w2, b2, ws, out, lds, 4 * xcd + i / 7, i % 7, tag, b == 0);
    while (__builtin_amdgcn_s_memrealtime() - t_entry < 6000ull) __builtin_amdgcn_s_sleep(1);
}

extern "C" void kernel_launch(void* const* d_in, const int* in_sizes, int n_in, void* d_out, int out_size, void* d_ws, size_t ws_size, hipStream_t stream) {
    const char* x = (const char*)d_in[0]; const char* wqkv = (const char*)d_in[1]; const char* wo = (const char*)d_in[2];
    const char* w1 = (const char*)d_in[4]; const char* b1 = (const char*)d_in[5]; const char* w2 = (const char*)d_in[6]; const char* b2 = (const char*)d_in[7];
    (void)hipFuncSetAttribute((const void*)fused_block, hipFuncAttributeMaxDynamicSharedMemorySize, L_TOTAL);
    hipLaunchKernelGGL(fused_block, dim3(256), dim3(512), L_TOTAL, stream, x, wqkv, wo, w1, b1, w2, b2, (char*)d_out, (char*)d_ws);
}
```

```cpp
#include <hip/hip_runtime.h>
#include <hip/hip_bf16.h>
#include <stdint.h>

typedef __bf16 bf16x2 __attribute__((ext_vector_type(2)));
typedef __bf16 bf16x8 __attribute__((ext_vector_type(8)));
typedef float f32x2 __attribute__((ext_vector_type(2)));
typedef float f32x4 __attribute__((ext_vector_type(4)));
typedef float f32x16 __attribute__((ext_vector_type(16)));
typedef short s16x4 __attribute__((ext_vector_type(4)));
typedef unsigned u32x2 __attribute__((ext_vector_type(2)));
typedef unsigned u32x4 __attribute__((ext_vector_type(4)));
typedef __attribute__((address_space(1))) unsigned long long gu64;
#define LAS __attribute__((address_space(3)))
#define DI __device__ __forceinline__
#define MFMA(a, b, c) __builtin_amdgcn_mfma_f32_32x32x16_bf16((a), (b), (c), 0, 0, 0)

constexpr size_t WS_EPOCH = 0;
constexpr size_t WS_FKV = 256;
constexpr size_t WS_FH = WS_FKV + 16 * 256;
constexpr size_t WS_FO = WS_FH + 16 * 256;
constexpr size_t WS_KN = 16384;
constexpr size_t WS_KV = 32768;
constexpr size_t WS_H = WS_KV + 16 * 64 * 4096;
constexpr size_t WS_O = WS_H + (size_t)64 * 56 * 2048;
constexpr size_t WS_END = WS_O + 64 * 16 * 2048;
#define STAMPC(wsp, k) do {} while (0)
#define STAMP(wsp, k) do {} while (0)

DI unsigned cvtpk(float lo, float hi) { f32x2 v = {lo, hi}; bf16x2 b = __builtin_convertvector(v, bf16x2); return __builtin_bit_cast(unsigned, b); }
DI bf16x2 asbf2(unsigned v) { return __builtin_bit_cast(bf16x2, v); }
DI bf16x8 asbf8(u32x4 v) { return __builtin_bit_cast(bf16x8, v); }
DI float bflo(unsigned v) { return __uint_as_float(v << 16); }
DI float bfhi(unsigned v) { return __uint_as_float(v & 0xffff0000u); }
DI unsigned rfl(unsigned x) { return __builtin_amdgcn_readfirstlane(x); }
DI const void* uptr(const void* p) { const uint64_t v = (uint64_t)p; return (const void*)(((uint64_t)rfl((unsigned)(v >> 32)) << 32) | rfl((unsigned)v)); }
DI void glds16(const void* sbase_, unsigned voff, unsigned lds_dst_) {
    unsigned sv; const void* sbase = uptr(sbase_); const unsigned lds_dst = rfl(lds_dst_);
    asm volatile("s_mov_b32 %0, m0\n\ts_mov_b32 m0, %3\n\ts_nop 4\n\tglobal_load_lds_dwordx4 %1, %2\n\ts_mov_b32 m0, %0"
                 : "=&s"(sv) : "v"(voff), "s"(sbase), "s"(lds_dst) : "memory");
}
DI u32x4 gload16(const void* sbase, unsigned voff) { u32x4 r; asm volatile("global_load_dwordx4 %0, %1, %2" : "=v"(r) : "v"(voff), "s"(uptr(sbase)) : "memory"); return r; }
DI u32x4 gload16_sc1(const void* sbase, unsigned voff) { u32x4 r; asm volatile("s_nop 4\n\tglobal_load_dwordx4 %0, %1, %2 sc1" : "=v"(r) : "v"(voff), "s"(uptr(sbase)) : "memory"); return r; }
DI void gstore16_sc1(void* sbase, unsigned voff, u32x4 v) { asm volatile("s_nop 4\n\tglobal_store_dwordx4 %0, %1, %2 sc1\n\ts_nop 1" :: "v"(voff), "v"(v), "s"(uptr(sbase)) : "memory"); }
DI s16x4 trread(const LAS char* p) { return __builtin_bit_cast(s16x4, __builtin_amdgcn_ds_read_tr16_b64_v4i16((LAS s16x4*)p)); }
DI bf16x8 trfrag(const LAS char* p0, const LAS char* p1) {
    s16x4 a = trread(p0), b = trread(p1);
    return __builtin_bit_cast(bf16x8, __builtin_shufflevector(a, b, 0, 1, 2, 3, 4, 5, 6, 7));
}
DI bf16x8 ldsfrag(const LAS char* p) { return *(const LAS bf16x8*)p; }
template <int CTRL> DI float dppf(float v) { return __int_as_float(__builtin_amdgcn_update_dpp(0, __float_as_int(v), CTRL, 0xf, 0xf, true)); }
DI u32x4 pack8(const f32x16& a, int s) {
    u32x4 p = {cvtpk(a[8 * s], a[8 * s + 1]), cvtpk(a[8 * s + 2], a[8 * s + 3]), cvtpk(a[8 * s + 4], a[8 * s + 5]), cvtpk(a[8 * s + 6], a[8 * s + 7])};
    return p;
}
DI float swap32_lo(float v) { auto sw = __builtin_amdgcn_permlane32_swap(__float_as_uint(v), __float_as_uint(v), false, false); return __uint_as_float(sw[0]); }
DI float xhalf_sum(float v) { auto sw = __builtin_amdgcn_permlane32_swap(__float_as_uint(v), __float_as_uint(v), false, false); return __uint_as_float(sw[0]) + __uint_as_float(sw[1]); }
DI float xhalf_max(float v) { auto sw = __builtin_amdgcn_permlane32_swap(__float_as_uint(v), __float_as_uint(v), false, false); return __builtin_fmaxf(__uint_as_float(sw[0]), __uint_as_float(sw[1])); }
DI float row32_max(float v) {
    v = __builtin_fmaxf(v, dppf<0xB1>(v)); v = __builtin_fmaxf(v, dppf<0x4E>(v)); v = __builtin_fmaxf(v, dppf<0x141>(v)); v = __builtin_fmaxf(v, dppf<0x140>(v));
    auto sw = __builtin_amdgcn_permlane16_swap(__float_as_uint(v), __float_as_uint(v), false, false); return __builtin_fmaxf(__uint_as_float(sw[0]), __uint_as_float(sw[1]));
}
#define BARRIER() asm volatile("s_barrier" ::: "memory")
#define WAIT_VM(n) asm volatile("s_waitcnt vmcnt(" #n ")" ::: "memory")
#define WAIT_LGKM0() asm volatile("s_waitcnt lgkmcnt(0)" ::: "memory")

typedef unsigned long long u64;
DI void flag_store(char* ws, size_t off, u64 tag) { __hip_atomic_store((gu64*)(ws + off), tag, __ATOMIC_RELAXED, __HIP_MEMORY_SCOPE_AGENT); }
DI void flag_wait(const char* ws, size_t off, int n, u64 tag, int l) {
    gu64* p = (gu64*)(ws + off) + (l < n ? l : 0);
    for (unsigned spins = 0; spins < (1u << 20); ++spins) {
        const u64 v = __hip_atomic_load(p, __ATOMIC_RELAXED, __HIP_MEMORY_SCOPE_AGENT);
        if (__all(v == tag)) break;
        __builtin_amdgcn_s_sleep(2);
    }
}
#define ATTN_FAST_ASM \
    "s_mov_b32 s43, m0\n\t" \
    "s_waitcnt vmcnt(12)\n\t" \
    "s_barrier\n\t" \
    "ds_read_b128 v[56:59], v96 offset:0\n\t" \
    "ds_read_b128 v[60:63], v96 offset:1024\n\t" \
    "ds_read_b128 v[64:67], v96 offset:4096\n\t" \
    "ds_read_b128 v[68:71], v96 offset:5120\n\t" \
    "ds_read_b128 v[72:75], v96 offset:2048\n\t" \
    "ds_read_b128 v[76:79], v96 offset:3072\n\t" \
    "s_waitcnt lgkmcnt(4)\n\t" \
    "v_mfma_f32_32x32x16_bf16 v[16:31], v[56:59], v[48:51], 0\n\t" \
    "v_mfma_f32_32x32x16_bf16 v[16:31], v[60:63], v[52:55], v[16:31]\n\t" \
    "s_nop 7\n\t" \
    "s_nop 7\n\t" \
    "v_exp_f32 v16, v16\n\t" \
    "v_exp_f32 v17, v17\n\t" \
    "ds_read_b128 v[56:59], v96 offset:8192\n\t" \
    "v_exp_f32 v18, v18\n\t" \
    "v_exp_f32 v19, v19\n\t" \
    "v_cvt_pk_bf16_f32 v88, v16, v17\n\t" \
    "v_cvt_pk_bf16_f32 v89, v18, v19\n\t" \
    "s_waitcnt lgkmcnt(2)\n\t" \
    "v_mfma_f32_32x32x16_bf16 v[32:47], v[64:67], v[48:51], 0\n\t" \
    "ds_read_b128 v[60:63], v96 offset:9216\n\t" \
    "v_exp_f32 v20, v20\n\t" \
    "v_exp_f32 v21, v21\n\t" \
    "v_exp_f32 v22, v22\n\t" \
    "v_exp_f32 v23, v23\n\t" \
    "v_cvt_pk_bf16_f32 v90, v20, v21\n\t" \
    "v_cvt_pk_bf16_f32 v91, v22, v23\n\t" \
    "v_mfma_f32_32x32x16_bf16 v[32:47], v[68:71], v[52:55], v[32:47]\n\t" \
    "ds_read_b128 v[80:83], v96 offset:6144\n\t" \
    "v_exp_f32 v24, v24\n\t" \
    "v_exp_f32 v25, v25\n\t" \
    "v_exp_f32 v26, v26\n\t" \
    "v_exp_f32 v27, v27\n\t" \
    "v_cvt_pk_bf16_f32 v92, v24, v25\n\t" \
    "v_cvt_pk_bf16_f32 v93, v26, v27\n\t" \
    "v_mfma_f32_32x32x16_bf16 v[0:15], v[72:75], v[88:91], 0\n\t" \
    "ds_read_b128 v[84:87], v96 offset:7168\n\t" \
    "v_exp_f32 v28, v28\n\t" \
    "v_exp_f32 v29, v29\n\t" \
    "v_exp_f32 v30, v30\n\t" \
    "v_exp_f32 v31, v31\n\t" \
    "v_cvt_pk_bf16_f32 v94, v28, v29\n\t" \
    "v_cvt_pk_bf16_f32 v95, v30, v31\n\t" \
    "v_exp_f32 v32, v32\n\t" \
    "v_exp_f32 v33, v33\n\t" \
    "v_mfma_f32_32x32x16_bf16 v[0:15], v[76:79], v[92:95], v[0:15]\n\t" \
    "ds_read_b128 v[64:67], v96 offset:12288\n\t" \
    "v_exp_f32 v34, v34\n\t" \
    "v_exp_f32 v35, v35\n\t" \
    "v_cvt_pk_bf16_f32 v88, v32, v33\n\t" \
    "v_cvt_pk_bf16_f32 v89, v34, v35\n\t" \
    "s_waitcnt lgkmcnt(2)\n\t" \
    "v_mfma_f32_32x32x16_bf16 v[16:31], v[56:59], v[48:51], 0\n\t" \
    "ds_read_b128 v[68:71], v96 offset:13312\n\t" \
    "v_exp_f32 v36, v36\n\t" \
    "v_exp_f32 v37, v37\n\t" \
    "v_exp_f32 v38, v38\n\t" \
    "v_exp_f32 v39, v39\n\t" \
    "v_cvt_pk_bf16_f32 v90, v36, v37\n\t" \
    "v_cvt_pk_bf16_f32 v91, v38, v39\n\t" \
    "v_mfma_f32_32x32x16_bf16 v[16:31], v[60:63], v[52:55], v[16:31]\n\t" \
    "ds_read_b128 v[72:75], v96 offset:10240\n\t" \
    "v_exp_f32 v40, v40\n\t" \
    "v_exp_f32 v41, v41\n\t" \
    "v_exp_f32 v42, v42\n\t" \
    "v_exp_f32 v43, v43\n\t" \
    "v_cvt_pk_bf16_f32 v92, v40, v41\n\t" \
    "v_cvt_pk_bf16_f32 v93, v42, v43\n\t" \
    "v_mfma_f32_32x32x16_bf16 v[0:15], v[80:83], v[88:91], v[0:15]\n\t" \
    "ds_read_b128 v[76:79], v96 offset:11264\n\t" \
    "v_exp_f32 v44, v44\n\t" \
    "v_exp_f32 v45, v45\n\t" \
    "v_exp_f32 v46, v46\n\t" \
    "v_exp_f32 v47, v47\n\t" \
    "v_cvt_pk_bf16_f32 v94, v44, v45\n\t" \
    "v_cvt_pk_bf16_f32 v95, v46, v47\n\t" \
    "s_waitcnt vmcnt(8)\n\t" \
    "s_barrier\n\t" \
    "v_exp_f32 v16, v16\n\t" \
    "v_exp_f32 v17, v17\n\t" \
    "v_mfma_f32_32x32x16_bf16 v[0:15], v[84:87], v[92:95], v[0:15]\n\t" \
    "ds_read_b128 v[56:59], v96 offset:32768\n\t" \
    "v_exp_f32 v18, v18\n\t" \
    "v_exp_f32 v19, v19\n\t" \
    "v_cvt_pk_bf16_f32 v88, v16, v17\n\t" \
    "v_cvt_pk_bf16_f32 v89, v18, v19\n\t" \
    "s_waitcnt lgkmcnt(2)\n\t" \
    "v_mfma_f32_32x32x16_bf16 v[32:47], v[64:67], v[48:51], 0\n\t" \
    "ds_read_b128 v[60:63], v96 offset:33792\n\t" \
    "v_exp_f32 v20, v20\n\t" \
    "v_exp_f32 v21, v21\n\t" \
    "v_exp_f32 v22, v22\n\t" \
    "v_exp_f32 v23, v23\n\t" \
    "v_cvt_pk_bf16_f32 v90, v20, v21\n\t" \
    "v_cvt_pk_bf16_f32 v91, v22, v23\n\t" \
    "v_mfma_f32_32x32x16_bf16 v[32:47], v[68:71], v[52:55], v[32:47]\n\t" \
    "ds_read_b128 v[80:83], v96 offset:14336\n\t" \
    "v_exp_f32 v24, v24\n\t" \
    "v_exp_f32 v25, v25\n\t" \
    "v_exp_f32 v26, v26\n\t" \
    "v_exp_f32 v27, v27\n\t" \
    "v_cvt_pk_bf16_f32 v92, v24, v25\n\t" \
    "v_cvt_pk_bf16_f32 v93, v26, v27\n\t" \
    "v_mfma_f32_32x32x16_bf16 v[0:15], v[72:75], v[88:91], v[0:15]\n\t" \
    "ds_read_b128 v[84:87], v96 offset:15360\n\t" \
    "v_exp_f32 v28, v28\n\t" \
    "v_exp_f32 v29, v29\n\t" \
    "v_exp_f32 v30, v30\n\t" \
    "v_exp_f32 v31, v31\n\t" \
    "v_cvt_pk_bf16_f32 v94, v28, v29\n\t" \
    "v_cvt_pk_bf16_f32 v95, v30, v31\n\t" \
    "v_exp_f32 v32, v32\n\t" \
    "v_exp_f32 v33, v33\n\t" \
    "v_mfma_f32_32x32x16_bf16 v[0:15], v[76:79], v[92:95], v[0:15]\n\t" \
    "ds_read_b128 v[64:67], v96 offset:36864\n\t" \
    "v_exp_f32 v34, v34\n\t" \
    "v_exp_f32 v35, v35\n\t" \
    "v_cvt_pk_bf16_f32 v88, v32, v33\n\t" \
    "v_cvt_pk_bf16_f32 v89, v34, v35\n\t" \
    "s_waitcnt lgkmcnt(2)\n\t" \
    "v_mfma_f32_32x32x16_bf16 v[16:31], v[56:59], v[48:51], 0\n\t" \
    "ds_read_b128 v[68:71], v96 offset:37888\n\t" \
    "v_exp_f32 v36, v36\n\t" \
    "v_exp_f32 v37, v37\n\t" \
    "v_exp_f32 v38, v38\n\t" \
    "v_exp_f32 v39, v39\n\t" \
    "v_cvt_pk_bf16_f32 v90, v36, v37\n\t" \
    "v_cvt_pk_bf16_f32 v91, v38, v39\n\t" \
    "v_mfma_f32_32x32x16_bf16 v[16:31], v[60:63], v[52:55], v[16:31]\n\t" \
    "ds_read_b128 v[72:75], v96 offset:34816\n\t" \
    "v_exp_f32 v40, v40\n\t" \
    "v_exp_f32 v41, v41\n\t" \
    "v_exp_f32 v42, v42\n\t" \
    "v_exp_f32 v43, v43\n\t" \
    "v_cvt_pk_bf16_f32 v92, v40, v41\n\t" \
    "v_cvt_pk_bf16_f32 v93, v42, v43\n\t" \
    "v_mfma_f32_32x32x16_bf16 v[0:15], v[80:83], v[88:91], v[0:15]\n\t" \
    "ds_read_b128 v[76:79], v96 offset:35840\n\t" \
    "v_exp_f32 v44, v44\n\t" \
    "v_exp_f32 v45, v45\n\t" \
    "v_exp_f32 v46, v46\n\t" \
    "v_exp_f32 v47, v47\n\t" \
    "v_cvt_pk_bf16_f32 v94, v44, v45\n\t" \
    "v_cvt_pk_bf16_f32 v95, v46, v47\n\t" \
    "v_exp_f32 v16, v16\n\t" \
    "v_exp_f32 v17, v17\n\t" \
    "v_mfma_f32_32x32x16_bf16 v[0:15], v[84:87], v[92:95], v[0:15]\n\t" \
    "ds_read_b128 v[56:59], v96 offset:40960\n\t" \
    "v_exp_f32 v18, v18\n\t" \
    "v_exp_f32 v19, v19\n\t" \
    "v_cvt_pk_bf16_f32 v88, v16, v17\n\t" \
    "v_cvt_pk_bf16_f32 v89, v18, v19\n\t" \
    "s_waitcnt lgkmcnt(2)\n\t" \
    "v_mfma_f32_32x32x16_bf16 v[32:47], v[64:67], v[48:51], 0\n\t" \
    "ds_read_b128 v[60:63], v96 offset:41984\n\t" \
    "v_exp_f32 v20, v20\n\t" \
    "v_exp_f32 v21, v21\n\t" \
    "v_exp_f32 v22, v22\n\t" \
    "v_exp_f32 v23, v23\n\t" \
    "v_cvt_pk_bf16_f32 v90, v20, v21\n\t" \
    "v_cvt_pk_bf16_f32 v91, v22, v23\n\t" \
    "v_mfma_f32_32x32x16_bf16 v[32:47], v[68:71], v[52:55], v[32:47]\n\t" \
    "ds_read_b128 v[80:83], v96 offset:38912\n\t" \
    "v_exp_f32 v24, v24\n\t" \
    "v_exp_f32 v25, v25\n\t" \
    "v_exp_f32 v26, v26\n\t" \
    "v_exp_f32 v27, v27\n\t" \
    "v_cvt_pk_bf16_f32 v92, v24, v25\n\t" \
    "v_cvt_pk_bf16_f32 v93, v26, v27\n\t" \
    "v_mfma_f32_32x32x16_bf16 v[0:15], v[72:75], v[88:91], v[0:15]\n\t" \
    "ds_read_b128 v[84:87], v96 offset:39936\n\t" \
    "v_exp_f32 v28, v28\n\t" \
    "v_exp_f32 v29, v29\n\t" \
    "v_exp_f32 v30, v30\n\t" \
    "v_exp_f32 v31, v31\n\t" \
    "v_cvt_pk_bf16_f32 v94, v28, v29\n\t" \
    "v_cvt_pk_bf16_f32 v95, v30, v31\n\t" \
    "v_exp_f32 v32, v32\n\t" \
    "v_exp_f32 v33, v33\n\t" \
    "v_mfma_f32_32x32x16_bf16 v[0:15], v[76:79], v[92:95], v[0:15]\n\t" \
    "ds_read_b128 v[64:67], v96 offset:45056\n\t" \
    "v_exp_f32 v34, v34\n\t" \
    "v_exp_f32 v35, v35\n\t" \
    "v_cvt_pk_bf16_f32 v88, v32, v33\n\t" \
    "v_cvt_pk_bf16_f32 v89, v34, v35\n\t" \
    "s_waitcnt lgkmcnt(2)\n\t" \
    "v_mfma_f32_32x32x16_bf16 v[16:31], v[56:59], v[48:51], 0\n\t" \
    "ds_read_b128 v[68:71], v96 offset:46080\n\t" \
    "v_exp_f32 v36, v36\n\t" \
    "v_exp_f32 v37, v37\n\t" \
    "v_exp_f32 v38, v38\n\t" \
    "v_exp_f32 v39, v39\n\t" \
    "v_cvt_pk_bf16_f32 v90, v36, v37\n\t" \
    "v_cvt_pk_bf16_f32 v91, v38, v39\n\t" \
    "v_mfma_f32_32x32x16_bf16 v[16:31], v[60:63], v[52:55], v[16:31]\n\t" \
    "ds_read_b128 v[72:75], v96 offset:43008\n\t" \
    "v_exp_f32 v40, v40\n\t" \
    "v_exp_f32 v41, v41\n\t" \
    "v_exp_f32 v42, v42\n\t" \
    "v_exp_f32 v43, v43\n\t" \
    "v_cvt_pk_bf16_f32 v92, v40, v41\n\t" \
    "v_cvt_pk_bf16_f32 v93, v42, v43\n\t" \
    "v_mfma_f32_32x32x16_bf16 v[0:15], v[80:83], v[88:91], v[0:15]\n\t" \
    "ds_read_b128 v[76:79], v96 offset:44032\n\t" \
    "v_exp_f32 v44, v44\n\t" \
    "v_exp_f32 v45, v45\n\t" \
    "v_exp_f32 v46, v46\n\t" \
    "v_exp_f32 v47, v47\n\t" \
    "v_cvt_pk_bf16_f32 v94, v44, v45\n\t" \
    "v_cvt_pk_bf16_f32 v95, v46, v47\n\t" \
    "s_waitcnt vmcnt(4)\n\t" \
    "s_barrier\n\t" \
    "s_mov_b32 m0, s42\n\t" \
    "s_add_u32 s40, s40, 16384\n\t" \
    "s_addc_u32 s41, s41, 0\n\t" \
    "global_load_lds_dwordx4 v98, s[40:41] offset:0\n\t" \
    "global_load_lds_dwordx4 v98, s[40:41] offset:1024\n\t" \
    "global_load_lds_dwordx4 v98, s[40:41] offset:2048\n\t" \
    "global_load_lds_dwordx4 v98, s[40:41] offset:3072\n\t" \
    "v_exp_f32 v16, v16\n\t" \
    "v_exp_f32 v17, v17\n\t" \
    "v_mfma_f32_32x32x16_bf16 v[0:15], v[84:87], v[92:95], v[0:15]\n\t" \
    "ds_read_b128 v[56:59], v97 offset:0\n\t" \
    "v_exp_f32 v18, v18\n\t" \
    "v_exp_f32 v19, v19\n\t" \
    "v_cvt_pk_bf16_f32 v88, v16, v17\n\t" \
    "v_cvt_pk_bf16_f32 v89, v18, v19\n\t" \
    "s_waitcnt lgkmcnt(2)\n\t" \
    "v_mfma_f32_32x32x16_bf16 v[32:47], v[64:67], v[48:51], 0\n\t" \
    "ds_read_b128 v[60:63], v97 offset:1024\n\t" \
    "v_exp_f32 v20, v20\n\t" \
    "v_exp_f32 v21, v21\n\t" \
    "v_exp_f32 v22, v22\n\t" \
    "v_exp_f32 v23, v23\n\t" \
    "v_cvt_pk_bf16_f32 v90, v20, v21\n\t" \
    "v_cvt_pk_bf16_f32 v91, v22, v23\n\t" \
    "v_mfma_f32_32x32x16_bf16 v[32:47], v[68:71], v[52:55], v[32:47]\n\t" \
    "ds_read_b128 v[80:83], v96 offset:47104\n\t" \
    "v_exp_f32 v24, v24\n\t" \
    "v_exp_f32 v25, v25\n\t" \
    "v_exp_f32 v26, v26\n\t" \
    "v_exp_f32 v27, v27\n\t" \
    "v_cvt_pk_bf16_f32 v92, v24, v25\n\t" \
    "v_cvt_pk_bf16_f32 v93, v26, v27\n\t" \
    "v_mfma_f32_32x32x16_bf16 v[0:15], v[72:75], v[88:91], v[0:15]\n\t" \
    "ds_read_b128 v[84:87], v96 offset:48128\n\t" \
    "v_exp_f32 v28, v28\n\t" \
    "v_exp_f32 v29, v29\n\t" \
    "v_exp_f32 v30, v30\n\t" \
    "v_exp_f32 v31, v31\n\t" \
    "v_cvt_pk_bf16_f32 v94, v28, v29\n\t" \
    "v_cvt_pk_bf16_f32 v95, v30, v31\n\t" \
    "v_exp_f32 v32, v32\n\t" \
    "v_exp_f32 v33, v33\n\t" \
    "v_mfma_f32_32x32x16_bf16 v[0:15], v[76:79], v[92:95], v[0:15]\n\t" \
    "ds_read_b128 v[64:67], v97 offset:4096\n\t" \
    "v_exp_f32 v34, v34\n\t" \
    "v_exp_f32 v35, v35\n\t" \
    "v_cvt_pk_bf16_f32 v88, v32, v33\n\t" \
    "v_cvt_pk_bf16_f32 v89, v34, v35\n\t" \
    "s_waitcnt lgkmcnt(2)\n\t" \
    "v_mfma_f32_32x32x16_bf16 v[16:31], v[56:59], v[48:51], 0\n\t" \
    "ds_read_b128 v[68:71], v97 offset:5120\n\t" \
    "v_exp_f32 v36, v36\n\t" \
    "v_exp_f32 v37, v37\n\t" \
    "v_exp_f32 v38, v38\n\t" \
    "v_exp_f32 v39, v39\n\t" \
    "v_cvt_pk_bf16_f32 v90, v36, v37\n\t" \
    "v_cvt_pk_bf16_f32 v91, v38, v39\n\t" \
    "v_mfma_f32_32x32x16_bf16 v[16:31], v[60:63], v[52:55], v[16:31]\n\t" \
    "ds_read_b128 v[72:75], v97 offset:2048\n\t" \
    "v_exp_f32 v40, v40\n\t" \
    "v_exp_f32 v41, v41\n\t" \
    "v_exp_f32 v42, v42\n\t" \
    "v_exp_f32 v43, v43\n\t" \
    "v_cvt_pk_bf16_f32 v92, v40, v41\n\t" \
    "v_cvt_pk_bf16_f32 v93, v42, v43\n\t" \
    "v_mfma_f32_32x32x16_bf16 v[0:15], v[80:83], v[88:91], v[0:15]\n\t" \
    "ds_read_b128 v[76:79], v97 offset:3072\n\t" \
    "v_exp_f32 v44, v44\n\t" \
    "v_exp_f32 v45, v45\n\t" \
    "v_exp_f32 v46, v46\n\t" \
    "v_exp_f32 v47, v47\n\t" \
    "v_cvt_pk_bf16_f32 v94, v44, v45\n\t" \
    "v_cvt_pk_bf16_f32 v95, v46, v47\n\t" \
    "v_exp_f32 v16, v16\n\t" \
    "v_exp_f32 v17, v17\n\t" \
    "v_mfma_f32_32x32x16_bf16 v[0:15], v[84:87], v[92:95], v[0:15]\n\t" \
    "ds_read_b128 v[56:59], v97 offset:8192\n\t" \
    "v_exp_f32 v18, v18\n\t" \
    "v_exp_f32 v19, v19\n\t" \
    "v_cvt_pk_bf16_f32 v88, v16, v17\n\t" \
    "v_cvt_pk_bf16_f32 v89, v18, v19\n\t" \
    "s_waitcnt lgkmcnt(2)\n\t" \
    "v_mfma_f32_32x32x16_bf16 v[32:47], v[64:67], v[48:51], 0\n\t" \
    "ds_read_b128 v[60:63], v97 offset:9216\n\t" \
    "v_exp_f32 v20, v20\n\t" \
    "v_exp_f32 v21, v21\n\t" \
    "v_exp_f32 v22, v22\n\t" \
    "v_exp_f32 v23, v23\n\t" \
    "v_cvt_pk_bf16_f32 v90, v20, v21\n\t" \
    "v_cvt_pk_bf16_f32 v91, v22, v23\n\t" \
    "v_mfma_f32_32x32x16_bf16 v[32:47], v[68:71], v[52:55], v[32:47]\n\t" \
    "ds_read_b128 v[80:83], v97 offset:6144\n\t" \
    "v_exp_f32 v24, v24\n\t" \
    "v_exp_f32 v25, v25\n\t" \
    "v_exp_f32 v26, v26\n\t" \
    "v_exp_f32 v27, v27\n\t" \
    "v_cvt_pk_bf16_f32 v92, v24, v25\n\t" \
    "v_cvt_pk_bf16_f32 v93, v26, v27\n\t" \
    "v_mfma_f32_32x32x16_bf16 v[0:15], v[72:75], v[88:91], v[0:15]\n\t" \
    "ds_read_b128 v[84:87], v97 offset:7168\n\t" \
    "v_exp_f32 v28, v28\n\t" \
    "v_exp_f32 v29, v29\n\t" \
    "v_exp_f32 v30, v30\n\t" \
    "v_exp_f32 v31, v31\n\t" \
    "v_cvt_pk_bf16_f32 v94, v28, v29\n\t" \
    "v_cvt_pk_bf16_f32 v95, v30, v31\n\t" \
    "v_exp_f32 v32, v32\n\t" \
    "v_exp_f32 v33, v33\n\t" \
    "v_mfma_f32_32x32x16_bf16 v[0:15], v[76:79], v[92:95], v[0:15]\n\t" \
    "ds_read_b128 v[64:67], v97 offset:12288\n\t" \
    "v_exp_f32 v34, v34\n\t" \
    "v_exp_f32 v35, v35\n\t" \
    "v_cvt_pk_bf16_f32 v88, v32, v33\n\t" \
    "v_cvt_pk_bf16_f32 v89, v34, v35\n\t" \
    "s_waitcnt lgkmcnt(2)\n\t" \
    "v_mfma_f32_32x32x16_bf16 v[16:31], v[56:59], v[48:51], 0\n\t" \
    "ds_read_b128 v[68:71], v97 offset:13312\n\t" \
    "v_exp_f32 v36, v36\n\t" \
    "v_exp_f32 v37, v37\n\t" \
    "v_exp_f32 v38, v38\n\t" \
    "v_exp_f32 v39, v39\n\t" \
    "v_cvt_pk_bf16_f32 v90, v36, v37\n\t" \
    "v_cvt_pk_bf16_f32 v91, v38, v39\n\t" \
    "v_mfma_f32_32x32x16_bf16 v[16:31], v[60:63], v[52:55], v[16:31]\n\t" \
    "ds_read_b128 v[72:75], v97 offset:10240\n\t" \
    "v_exp_f32 v40, v40\n\t" \
    "v_exp_f32 v41, v41\n\t" \
    "v_exp_f32 v42, v42\n\t" \
    "v_exp_f32 v43, v43\n\t" \
    "v_cvt_pk_bf16_f32 v92, v40, v41\n\t" \
    "v_cvt_pk_bf16_f32 v93, v42, v43\n\t" \
    "v_mfma_f32_32x32x16_bf16 v[0:15], v[80:83], v[88:91], v[0:15]\n\t" \
    "ds_read_b128 v[76:79], v97 offset:11264\n\t" \
    "v_exp_f32 v44, v44\n\t" \
    "v_exp_f32 v45, v45\n\t" \
    "v_exp_f32 v46, v46\n\t" \
    "v_exp_f32 v47, v47\n\t" \
    "v_cvt_pk_bf16_f32 v94, v44, v45\n\t" \
    "v_cvt_pk_bf16_f32 v95, v46, v47\n\t" \
    "s_waitcnt vmcnt(4)\n\t" \
    "s_barrier\n\t" \
    "s_add_u32 m0, s42, 32768\n\t" \
    "s_add_u32 s40, s40, 16384\n\t" \
    "s_addc_u32 s41, s41, 0\n\t" \
    "global_load_lds_dwordx4 v98, s[40:41] offset:0\n\t" \
    "global_load_lds_dwordx4 v98, s[40:41] offset:1024\n\t" \
    "global_load_lds_dwordx4 v98, s[40:41] offset:2048\n\t" \
    "global_load_lds_dwordx4 v98, s[40:41] offset:3072\n\t" \
    "v_exp_f32 v16, v16\n\t" \
    "v_exp_f32 v17, v17\n\t" \
    "v_mfma_f32_32x32x16_bf16 v[0:15], v[84:87], v[92:95], v[0:15]\n\t" \
    "ds_read_b128 v[56:59], v97 offset:32768\n\t" \
    "v_exp_f32 v18, v18\n\t" \
    "v_exp_f32 v19, v19\n\t" \
    "v_cvt_pk_bf16_f32 v88, v16, v17\n\t" \
    "v_cvt_pk_bf16_f32 v89, v18, v19\n\t" \
    "s_waitcnt lgkmcnt(2)\n\t" \
    "v_mfma_f32_32x32x16_bf16 v[32:47], v[64:67], v[48:51], 0\n\t" \
    "ds_read_b128 v[60:63], v97 offset:33792\n\t" \
    "v_exp_f32 v20, v20\n\t" \
    "v_exp_f32 v21, v21\n\t" \
    "v_exp_f32 v22, v22\n\t" \
    "v_exp_f32 v23, v23\n\t" \
    "v_cvt_pk_bf16_f32 v90, v20, v21\n\t" \
    "v_cvt_pk_bf16_f32 v91, v22, v23\n\t" \
    "v_mfma_f32_32x32x16_bf16 v[32:47], v[68:71], v[52:55], v[32:47]\n\t" \
    "ds_read_b128 v[80:83], v97 offset:14336\n\t" \
    "v_exp_f32 v24, v24\n\t" \
    "v_exp_f32 v25, v25\n\t" \
    "v_exp_f32 v26, v26\n\t" \
    "v_exp_f32 v27, v27\n\t" \
    "v_cvt_pk_bf16_f32 v92, v24, v25\n\t" \
    "v_cvt_pk_bf16_f32 v93, v26, v27\n\t" \
    "v_mfma_f32_32x32x16_bf16 v[0:15], v[72:75], v[88:91], v[0:15]\n\t" \
    "ds_read_b128 v[84:87], v97 offset:15360\n\t" \
    "v_exp_f32 v28, v28\n\t" \
    "v_exp_f32 v29, v29\n\t" \
    "v_exp_f32 v30, v30\n\t" \
    "v_exp_f32 v31, v31\n\t" \
    "v_cvt_pk_bf16_f32 v94, v28, v29\n\t" \
    "v_cvt_pk_bf16_f32 v95, v30, v31\n\t" \
    "v_exp_f32 v32, v32\n\t" \
    "v_exp_f32 v33, v33\n\t" \
    "v_mfma_f32_32x32x16_bf16 v[0:15], v[76:79], v[92:95], v[0:15]\n\t" \
    "ds_read_b128 v[64:67], v97 offset:36864\n\t" \
    "v_exp_f32 v34, v34\n\t" \
    "v_exp_f32 v35, v35\n\t" \
    "v_cvt_pk_bf16_f32 v88, v32, v33\n\t" \
    "v_cvt_pk_bf16_f32 v89, v34, v35\n\t" \
    "s_waitcnt lgkmcnt(2)\n\t" \
    "v_mfma_f32_32x32x16_bf16 v[16:31], v[56:59], v[48:51], 0\n\t" \
    "ds_read_b128 v[68:71], v97 offset:37888\n\t" \
    "v_exp_f32 v36, v36\n\t" \
    "v_exp_f32 v37, v37\n\t" \
    "v_exp_f32 v38, v38\n\t" \
    "v_exp_f32 v39, v39\n\t" \
    "v_cvt_pk_bf16_f32 v90, v36, v37\n\t" \
    "v_cvt_pk_bf16_f32 v91, v38, v39\n\t" \
    "v_mfma_f32_32x32x16_bf16 v[16:31], v[60:63], v[52:55], v[16:31]\n\t" \
    "ds_read_b128 v[72:75], v97 offset:34816\n\t" \
    "v_exp_f32 v40, v40\n\t" \
    "v_exp_f32 v41, v41\n\t" \
    "v_exp_f32 v42, v42\n\t" \
    "v_exp_f32 v43, v43\n\t" \
    "v_cvt_pk_bf16_f32 v92, v40, v41\n\t" \
    "v_cvt_pk_bf16_f32 v93, v42, v43\n\t" \
    "v_mfma_f32_32x32x16_bf16 v[0:15], v[80:83], v[88:91], v[0:15]\n\t" \
    "ds_read_b128 v[76:79], v97 offset:35840\n\t" \
    "v_exp_f32 v44, v44\n\t" \
    "v_exp_f32 v45, v45\n\t" \
    "v_exp_f32 v46, v46\n\t" \
    "v_exp_f32 v47, v47\n\t" \
    "v_cvt_pk_bf16_f32 v94, v44, v45\n\t" \
    "v_cvt_pk_bf16_f32 v95, v46, v47\n\t" \
    "v_exp_f32 v16, v16\n\t" \
    "v_exp_f32 v17, v17\n\t" \
    "v_mfma_f32_32x32x16_bf16 v[0:15], v[84:87], v[92:95], v[0:15]\n\t" \
    "ds_read_b128 v[56:59], v97 offset:40960\n\t" \
    "v_exp_f32 v18, v18\n\t" \
    "v_exp_f32 v19, v19\n\t" \
    "v_cvt_pk_bf16_f32 v88, v16, v17\n\t" \
    "v_cvt_pk_bf16_f32 v89, v18, v19\n\t" \
    "s_waitcnt lgkmcnt(2)\n\t" \
    "v_mfma_f32_32x32x16_bf16 v[32:47], v[64:67], v[48:51], 0\n\t" \
    "ds_read_b128 v[60:63], v97 offset:41984\n\t" \
    "v_exp_f32 v20, v20\n\t" \
    "v_exp_f32 v21, v21\n\t" \
    "v_exp_f32 v22, v22\n\t" \
    "v_exp_f32 v23, v23\n\t" \
    "v_cvt_pk_bf16_f32 v90, v20, v21\n\t" \
    "v_cvt_pk_bf16_f32 v91, v22, v23\n\t" \
    "v_mfma_f32_32x32x16_bf16 v[32:47], v[68:71], v[52:55], v[32:47]\n\t" \
    "ds_read_b128 v[80:83], v97 offset:38912\n\t" \
    "v_exp_f32 v24, v24\n\t" \
    "v_exp_f32 v25, v25\n\t" \
    "v_exp_f32 v26, v26\n\t" \
    "v_exp_f32 v27, v27\n\t" \
    "v_cvt_pk_bf16_f32 v92, v24, v25\n\t" \
    "v_cvt_pk_bf16_f32 v93, v26, v27\n\t" \
    "v_mfma_f32_32x32x16_bf16 v[0:15], v[72:75], v[88:91], v[0:15]\n\t" \
    "ds_read_b128 v[84:87], v97 offset:39936\n\t" \
    "v_exp_f32 v28, v28\n\t" \
    "v_exp_f32 v29, v29\n\t" \
    "v_exp_f32 v30, v30\n\t" \
    "v_exp_f32 v31, v31\n\t" \
    "v_cvt_pk_bf16_f32 v94, v28, v29\n\t" \
    "v_cvt_pk_bf16_f32 v95, v30, v31\n\t" \
    "v_exp_f32 v32, v32\n\t" \
    "v_exp_f32 v33, v33\n\t" \
    "v_mfma_f32_32x32x16_bf16 v[0:15], v[76:79], v[92:95], v[0:15]\n\t" \
    "ds_read_b128 v[64:67], v97 offset:45056\n\t" \
    "v_exp_f32 v34, v34\n\t" \
    "v_exp_f32 v35, v35\n\t" \
    "v_cvt_pk_bf16_f32 v88, v32, v33\n\t" \
    "v_cvt_pk_bf16_f32 v89, v34, v35\n\t" \
    "s_waitcnt lgkmcnt(2)\n\t" \
    "v_mfma_f32_32x32x16_bf16 v[16:31], v[56:59], v[48:51], 0\n\t" \
    "ds_read_b128 v[68:71], v97 offset:46080\n\t" \
    "v_exp_f32 v36, v36\n\t" \
    "v_exp_f32 v37, v37\n\t" \
    "v_exp_f32 v38, v38\n\t" \
    "v_exp_f32 v39, v39\n\t" \
    "v_cvt_pk_bf16_f32 v90, v36, v37\n\t" \
    "v_cvt_pk_bf16_f32 v91, v38, v39\n\t" \
    "v_mfma_f32_32x32x16_bf16 v[16:31], v[60:63], v[52:55], v[16:31]\n\t" \
    "ds_read_b128 v[72:75], v97 offset:43008\n\t" \
    "v_exp_f32 v40, v40\n\t" \
    "v_exp_f32 v41, v41\n\t" \
    "v_exp_f32 v42, v42\n\t" \
    "v_exp_f32 v43, v43\n\t" \
    "v_cvt_pk_bf16_f32 v92, v40, v41\n\t" \
    "v_cvt_pk_bf16_f32 v93, v42, v43\n\t" \
    "v_mfma_f32_32x32x16_bf16 v[0:15], v[80:83], v[88:91], v[0:15]\n\t" \
    "ds_read_b128 v[76:79], v97 offset:44032\n\t" \
    "v_exp_f32 v44, v44\n\t" \
    "v_exp_f32 v45, v45\n\t" \
    "v_exp_f32 v46, v46\n\t" \
    "v_exp_f32 v47, v47\n\t" \
    "v_cvt_pk_bf16_f32 v94, v44, v45\n\t" \
    "v_cvt_pk_bf16_f32 v95, v46, v47\n\t" \
    "s_waitcnt vmcnt(4)\n\t" \
    "s_barrier\n\t" \
    "s_add_u32 m0, s42, 65536\n\t" \
    "s_add_u32 s40, s40, 16384\n\t" \
    "s_addc_u32 s41, s41, 0\n\t" \
    "global_load_lds_dwordx4 v98, s[40:41] offset:0\n\t" \
    "global_load_lds_dwordx4 v98, s[40:41] offset:1024\n\t" \
    "global_load_lds_dwordx4 v98, s[40:41] offset:2048\n\t" \
    "global_load_lds_dwordx4 v98, s[40:41] offset:3072\n\t" \
    "v_exp_f32 v16, v16\n\t" \
    "v_exp_f32 v17, v17\n\t" \
    "v_mfma_f32_32x32x16_bf16 v[0:15], v[84:87], v[92:95], v[0:15]\n\t" \
    "ds_read_b128 v[56:59], v96 offset:0\n\t" \
    "v_exp_f32 v18, v18\n\t" \
    "v_exp_f32 v19, v19\n\t" \
    "v_cvt_pk_bf16_f32 v88, v16, v17\n\t" \
    "v_cvt_pk_bf16_f32 v89, v18, v19\n\t" \
    "s_waitcnt lgkmcnt(2)\n\t" \
    "v_mfma_f32_32x32x16_bf16 v[32:47], v[64:67], v[48:51], 0\n\t" \
    "ds_read_b128 v[60:63], v96 offset:1024\n\t" \
    "v_exp_f32 v20, v20\n\t" \
    "v_exp_f32 v21, v21\n\t" \
    "v_exp_f32 v22, v22\n\t" \
    "v_exp_f32 v23, v23\n\t" \
    "v_cvt_pk_bf16_f32 v90, v20, v21\n\t" \
    "v_cvt_pk_bf16_f32 v91, v22, v23\n\t" \
    "v_mfma_f32_32x32x16_bf16 v[32:47], v[68:71], v[52:55], v[32:47]\n\t" \
    "ds_read_b128 v[80:83], v97 offset:47104\n\t" \
    "v_exp_f32 v24, v24\n\t" \
    "v_exp_f32 v25, v25\n\t" \
    "v_exp_f32 v26, v26\n\t" \
    "v_exp_f32 v27, v27\n\t" \
    "v_cvt_pk_bf16_f32 v92, v24, v25\n\t" \
    "v_cvt_pk_bf16_f32 v93, v26, v27\n\t" \
    "v_mfma_f32_32x32x16_bf16 v[0:15], v[72:75], v[88:91], v[0:15]\n\t" \
    "ds_read_b128 v[84:87], v97 offset:48128\n\t" \
    "v_exp_f32 v28, v28\n\t" \
    "v_exp_f32 v29, v29\n\t" \
    "v_exp_f32 v30, v30\n\t" \
    "v_exp_f32 v31, v31\n\t" \
    "v_cvt_pk_bf16_f32 v94, v28, v29\n\t" \
    "v_cvt_pk_bf16_f32 v95, v30, v31\n\t" \
    "v_exp_f32 v32, v32\n\t" \
    "v_exp_f32 v33, v33\n\t" \
    "v_mfma_f32_32x32x16_bf16 v[0:15], v[76:79], v[92:95], v[0:15]\n\t" \
    "ds_read_b128 v[64:67], v96 offset:4096\n\t" \
    "v_exp_f32 v34, v34\n\t" \
    "v_exp_f32 v35, v35\n\t" \
    "v_cvt_pk_bf16_f32 v88, v32, v33\n\t" \
    "v_cvt_pk_bf16_f32 v89, v34, v35\n\t" \
    "s_waitcnt lgkmcnt(2)\n\t" \
    "v_mfma_f32_32x32x16_bf16 v[16:31], v[56:59], v[48:51], 0\n\t" \
    "ds_read_b128 v[68:71], v96 offset:5120\n\t" \
    "v_exp_f32 v36, v36\n\t" \
    "v_exp_f32 v37, v37\n\t" \
    "v_exp_f32 v38, v38\n\t" \
    "v_exp_f32 v39, v39\n\t" \
    "v_cvt_pk_bf16_f32 v90, v36, v37\n\t" \
    "v_cvt_pk_bf16_f32 v91, v38, v39\n\t" \
    "v_mfma_f32_32x32x16_bf16 v[16:31], v[60:63], v[52:55], v[16:31]\n\t" \
    "ds_read_b128 v[72:75], v96 offset:2048\n\t" \
    "v_exp_f32 v40, v40\n\t" \
    "v_exp_f32 v41, v41\n\t" \
    "v_exp_f32 v42, v42\n\t" \
    "v_exp_f32 v43, v43\n\t" \
    "v_cvt_pk_bf16_f32 v92, v40, v41\n\t" \
    "v_cvt_pk_bf16_f32 v93, v42, v43\n\t" \
    "v_mfma_f32_32x32x16_bf16 v[0:15], v[80:83], v[88:91], v[0:15]\n\t" \
    "ds_read_b128 v[76:79], v96 offset:3072\n\t" \
    "v_exp_f32 v44, v44\n\t" \
    "v_exp_f32 v45, v45\n\t" \
    "v_exp_f32 v46, v46\n\t" \
    "v_exp_f32 v47, v47\n\t" \
    "v_cvt_pk_bf16_f32 v94, v44, v45\n\t" \
    "v_cvt_pk_bf16_f32 v95, v46, v47\n\t" \
    "v_exp_f32 v16, v16\n\t" \
    "v_exp_f32 v17, v17\n\t" \
    "v_mfma_f32_32x32x16_bf16 v[0:15], v[84:87], v[92:95], v[0:15]\n\t" \
    "ds_read_b128 v[56:59], v96 offset:8192\n\t" \
    "v_exp_f32 v18, v18\n\t" \
    "v_exp_f32 v19, v19\n\t" \
    "v_cvt_pk_bf16_f32 v88, v16, v17\n\t" \
    "v_cvt_pk_bf16_f32 v89, v18, v19\n\t" \
    "s_waitcnt lgkmcnt(2)\n\t" \
    "v_mfma_f32_32x32x16_bf16 v[32:47], v[64:67], v[48:51], 0\n\t" \
    "ds_read_b128 v[60:63], v96 offset:9216\n\t" \
    "v_exp_f32 v20, v20\n\t" \
    "v_exp_f32 v21, v21\n\t" \
    "v_exp_f32 v22, v22\n\t" \
    "v_exp_f32 v23, v23\n\t" \
    "v_cvt_pk_bf16_f32 v90, v20, v21\n\t" \
    "v_cvt_pk_bf16_f32 v91, v22, v23\n\t" \
    "v_mfma_f32_32x32x16_bf16 v[32:47], v[68:71], v[52:55], v[32:47]\n\t" \
    "ds_read_b128 v[80:83], v96 offset:6144\n\t" \
    "v_exp_f32 v24, v24\n\t" \
    "v_exp_f32 v25, v25\n\t" \
    "v_exp_f32 v26, v26\n\t" \
    "v_exp_f32 v27, v27\n\t" \
    "v_cvt_pk_bf16_f32 v92, v24, v25\n\t" \
    "v_cvt_pk_bf16_f32 v93, v26, v27\n\t" \
    "v_mfma_f32_32x32x16_bf16 v[0:15], v[72:75], v[88:91], v[0:15]\n\t" \
    "ds_read_b128 v[84:87], v96 offset:7168\n\t" \
    "v_exp_f32 v28, v28\n\t" \
    "v_exp_f32 v29, v29\n\t" \
    "v_exp_f32 v30, v30\n\t" \
    "v_exp_f32 v31, v31\n\t" \
    "v_cvt_pk_bf16_f32 v94, v28, v29\n\t" \
    "v_cvt_pk_bf16_f32 v95, v30, v31\n\t" \
    "v_exp_f32 v32, v32\n\t" \
    "v_exp_f32 v33, v33\n\t" \
    "v_mfma_f32_32x32x16_bf16 v[0:15], v[76:79], v[92:95], v[0:15]\n\t" \
    "ds_read_b128 v[64:67], v96 offset:12288\n\t" \
    "v_exp_f32 v34, v34\n\t" \
    "v_exp_f32 v35, v35\n\t" \
    "v_cvt_pk_bf16_f32 v88, v32, v33\n\t" \
    "v_cvt_pk_bf16_f32 v89, v34, v35\n\t" \
    "s_waitcnt lgkmcnt(2)\n\t" \
    "v_mfma_f32_32x32x16_bf16 v[16:31], v[56:59], v[48:51], 0\n\t" \
    "ds_read_b128 v[68:71], v96 offset:13312\n\t" \
    "v_exp_f32 v36, v36\n\t" \
    "v_exp_f32 v37, v37\n\t" \
    "v_exp_f32 v38, v38\n\t" \
    "v_exp_f32 v39, v39\n\t" \
    "v_cvt_pk_bf16_f32 v90, v36, v37\n\t" \
    "v_cvt_pk_bf16_f32 v91, v38, v39\n\t" \
    "v_mfma_f32_32x32x16_bf16 v[16:31], v[60:63], v[52:55], v[16:31]\n\t" \
    "ds_read_b128 v[72:75], v96 offset:10240\n\t" \
    "v_exp_f32 v40, v40\n\t" \
    "v_exp_f32 v41, v41\n\t" \
    "v_exp_f32 v42, v42\n\t" \
    "v_exp_f32 v43, v43\n\t" \
    "v_cvt_pk_bf16_f32 v92, v40, v41\n\t" \
    "v_cvt_pk_bf16_f32 v93, v42, v43\n\t" \
    "v_mfma_f32_32x32x16_bf16 v[0:15], v[80:83], v[88:91], v[0:15]\n\t" \
    "ds_read_b128 v[76:79], v96 offset:11264\n\t" \
    "v_exp_f32 v44, v44\n\t" \
    "v_exp_f32 v45, v45\n\t" \
    "v_exp_f32 v46, v46\n\t" \
    "v_exp_f32 v47, v47\n\t" \
    "v_cvt_pk_bf16_f32 v94, v44, v45\n\t" \
    "v_cvt_pk_bf16_f32 v95, v46, v47\n\t" \
    "s_waitcnt vmcnt(4)\n\t" \
    "s_barrier\n\t" \
    "s_add_u32 m0, s42, 98304\n\t" \
    "s_add_u32 s40, s40, 16384\n\t" \
    "s_addc_u32 s41, s41, 0\n\t" \
    "global_load_lds_dwordx4 v98, s[40:41] offset:0\n\t" \
    "global_load_lds_dwordx4 v98, s[40:41] offset:1024\n\t" \
    "global_load_lds_dwordx4 v98, s[40:41] offset:2048\n\t" \
    "global_load_lds_dwordx4 v98, s[40:41] offset:3072\n\t" \
    "v_exp_f32 v16, v16\n\t" \
    "v_exp_f32 v17, v17\n\t" \
    "v_mfma_f32_32x32x16_bf16 v[0:15], v[84:87], v[92:95], v[0:15]\n\t" \
    "ds_read_b128 v[56:59], v96 offset:32768\n\t" \
    "v_exp_f32 v18, v18\n\t" \
    "v_exp_f32 v19, v19\n\t" \
    "v_cvt_pk_bf16_f32 v88, v16, v17\n\t" \
    "v_cvt_pk_bf16_f32 v89, v18, v19\n\t" \
    "s_waitcnt lgkmcnt(2)\n\t" \
    "v_mfma_f32_32x32x16_bf16 v[32:47], v[64:67], v[48:51], 0\n\t" \
    "ds_read_b128 v[60:63], v96 offset:33792\n\t" \
    "v_exp_f32 v20, v20\n\t" \
    "v_exp_f32 v21, v21\n\t" \
    "v_exp_f32 v22, v22\n\t" \
    "v_exp_f32 v23, v23\n\t" \
    "v_cvt_pk_bf16_f32 v90, v20, v21\n\t" \
    "v_cvt_pk_bf16_f32 v91, v22, v23\n\t" \
    "v_mfma_f32_32x32x16_bf16 v[32:47], v[68:71], v[52:55], v[32:47]\n\t" \
    "ds_read_b128 v[80:83], v96 offset:14336\n\t" \
    "v_exp_f32 v24, v24\n\t" \
    "v_exp_f32 v25, v25\n\t" \
    "v_exp_f32 v26, v26\n\t" \
    "v_exp_f32 v27, v27\n\t" \
    "v_cvt_pk_bf16_f32 v92, v24, v25\n\t" \
    "v_cvt_pk_bf16_f32 v93, v26, v27\n\t" \
    "v_mfma_f32_32x32x16_bf16 v[0:15], v[72:75], v[88:91], v[0:15]\n\t" \
    "ds_read_b128 v[84:87], v96 offset:15360\n\t" \
    "v_exp_f32 v28, v28\n\t" \
    "v_exp_f32 v29, v29\n\t" \
    "v_exp_f32 v30, v30\n\t" \
    "v_exp_f32 v31, v31\n\t" \
    "v_cvt_pk_bf16_f32 v94, v28, v29\n\t" \
    "v_cvt_pk_bf16_f32 v95, v30, v31\n\t" \
    "v_exp_f32 v32, v32\n\t" \
    "v_exp_f32 v33, v33\n\t" \
    "v_mfma_f32_32x32x16_bf16 v[0:15], v[76:79], v[92:95], v[0:15]\n\t" \
    "ds_read_b128 v[64:67], v96 offset:36864\n\t" \
    "v_exp_f32 v34, v34\n\t" \
    "v_exp_f32 v35, v35\n\t" \
    "v_cvt_pk_bf16_f32 v88, v32, v33\n\t" \
    "v_cvt_pk_bf16_f32 v89, v34, v35\n\t" \
    "s_waitcnt lgkmcnt(2)\n\t" \
    "v_mfma_f32_32x32x16_bf16 v[16:31], v[56:59], v[48:51], 0\n\t" \
    "ds_read_b128 v[68:71], v96 offset:37888\n\t" \
    "v_exp_f32 v36, v36\n\t" \
    "v_exp_f32 v37, v37\n\t" \
    "v_exp_f32 v38, v38\n\t" \
    "v_exp_f32 v39, v39\n\t" \
    "v_cvt_pk_bf16_f32 v90, v36, v37\n\t" \
    "v_cvt_pk_bf16_f32 v91, v38, v39\n\t" \
    "v_mfma_f32_32x32x16_bf16 v[16:31], v[60:63], v[52:55], v[16:31]\n\t" \
    "ds_read_b128 v[72:75], v96 offset:34816\n\t" \
    "v_exp_f32 v40, v40\n\t" \
    "v_exp_f32 v41, v41\n\t" \
    "v_exp_f32 v42, v42\n\t" \
    "v_exp_f32 v43, v43\n\t" \
    "v_cvt_pk_bf16_f32 v92, v40, v41\n\t" \
    "v_cvt_pk_bf16_f32 v93, v42, v43\n\t" \
    "v_mfma_f32_32x32x16_bf16 v[0:15], v[80:83], v[88:91], v[0:15]\n\t" \
    "ds_read_b128 v[76:79], v96 offset:35840\n\t" \
    "v_exp_f32 v44, v44\n\t" \
    "v_exp_f32 v45, v45\n\t" \
    "v_exp_f32 v46, v46\n\t" \
    "v_exp_f32 v47, v47\n\t" \
    "v_cvt_pk_bf16_f32 v94, v44, v45\n\t" \
    "v_cvt_pk_bf16_f32 v95, v46, v47\n\t" \
    "v_exp_f32 v16, v16\n\t" \
    "v_exp_f32 v17, v17\n\t" \
    "v_mfma_f32_32x32x16_bf16 v[0:15], v[84:87], v[92:95], v[0:15]\n\t" \
    "ds_read_b128 v[56:59], v96 offset:40960\n\t" \
    "v_exp_f32 v18, v18\n\t" \
    "v_exp_f32 v19, v19\n\t" \
    "v_cvt_pk_bf16_f32 v88, v16, v17\n\t" \
    "v_cvt_pk_bf16_f32 v89, v18, v19\n\t" \
    "s_waitcnt lgkmcnt(2)\n\t" \
    "v_mfma_f32_32x32x16_bf16 v[32:47], v[64:67], v[48:51], 0\n\t" \
    "ds_read_b128 v[60:63], v96 offset:41984\n\t" \
    "v_exp_f32 v20, v20\n\t" \
    "v_exp_f32 v21, v21\n\t" \
    "v_exp_f32 v22, v22\n\t" \
    "v_exp_f32 v23, v23\n\t" \
    "v_cvt_pk_bf16_f32 v90, v20, v21\n\t" \
    "v_cvt_pk_bf16_f32 v91, v22, v23\n\t" \
    "v_mfma_f32_32x32x16_bf16 v[32:47], v[68:71], v[52:55], v[32:47]\n\t" \
    "ds_read_b128 v[80:83], v96 offset:38912\n\t" \
    "v_exp_f32 v24, v24\n\t" \
    "v_exp_f32 v25, v25\n\t" \
    "v_exp_f32 v26, v26\n\t" \
    "v_exp_f32 v27, v27\n\t" \
    "v_cvt_pk_bf16_f32 v92, v24, v25\n\t" \
    "v_cvt_pk_bf16_f32 v93, v26, v27\n\t" \
    "v_mfma_f32_32x32x16_bf16 v[0:15], v[72:75], v[88:91], v[0:15]\n\t" \
    "ds_read_b128 v[84:87], v96 offset:39936\n\t" \
    "v_exp_f32 v28, v28\n\t" \
    "v_exp_f32 v29, v29\n\t" \
    "v_exp_f32 v30, v30\n\t" \
    "v_exp_f32 v31, v31\n\t" \
    "v_cvt_pk_bf16_f32 v94, v28, v29\n\t" \
    "v_cvt_pk_bf16_f32 v95, v30, v31\n\t" \
    "v_exp_f32 v32, v32\n\t" \
    "v_exp_f32 v33, v33\n\t" \
    "v_mfma_f32_32x32x16_bf16 v[0:15], v[76:79], v[92:95], v[0:15]\n\t" \
    "ds_read_b128 v[64:67], v96 offset:45056\n\t" \
    "v_exp_f32 v34, v34\n\t" \
    "v_exp_f32 v35, v35\n\t" \
    "v_cvt_pk_bf16_f32 v88, v32, v33\n\t" \
    "v_cvt_pk_bf16_f32 v89, v34, v35\n\t" \
    "s_waitcnt lgkmcnt(2)\n\t" \
    "v_mfma_f32_32x32x16_bf16 v[16:31], v[56:59], v[48:51], 0\n\t" \
    "ds_read_b128 v[68:71], v96 offset:46080\n\t" \
    "v_exp_f32 v36, v36\n\t" \
    "v_exp_f32 v37, v37\n\t" \
    "v_exp_f32 v38, v38\n\t" \
    "v_exp_f32 v39, v39\n\t" \
    "v_cvt_pk_bf16_f32 v90, v36, v37\n\t" \
    "v_cvt_pk_bf16_f32 v91, v38, v39\n\t" \
    "v_mfma_f32_32x32x16_bf16 v[16:31], v[60:63], v[52:55], v[16:31]\n\t" \
    "ds_read_b128 v[72:75], v96 offset:43008\n\t" \
    "v_exp_f32 v40, v40\n\t" \
    "v_exp_f32 v41, v41\n\t" \
    "v_exp_f32 v42, v42\n\t" \
    "v_exp_f32 v43, v43\n\t" \
    "v_cvt_pk_bf16_f32 v92, v40, v41\n\t" \
    "v_cvt_pk_bf16_f32 v93, v42, v43\n\t" \
    "v_mfma_f32_32x32x16_bf16 v[0:15], v[80:83], v[88:91], v[0:15]\n\t" \
    "ds_read_b128 v[76:79], v96 offset:44032\n\t" \
    "v_exp_f32 v44, v44\n\t" \
    "v_exp_f32 v45, v45\n\t" \
    "v_exp_f32 v46, v46\n\t" \
    "v_exp_f32 v47, v47\n\t" \
    "v_cvt_pk_bf16_f32 v94, v44, v45\n\t" \
    "v_cvt_pk_bf16_f32 v95, v46, v47\n\t" \
    "s_waitcnt vmcnt(4)\n\t" \
    "s_barrier\n\t" \
    "v_exp_f32 v16, v16\n\t" \
    "v_exp_f32 v17, v17\n\t" \
    "v_mfma_f32_32x32x16_bf16 v[0:15], v[84:87], v[92:95], v[0:15]\n\t" \
    "ds_read_b128 v[56:59], v97 offset:0\n\t" \
    "v_exp_f32 v18, v18\n\t" \
    "v_exp_f32 v19, v19\n\t" \
    "v_cvt_pk_bf16_f32 v88, v16, v17\n\t" \
    "v_cvt_pk_bf16_f32 v89, v18, v19\n\t" \
    "s_waitcnt lgkmcnt(2)\n\t" \
    "v_mfma_f32_32x32x16_bf16 v[32:47], v[64:67], v[48:51], 0\n\t" \
    "ds_read_b128 v[60:63], v97 offset:1024\n\t" \
    "v_exp_f32 v20, v20\n\t" \
    "v_exp_f32 v21, v21\n\t" \
    "v_exp_f32 v22, v22\n\t" \
    "v_exp_f32 v23, v23\n\t" \
    "v_cvt_pk_bf16_f32 v90, v20, v21\n\t" \
    "v_cvt_pk_bf16_f32 v91, v22, v23\n\t" \
    "v_mfma_f32_32x32x16_bf16 v[32:47], v[68:71], v[52:55], v[32:47]\n\t" \
    "ds_read_b128 v[80:83], v96 offset:47104\n\t" \
    "v_exp_f32 v24, v24\n\t" \
    "v_exp_f32 v25, v25\n\t" \
    "v_exp_f32 v26, v26\n\t" \
    "v_exp_f32 v27, v27\n\t" \
    "v_cvt_pk_bf16_f32 v92, v24, v25\n\t" \
    "v_cvt_pk_bf16_f32 v93, v26, v27\n\t" \
    "v_mfma_f32_32x32x16_bf16 v[0:15], v[72:75], v[88:91], v[0:15]\n\t" \
    "ds_read_b128 v[84:87], v96 offset:48128\n\t" \
    "v_exp_f32 v28, v28\n\t" \
    "v_exp_f32 v29, v29\n\t" \
    "v_exp_f32 v30, v30\n\t" \
    "v_exp_f32 v31, v31\n\t" \
    "v_cvt_pk_bf16_f32 v94, v28, v29\n\t" \
    "v_cvt_pk_bf16_f32 v95, v30, v31\n\t" \
    "v_exp_f32 v32, v32\n\t" \
    "v_exp_f32 v33, v33\n\t" \
    "v_mfma_f32_32x32x16_bf16 v[0:15], v[76:79], v[92:95], v[0:15]\n\t" \
    "ds_read_b128 v[64:67], v97 offset:4096\n\t" \
    "v_exp_f32 v34, v34\n\t" \
    "v_exp_f32 v35, v35\n\t" \
    "v_cvt_pk_bf16_f32 v88, v32, v33\n\t" \
    "v_cvt_pk_bf16_f32 v89, v34, v35\n\t" \
    "s_waitcnt lgkmcnt(2)\n\t" \
    "v_mfma_f32_32x32x16_bf16 v[16:31], v[56:59], v[48:51], 0\n\t" \
    "ds_read_b128 v[68:71], v97 offset:5120\n\t" \
    "v_exp_f32 v36, v36\n\t" \
    "v_exp_f32 v37, v37\n\t" \
    "v_exp_f32 v38, v38\n\t" \
    "v_exp_f32 v39, v39\n\t" \
    "v_cvt_pk_bf16_f32 v90, v36, v37\n\t" \
    "v_cvt_pk_bf16_f32 v91, v38, v39\n\t" \
    "v_mfma_f32_32x32x16_bf16 v[16:31], v[60:63], v[52:55], v[16:31]\n\t" \
    "ds_read_b128 v[72:75], v97 offset:2048\n\t" \
    "v_exp_f32 v40, v40\n\t" \
    "v_exp_f32 v41, v41\n\t" \
    "v_exp_f32 v42, v42\n\t" \
    "v_exp_f32 v43, v43\n\t" \
    "v_cvt_pk_bf16_f32 v92, v40, v41\n\t" \
    "v_cvt_pk_bf16_f32 v93, v42, v43\n\t" \
    "v_mfma_f32_32x32x16_bf16 v[0:15], v[80:83], v[88:91], v[0:15]\n\t" \
    "ds_read_b128 v[76:79], v97 offset:3072\n\t" \
    "v_exp_f32 v44, v44\n\t" \
    "v_exp_f32 v45, v45\n\t" \
    "v_exp_f32 v46, v46\n\t" \
    "v_exp_f32 v47, v47\n\t" \
    "v_cvt_pk_bf16_f32 v94, v44, v45\n\t" \
    "v_cvt_pk_bf16_f32 v95, v46, v47\n\t" \
    "v_exp_f32 v16, v16\n\t" \
    "v_exp_f32 v17, v17\n\t" \
    "v_mfma_f32_32x32x16_bf16 v[0:15], v[84:87], v[92:95], v[0:15]\n\t" \
    "ds_read_b128 v[56:59], v97 offset:8192\n\t" \
    "v_exp_f32 v18, v18\n\t" \
    "v_exp_f32 v19, v19\n\t" \
    "v_cvt_pk_bf16_f32 v88, v16, v17\n\t" \
    "v_cvt_pk_bf16_f32 v89, v18, v19\n\t" \
    "s_waitcnt lgkmcnt(2)\n\t" \
    "v_mfma_f32_32x32x16_bf16 v[32:47], v[64:67], v[48:51], 0\n\t" \
    "ds_read_b128 v[60:63], v97 offset:9216\n\t" \
    "v_exp_f32 v20, v20\n\t" \
    "v_exp_f32 v21, v21\n\t" \
    "v_exp_f32 v22, v22\n\t" \
    "v_exp_f32 v23, v23\n\t" \
    "v_cvt_pk_bf16_f32 v90, v20, v21\n\t" \
    "v_cvt_pk_bf16_f32 v91, v22, v23\n\t" \
    "v_mfma_f32_32x32x16_bf16 v[32:47], v[68:71], v[52:55], v[32:47]\n\t" \
    "ds_read_b128 v[80:83], v97 offset:6144\n\t" \
    "v_exp_f32 v24, v24\n\t" \
    "v_exp_f32 v25, v25\n\t" \
    "v_exp_f32 v26, v26\n\t" \
    "v_exp_f32 v27, v27\n\t" \
    "v_cvt_pk_bf16_f32 v92, v24, v25\n\t" \
    "v_cvt_pk_bf16_f32 v93, v26, v27\n\t" \
    "v_mfma_f32_32x32x16_bf16 v[0:15], v[72:75], v[88:91], v[0:15]\n\t" \
    "ds_read_b128 v[84:87], v97 offset:7168\n\t" \
    "v_exp_f32 v28, v28\n\t" \
    "v_exp_f32 v29, v29\n\t" \
    "v_exp_f32 v30, v30\n\t" \
    "v_exp_f32 v31, v31\n\t" \
    "v_cvt_pk_bf16_f32 v94, v28, v29\n\t" \
    "v_cvt_pk_bf16_f32 v95, v30, v31\n\t" \
    "v_exp_f32 v32, v32\n\t" \
    "v_exp_f32 v33, v33\n\t" \
    "v_mfma_f32_32x32x16_bf16 v[0:15], v[76:79], v[92:95], v[0:15]\n\t" \
    "ds_read_b128 v[64:67], v97 offset:12288\n\t" \
    "v_exp_f32 v34, v34\n\t" \
    "v_exp_f32 v35, v35\n\t" \
    "v_cvt_pk_bf16_f32 v88, v32, v33\n\t" \
    "v_cvt_pk_bf16_f32 v89, v34, v35\n\t" \
    "s_waitcnt lgkmcnt(2)\n\t" \
    "v_mfma_f32_32x32x16_bf16 v[16:31], v[56:59], v[48:51], 0\n\t" \
    "ds_read_b128 v[68:71], v97 offset:13312\n\t" \
    "v_exp_f32 v36, v36\n\t" \
    "v_exp_f32 v37, v37\n\t" \
    "v_exp_f32 v38, v38\n\t" \
    "v_exp_f32 v39, v39\n\t" \
    "v_cvt_pk_bf16_f32 v90, v36, v37\n\t" \
    "v_cvt_pk_bf16_f32 v91, v38, v39\n\t" \
    "v_mfma_f32_32x32x16_bf16 v[16:31], v[60:63], v[52:55], v[16:31]\n\t" \
    "ds_read_b128 v[72:75], v97 offset:10240\n\t" \
    "v_exp_f32 v40, v40\n\t" \
    "v_exp_f32 v41, v41\n\t" \
    "v_exp_f32 v42, v42\n\t" \
    "v_exp_f32 v43, v43\n\t" \
    "v_cvt_pk_bf16_f32 v92, v40, v41\n\t" \
    "v_cvt_pk_bf16_f32 v93, v42, v43\n\t" \
    "v_mfma_f32_32x32x16_bf16 v[0:15], v[80:83], v[88:91], v[0:15]\n\t" \
    "ds_read_b128 v[76:79], v97 offset:11264\n\t" \
    "v_exp_f32 v44, v44\n\t" \
    "v_exp_f32 v45, v45\n\t" \
    "v_exp_f32 v46, v46\n\t" \
    "v_exp_f32 v47, v47\n\t" \
    "v_cvt_pk_bf16_f32 v94, v44, v45\n\t" \
    "v_cvt_pk_bf16_f32 v95, v46, v47\n\t" \
    "s_waitcnt vmcnt(0)\n\t" \
    "s_barrier\n\t" \
    "v_exp_f32 v16, v16\n\t" \
    "v_exp_f32 v17, v17\n\t" \
    "v_mfma_f32_32x32x16_bf16 v[0:15], v[84:87], v[92:95], v[0:15]\n\t" \
    "ds_read_b128 v[56:59], v97 offset:32768\n\t" \
    "v_exp_f32 v18, v18\n\t" \
    "v_exp_f32 v19, v19\n\t" \
    "v_cvt_pk_bf16_f32 v88, v16, v17\n\t" \
    "v_cvt_pk_bf16_f32 v89, v18, v19\n\t" \
    "s_waitcnt lgkmcnt(2)\n\t" \
    "v_mfma_f32_32x32x16_bf16 v[32:47], v[64:67], v[48:51], 0\n\t" \
    "ds_read_b128 v[60:63], v97 offset:33792\n\t" \
    "v_exp_f32 v20, v20\n\t" \
    "v_exp_f32 v21, v21\n\t" \
    "v_exp_f32 v22, v22\n\t" \
    "v_exp_f32 v23, v23\n\t" \
    "v_cvt_pk_bf16_f32 v90, v20, v21\n\t" \
    "v_cvt_pk_bf16_f32 v91, v22, v23\n\t" \
    "v_mfma_f32_32x32x16_bf16 v[32:47], v[68:71], v[52:55], v[32:47]\n\t" \
    "ds_read_b128 v[80:83], v97 offset:14336\n\t" \
    "v_exp_f32 v24, v24\n\t" \
    "v_exp_f32 v25, v25\n\t" \
    "v_exp_f32 v26, v26\n\t" \
    "v_exp_f32 v27, v27\n\t" \
    "v_cvt_pk_bf16_f32 v92, v24, v25\n\t" \
    "v_cvt_pk_bf16_f32 v93, v26, v27\n\t" \
    "v_mfma_f32_32x32x16_bf16 v[0:15], v[72:75], v[88:91], v[0:15]\n\t" \
    "ds_read_b128 v[84:87], v97 offset:15360\n\t" \
    "v_exp_f32 v28, v28\n\t" \
    "v_exp_f32 v29, v29\n\t" \
    "v_exp_f32 v30, v30\n\t" \
    "v_exp_f32 v31, v31\n\t" \
    "v_cvt_pk_bf16_f32 v94, v28, v29\n\t" \
    "v_cvt_pk_bf16_f32 v95, v30, v31\n\t" \
    "v_exp_f32 v32, v32\n\t" \
    "v_exp_f32 v33, v33\n\t" \
    "v_mfma_f32_32x32x16_bf16 v[0:15], v[76:79], v[92:95], v[0:15]\n\t" \
    "ds_read_b128 v[64:67], v97 offset:36864\n\t" \
    "v_exp_f32 v34, v34\n\t" \
    "v_exp_f32 v35, v35\n\t" \
    "v_cvt_pk_bf16_f32 v88, v32, v33\n\t" \
    "v_cvt_pk_bf16_f32 v89, v34, v35\n\t" \
    "s_waitcnt lgkmcnt(2)\n\t" \
    "v_mfma_f32_32x32x16_bf16 v[16:31], v[56:59], v[48:51], 0\n\t" \
    "ds_read_b128 v[68:71], v97 offset:37888\n\t" \
    "v_exp_f32 v36, v36\n\t" \
    "v_exp_f32 v37, v37\n\t" \
    "v_exp_f32 v38, v38\n\t" \
    "v_exp_f32 v39, v39\n\t" \
    "v_cvt_pk_bf16_f32 v90, v36, v37\n\t" \
    "v_cvt_pk_bf16_f32 v91, v38, v39\n\t" \
    "v_mfma_f32_32x32x16_bf16 v[16:31], v[60:63], v[52:55], v[16:31]\n\t" \
    "ds_read_b128 v[72:75], v97 offset:34816\n\t" \
    "v_exp_f32 v40, v40\n\t" \
    "v_exp_f32 v41, v41\n\t" \
    "v_exp_f32 v42, v42\n\t" \
    "v_exp_f32 v43, v43\n\t" \
    "v_cvt_pk_bf16_f32 v92, v40, v41\n\t" \
    "v_cvt_pk_bf16_f32 v93, v42, v43\n\t" \
    "v_mfma_f32_32x32x16_bf16 v[0:15], v[80:83], v[88:91], v[0:15]\n\t" \
    "ds_read_b128 v[76:79], v97 offset:35840\n\t" \
    "v_exp_f32 v44, v44\n\t" \
    "v_exp_f32 v45, v45\n\t" \
    "v_exp_f32 v46, v46\n\t" \
    "v_exp_f32 v47, v47\n\t" \
    "v_cvt_pk_bf16_f32 v94, v44, v45\n\t" \
    "v_cvt_pk_bf16_f32 v95, v46, v47\n\t" \
    "v_exp_f32 v16, v16\n\t" \
    "v_exp_f32 v17, v17\n\t" \
    "v_mfma_f32_32x32x16_bf16 v[0:15], v[84:87], v[92:95], v[0:15]\n\t" \
    "ds_read_b128 v[56:59], v97 offset:40960\n\t" \
    "v_exp_f32 v18, v18\n\t" \
    "v_exp_f32 v19, v19\n\t" \
    "v_cvt_pk_bf16_f32 v88, v16, v17\n\t" \
    "v_cvt_pk_bf16_f32 v89, v18, v19\n\t" \
    "s_waitcnt lgkmcnt(2)\n\t" \
    "v_mfma_f32_32x32x16_bf16 v[32:47], v[64:67], v[48:51], 0\n\t" \
    "ds_read_b128 v[60:63], v97 offset:41984\n\t" \
    "v_exp_f32 v20, v20\n\t" \
    "v_exp_f32 v21, v21\n\t" \
    "v_exp_f32 v22, v22\n\t" \
    "v_exp_f32 v23, v23\n\t" \
    "v_cvt_pk_bf16_f32 v90, v20, v21\n\t" \
    "v_cvt_pk_bf16_f32 v91, v22, v23\n\t" \
    "v_mfma_f32_32x32x16_bf16 v[32:47], v[68:71], v[52:55], v[32:47]\n\t" \
    "ds_read_b128 v[80:83], v97 offset:38912\n\t" \
    "v_exp_f32 v24, v24\n\t" \
    "v_exp_f32 v25, v25\n\t" \
    "v_exp_f32 v26, v26\n\t" \
    "v_exp_f32 v27, v27\n\t" \
    "v_cvt_pk_bf16_f32 v92, v24, v25\n\t" \
    "v_cvt_pk_bf16_f32 v93, v26, v27\n\t" \
    "v_mfma_f32_32x32x16_bf16 v[0:15], v[72:75], v[88:91], v[0:15]\n\t" \
    "ds_read_b128 v[84:87], v97 offset:39936\n\t" \
    "v_exp_f32 v28, v28\n\t" \
    "v_exp_f32 v29, v29\n\t" \
    "v_exp_f32 v30, v30\n\t" \
    "v_exp_f32 v31, v31\n\t" \
    "v_cvt_pk_bf16_f32 v94, v28, v29\n\t" \
    "v_cvt_pk_bf16_f32 v95, v30, v31\n\t" \
    "v_exp_f32 v32, v32\n\t" \
    "v_exp_f32 v33, v33\n\t" \
    "v_mfma_f32_32x32x16_bf16 v[0:15], v[76:79], v[92:95], v[0:15]\n\t" \
    "ds_read_b128 v[64:67], v97 offset:45056\n\t" \
    "v_exp_f32 v34, v34\n\t" \
    "v_exp_f32 v35, v35\n\t" \
    "v_cvt_pk_bf16_f32 v88, v32, v33\n\t" \
    "v_cvt_pk_bf16_f32 v89, v34, v35\n\t" \
    "s_waitcnt lgkmcnt(2)\n\t" \
    "v_mfma_f32_32x32x16_bf16 v[16:31], v[56:59], v[48:51], 0\n\t" \
    "ds_read_b128 v[68:71], v97 offset:46080\n\t" \
    "v_exp_f32 v36, v36\n\t" \
    "v_exp_f32 v37, v37\n\t" \
    "v_exp_f32 v38, v38\n\t" \
    "v_exp_f32 v39, v39\n\t" \
    "v_cvt_pk_bf16_f32 v90, v36, v37\n\t" \
    "v_cvt_pk_bf16_f32 v91, v38, v39\n\t" \
    "v_mfma_f32_32x32x16_bf16 v[16:31], v[60:63], v[52:55], v[16:31]\n\t" \
    "ds_read_b128 v[72:75], v97 offset:43008\n\t" \
    "v_exp_f32 v40, v40\n\t" \
    "v_exp_f32 v41, v41\n\t" \
    "v_exp_f32 v42, v42\n\t" \
    "v_exp_f32 v43, v43\n\t" \
    "v_cvt_pk_bf16_f32 v92, v40, v41\n\t" \
    "v_cvt_pk_bf16_f32 v93, v42, v43\n\t" \
    "v_mfma_f32_32x32x16_bf16 v[0:15], v[80:83], v[88:91], v[0:15]\n\t" \
    "ds_read_b128 v[76:79], v97 offset:44032\n\t" \
    "v_exp_f32 v44, v44\n\t" \
    "v_exp_f32 v45, v45\n\t" \
    "v_exp_f32 v46, v46\n\t" \
    "v_exp_f32 v47, v47\n\t" \
    "v_cvt_pk_bf16_f32 v94, v44, v45\n\t" \
    "v_cvt_pk_bf16_f32 v95, v46, v47\n\t" \
    "v_exp_f32 v16, v16\n\t" \
    "v_exp_f32 v17, v17\n\t" \
    "v_mfma_f32_32x32x16_bf16 v[0:15], v[84:87], v[92:95], v[0:15]\n\t" \
    "v_exp_f32 v18, v18\n\t" \
    "v_exp_f32 v19, v19\n\t" \
    "v_cvt_pk_bf16_f32 v88, v16, v17\n\t" \
    "v_cvt_pk_bf16_f32 v89, v18, v19\n\t" \
    "s_waitcnt lgkmcnt(1)\n\t" \
    "v_mfma_f32_32x32x16_bf16 v[32:47], v[64:67], v[48:51], 0\n\t" \
    "v_exp_f32 v20, v20\n\t" \
    "v_exp_f32 v21, v21\n\t" \
    "v_exp_f32 v22, v22\n\t" \
    "v_exp_f32 v23, v23\n\t" \
    "v_cvt_pk_bf16_f32 v90, v20, v21\n\t" \
    "v_cvt_pk_bf16_f32 v91, v22, v23\n\t" \
    "v_mfma_f32_32x32x16_bf16 v[32:47], v[68:71], v[52:55], v[32:47]\n\t" \
    "ds_read_b128 v[80:83], v97 offset:47104\n\t" \
    "v_exp_f32 v24, v24\n\t" \
    "v_exp_f32 v25, v25\n\t" \
    "v_exp_f32 v26, v26\n\t" \
    "v_exp_f32 v27, v27\n\t" \
    "v_cvt_pk_bf16_f32 v92, v24, v25\n\t" \
    "v_cvt_pk_bf16_f32 v93, v26, v27\n\t" \
    "v_mfma_f32_32x32x16_bf16 v[0:15], v[72:75], v[88:91], v[0:15]\n\t" \
    "ds_read_b128 v[84:87], v97 offset:48128\n\t" \
    "v_exp_f32 v28, v28\n\t" \
    "v_exp_f32 v29, v29\n\t" \
    "v_exp_f32 v30, v30\n\t" \
    "v_exp_f32 v31, v31\n\t" \
    "v_cvt_pk_bf16_f32 v94, v28, v29\n\t" \
    "v_cvt_pk_bf16_f32 v95, v30, v31\n\t" \
    "v_exp_f32 v32, v32\n\t" \
    "v_exp_f32 v33, v33\n\t" \
    "v_mfma_f32_32x32x16_bf16 v[0:15], v[76:79], v[92:95], v[0:15]\n\t" \
    "v_exp_f32 v34, v34\n\t" \
    "v_exp_f32 v35, v35\n\t" \
    "v_cvt_pk_bf16_f32 v88, v32, v33\n\t" \
    "v_cvt_pk_bf16_f32 v89, v34, v35\n\t" \
    "s_waitcnt lgkmcnt(1)\n\t" \
    "v_exp_f32 v36, v36\n\t" \
    "v_exp_f32 v37, v37\n\t" \
    "v_exp_f32 v38, v38\n\t" \
    "v_exp_f32 v39, v39\n\t" \
    "v_cvt_pk_bf16_f32 v90, v36, v37\n\t" \
    "v_cvt_pk_bf16_f32 v91, v38, v39\n\t" \
    "v_exp_f32 v40, v40\n\t" \
    "v_exp_f32 v41, v41\n\t" \
    "v_exp_f32 v42, v42\n\t" \
    "v_exp_f32 v43, v43\n\t" \
    "v_cvt_pk_bf16_f32 v92, v40, v41\n\t" \
    "v_cvt_pk_bf16_f32 v93, v42, v43\n\t" \
    "v_mfma_f32_32x32x16_bf16 v[0:15], v[80:83], v[88:91], v[0:15]\n\t" \
    "v_exp_f32 v44, v44\n\t" \
    "v_exp_f32 v45, v45\n\t" \
    "v_exp_f32 v46, v46\n\t" \
    "v_exp_f32 v47, v47\n\t" \
    "v_cvt_pk_bf16_f32 v94, v44, v45\n\t" \
    "v_cvt_pk_bf16_f32 v95, v46, v47\n\t" \
    "s_waitcnt lgkmcnt(0)\n\t" \
    "s_nop 0\n\t" \
    "v_mfma_f32_32x32x16_bf16 v[0:15], v[84:87], v[92:95], v[0:15]\n\t" \
    "s_mov_b32 m0, s43\n\t" \
    "s_nop 7\n\t" \
    "s_nop 7\n\t"
#define ATTN_FAST_CLOBBERS "v16", "v17", "v18", "v19", "v20", "v21", "v22", "v23", "v24", "v25", "v26", "v27", "v28", "v29", "v30", "v31", "v32", "v33", "v34", "v35", "v36", "v37", "v38", "v39", "v40", "v41", "v42", "v43", "v44", "v45", "v46", "v47", "v56", "v57", "v58", "v59", "v60", "v61", "v62", "v63", "v64", "v65", "v66", "v67", "v68", "v69", "v70", "v71", "v72", "v73", "v74", "v75", "v76", "v77", "v78", "v79", "v80", "v81", "v82", "v83", "v84", "v85", "v86", "v87", "v88", "v89", "v90", "v91", "v92", "v93", "v94", "v95", "s43", "scc", "memory"

constexpr int A_SLOT = 28672;
constexpr int A_XS = 3 * A_SLOT;
constexpr int A_XROW = 464;
constexpr int L_Q = A_XS + 128 * A_XROW;
constexpr int L_CTL = L_Q + 8192;
constexpr int L_TOTAL = L_CTL + 256;
constexpr int B_RED = 131072;
constexpr int C_RROW = 272;

DI float gelu_tanh(float h) {
    const float c0 = -2.0f * 0.7978845608028654f * 1.4426950408889634f, c1 = c0 * 0.044715f;
    const float z = h * __builtin_fmaf(h * h, c1, c0);
    const float e = __builtin_amdgcn_exp2f(z);
    return h * __builtin_amdgcn_rcpf(1.0f + e);
}

DI void phaseA(const char* __restrict__ x, const char* __restrict__ wqkv, const char* __restrict__ w1, const char* __restrict__ b1,
               char* __restrict__ ws, char* lds_generic, int Rb, int c, u64& tag) {
    LAS char* lds = (LAS char*)lds_generic;
    const unsigned lds0 = (unsigned)(uintptr_t)lds_generic;
    const int tid = threadIdx.x, l = tid & 63, w = rfl(tid >> 6) & 7, r = l & 31, h = l >> 5;
    const int tt = w & 3, half = w >> 2;
    STAMP(ws, 0);
    u32x2 e0; { const unsigned z = 0; asm volatile("global_load_dwordx2 %0, %1, %2 sc1" : "=v"(e0) : "v"(z), "s"(uptr(ws + WS_EPOCH)) : "memory"); }
    u32x4 xr[2][7];
    {
        const char* xb = x + (size_t)(Rb * 128 + 16 * w) * 896;
        const unsigned vo = (l >> 3) * 896 + (l & 7) * 16;
#pragma unroll
        for (int g = 0; g < 2; ++g)
#pragma unroll
            for (int i = 0; i < 7; ++i) xr[g][i] = gload16(xb + g * 8 * 896 + i * 128, vo);
    }
    {
        const unsigned vo = (l >> 2) * 2688 + ((l & 3) < 3 ? (l & 3) * 16 : 40);
#pragma unroll
        for (int i = 0; i < 11; ++i) {
            const int n = w + 8 * i;
            if (n < 84) { const int m = n / 28, kb = n % 28;
                glds16(wqkv + (size_t)kb * 16 * 2688 + m * 896 + c * 56, vo, lds0 + m * A_SLOT + kb * 1024); }
        }
    }
    asm volatile("s_waitcnt vmcnt(10)" : "+v"(e0), "+v"(xr[0][0]), "+v"(xr[0][1]), "+v"(xr[0][2]), "+v"(xr[0][3]), "+v"(xr[0][4]), "+v"(xr[0][5]), "+v"(xr[0][6]),
                 "+v"(xr[1][0]), "+v"(xr[1][1]), "+v"(xr[1][2]), "+v"(xr[1][3]), "+v"(xr[1][4]), "+v"(xr[1][5]), "+v"(xr[1][6]) :: "memory");
    tag = (((u64)rfl(e0[1]) << 32) | rfl(e0[0])) + 0x9E3779B97F4A7C15ull;
    STAMP(ws, 1);
#pragma unroll
    for (int g = 0; g < 2; ++g) {
        float sm = 0.f, sq = 0.f; unsigned ones_ = 0x3f803f80u; asm volatile("" : "+v"(ones_)); const bf16x2 one = asbf2(ones_);
#pragma unroll
        for (int i = 0; i < 7; ++i)
#pragma unroll
            for (int e = 0; e < 4; ++e) { const bf16x2 v = asbf2(xr[g][i][e]); sm = __builtin_amdgcn_fdot2_f32_bf16(v, one, sm, false); sq = __builtin_amdgcn_fdot2_f32_bf16(v, v, sq, false); }
        sm += dppf<0xB1>(sm); sq += dppf<0xB1>(sq); sm += dppf<0x4E>(sm); sq += dppf<0x4E>(sq); sm += dppf<0x141>(sm); sq += dppf<0x141>(sq);
        const float mean = sm * (1.0f / 448.0f); const float var = __builtin_fmaxf(sq * (1.0f / 448.0f) - mean * mean, 0.f);
        const float rstd = __builtin_amdgcn_rsqf(var + 1e-5f), nb = -mean * rstd;
#pragma unroll
        for (int i = 0; i < 7; ++i)
#pragma unroll
            for (int e = 0; e < 4; ++e) { const unsigned v = xr[g][i][e]; xr[g][i][e] = cvtpk(__builtin_fmaf(bflo(v), rstd, nb), __builtin_fmaf(bfhi(v), rstd, nb)); }
    }
    STAMP(ws, 2);
    bf16x8 xf[28];
    {
        LAS char* xs = lds + A_XS; const int sub = l & 7;
        const int wrow = 16 * w + (l >> 3);
        const LAS char* rd = xs + (32 * tt + r) * A_XROW + h * 16;
#pragma unroll
        for (int g = 0; g < 2; ++g) {
#pragma unroll
            for (int i = 0; i < 3; ++i) *(LAS u32x4*)(xs + (wrow + 8 * g) * A_XROW + (sub + 8 * i) * 16) = xr[g][i];
            if (sub < 4) *(LAS u32x4*)(xs + (wrow + 8 * g) * A_XROW + (sub + 24) * 16) = xr[g][3];
        }
        WAIT_LGKM0(); BARRIER();
        STAMP(ws, 15);
#pragma unroll
        for (int s = 0; s < 14; ++s) xf[s] = ldsfrag(rd + s * 32);
        WAIT_LGKM0(); BARRIER();
        STAMP(ws, 16);
#pragma unroll
        for (int g = 0; g < 2; ++g) {
            if (sub >= 4) *(LAS u32x4*)(xs + (wrow + 8 * g) * A_XROW + (sub - 4) * 16) = xr[g][3];
#pragma unroll
            for (int i = 4; i < 7; ++i) *(LAS u32x4*)(xs + (wrow + 8 * g) * A_XROW + (sub + 8 * i - 28) * 16) = xr[g][i];
        }
        WAIT_LGKM0(); BARRIER();
#pragma unroll
        for (int s = 0; s < 14; ++s) xf[14 + s] = ldsfrag(rd + s * 32);
        WAIT_LGKM0();
        STAMP(ws, 17);
        WAIT_VM(0);
        STAMP(ws, 18);
        BARRIER();
    }
    STAMP(ws, 3);
    const bool ffn = c < 14;
    const unsigned vo1 = (l >> 2) * 3584 + (l & 3) * 16;
    if (ffn) {
#pragma unroll
        for (int i = 0; i < 7; ++i) { const int n = w + 8 * i, t = n / 28, kb = n % 28;
            glds16(w1 + (size_t)kb * 16 * 3584 + (4 * c + t) * 64, vo1, lds0 + A_XS + t * A_SLOT + kb * 1024); }
    }
    const int troff = (8 * h + ((l & 15) >> 2)) * 64 + ((l >> 4) & 1) * 32 + (l & 3) * 8;
    const int tglob = Rb * 4 + tt;
    const unsigned vl = l * 16;
    if (half == 0) {
        f32x16 aq = {}, ak = {};
        const LAS char* pq = lds + troff; const LAS char* pk = lds + A_SLOT + troff;
#pragma unroll
        for (int s = 0; s < 28; ++s) {
            aq = MFMA(trfrag(pq + s * 1024, pq + s * 1024 + 256), xf[s], aq);
            ak = MFMA(trfrag(pk + s * 1024, pk + s * 1024 + 256), xf[s], ak);
        }
        const float qs = 0.18898223650461361f * 1.4426950408889634f;
        float nk = 0.f;
#pragma unroll
        for (int i = 0; i < 16; ++i) { const bool pad = (i >= 12) && (h == 0); aq[i] = pad ? 0.f : aq[i] * qs; const float kv = pad ? 0.f : ak[i]; nk = __builtin_fmaf(kv, kv, nk); ak[i] = kv; }
        if (h == 0) ak[12] = 1.0f;
        nk = row32_max(xhalf_sum(nk));
        char* kd = ws + WS_KV + (size_t)(c * 64 + tglob) * 4096;
        gstore16_sc1(kd, vl, pack8(ak, 0)); gstore16_sc1(kd + 1024, vl, pack8(ak, 1));
        if (l == 0) __hip_atomic_store((__attribute__((address_space(1))) unsigned*)(ws + WS_KN + (c * 64 + tglob) * 4), __float_as_uint(nk), __ATOMIC_RELAXED, __HIP_MEMORY_SCOPE_AGENT);
        *(LAS u32x4*)(lds + L_Q + tt * 2048 + vl) = pack8(aq, 0); *(LAS u32x4*)(lds + L_Q + tt * 2048 + 1024 + vl) = pack8(aq, 1);
    } else {
        f32x16 av = {};
        const LAS char* pv = lds + 2 * A_SLOT + troff;
#pragma unroll
        for (int s = 0; s < 28; ++s) av = MFMA(xf[s], trfrag(pv + s * 1024, pv + s * 1024 + 256), av);
        const bool pad = (r >= 24) && (r < 28); const float pv1 = (r == 24) ? 1.0f : 0.f;
#pragma unroll
        for (int i = 0; i < 16; ++i) av[i] = pad ? pv1 : av[i];
        char* vd = ws + WS_KV + (size_t)(c * 64 + tglob) * 4096 + 2048;
        gstore16_sc1(vd, vl, pack8(av, 0)); gstore16_sc1(vd + 1024, vl, pack8(av, 1));
    }
    STAMP(ws, 4);
    WAIT_LGKM0(); WAIT_VM(0); BARRIER();
    if (tid == 0) flag_store(ws, WS_FKV + c * 256 + Rb * 8, tag);
    STAMP(ws, 5);
    if (!ffn) return;
#pragma unroll
    for (int i = 0; i < 7; ++i) { const int n = w + 8 * i, t = n / 28, kb = n % 28;
        glds16(w1 + (size_t)kb * 16 * 3584 + (4 * c + 2 + t) * 64, vo1, lds0 + t * A_SLOT + kb * 1024); }
#pragma unroll
    for (int u = 0; u < 2; ++u) {
        const int ft = 4 * c + 2 * u + half;
        u32x2 bb[4];
#pragma unroll
        for (int g = 0; g < 4; ++g) bb[g] = *(const u32x2*)(b1 + (ft * 32 + 8 * g + 4 * h) * 2);
        if (u == 1) { WAIT_VM(0); BARRIER(); }
        f32x16 a = {};
        const LAS char* pw = lds + (u == 0 ? A_XS : 0) + half * A_SLOT + troff;
#pragma unroll
        for (int s = 0; s < 28; ++s) a = MFMA(trfrag(pw + s * 1024, pw + s * 1024 + 256), xf[s], a);
#pragma unroll
        for (int g = 0; g < 4; ++g) {
            a[4 * g + 0] = gelu_tanh(a[4 * g + 0] + bflo(bb[g][0])); a[4 * g + 1] = gelu_tanh(a[4 * g + 1] + bfhi(bb[g][0]));
            a[4 * g + 2] = gelu_tanh(a[4 * g + 2] + bflo(bb[g][1])); a[4 * g + 3] = gelu_tanh(a[4 * g + 3] + bfhi(bb[g][1]));
        }
        char* hd = ws + WS_H + ((size_t)tglob * 56 + ft) * 2048;
        gstore16_sc1(hd, vl, pack8(a, 0)); gstore16_sc1(hd + 1024, vl, pack8(a, 1));
        STAMP(ws, 6 + u);
    }
    WAIT_VM(0); BARRIER();
    if (tid == 0) flag_store(ws, WS_FH + Rb * 256 + c * 8, tag);
}

#define EX2(v) __builtin_amdgcn_exp2f(v)
DI void attn_safe(f32x16& o, const bf16x8 q0, const bf16x8 q1, const char* src, unsigned dst, unsigned vo, const LAS char* rb, int l) {
    float m = -1e30f;
    const f32x16 zero = {};
    WAIT_VM(0); BARRIER();
    for (int i = 0; i < 8; ++i) {
        const LAS char* sb = rb + (i & 3) * 32768;
#pragma unroll 1
        for (int t = 0; t < 4; ++t) {
            f32x16 sc = MFMA(ldsfrag(sb + t * 4096), q0, zero); sc = MFMA(ldsfrag(sb + t * 4096 + 1024), q1, sc);
            float mx = sc[0];
#pragma unroll
            for (int e = 1; e < 16; ++e) mx = __builtin_fmaxf(mx, sc[e]);
            mx = xhalf_max(mx);
            const float mn = __builtin_fmaxf(m, mx), al = EX2(m - mn); m = mn;
#pragma unroll
            for (int e = 0; e < 16; ++e) { o[e] *= al; sc[e] = EX2(sc[e] - mn); }
            o = MFMA(ldsfrag(sb + t * 4096 + 2048), asbf8(pack8(sc, 0)), o); o = MFMA(ldsfrag(sb + t * 4096 + 3072), asbf8(pack8(sc, 1)), o);
        }
        WAIT_LGKM0(); WAIT_VM(0); BARRIER();
        if (i + 4 < 8) {
#pragma unroll
            for (int e = 0; e < 4; ++e) glds16(src + (size_t)(i + 4) * 16384 + e * 1024, vo, dst + (i & 3) * 32768 + e * 1024);
        }
    }
    o[13] = (l < 32) ? m : o[13];
}

DI void phaseB(char* __restrict__ ws, char* lds_generic, int hd, int qb, u64 tag) {
    LAS char* lds = (LAS char*)lds_generic;
    const unsigned lds0 = (unsigned)(uintptr_t)lds_generic;
    const int tid = threadIdx.x, l = tid & 63, w = rfl(tid >> 6) & 7, h = l >> 5;
    const int qt = w & 3, kh = w >> 2, tglob = qb * 4 + qt;
    STAMP(ws, 10);
    if (w == 0) {
        flag_wait(ws, WS_FKV + hd * 256, 16, tag, l);
        __builtin_amdgcn_fence(__ATOMIC_ACQUIRE, "agent");
        WAIT_VM(0);
    }
    if (tid == 64) *(LAS unsigned*)(lds + L_CTL) = 0u;
    WAIT_LGKM0(); BARRIER();
    STAMP(ws, 11);
    const unsigned vo = l * 16;
    const char* src = ws + WS_KV + (size_t)hd * 262144 + (size_t)kh * 131072 + qt * 4096;
    const unsigned dst = lds0 + kh * 16384 + qt * 4096;
    float kn = *(const float*)(ws + WS_KN + (hd * 64 + l) * 4);
    u32x4 q0 = *(const LAS u32x4*)(lds + L_Q + qt * 2048 + vo), q1 = *(const LAS u32x4*)(lds + L_Q + qt * 2048 + 1024 + vo);
    kn = xhalf_max(row32_max(kn));
    float qn = 0.f;
#pragma unroll
    for (int e = 0; e < 4; ++e) { qn = __builtin_amdgcn_fdot2_f32_bf16(asbf2(q0[e]), asbf2(q0[e]), qn, false); qn = __builtin_amdgcn_fdot2_f32_bf16(asbf2(q1[e]), asbf2(q1[e]), qn, false); }
    qn = xhalf_sum(qn);
    const float bound = __builtin_sqrtf(qn * kn) * 1.02f + 1e-3f;
    if (__any(!(bound <= 48.0f)) && l == 0) *(LAS unsigned*)(lds + L_CTL) = 1u;
#pragma unroll
    for (int i = 0; i < 4; ++i)
#pragma unroll
        for (int e = 0; e < 4; ++e) glds16(src + (size_t)i * 16384 + e * 1024, vo, dst + i * 32768 + e * 1024);
    WAIT_LGKM0(); BARRIER();
    const bool safe = rfl(*(const LAS unsigned*)(lds + L_CTL)) != 0u;
    f32x16 o;
    STAMP(ws, 12); STAMPC(ws, 28);
    if (!safe) {
        if (h == 0) q1[2] = cvtpk(-bound, 0.f);
        const unsigned ad0 = lds0 + kh * 16384 + vo, ad1 = ad0 + 65536;
        const char* s3 = (const char*)uptr(src + 3 * 16384);
        const unsigned dsts = rfl(dst);
        asm volatile(ATTN_FAST_ASM
                     : "={v[0:15]}"(o), "+{s[40:41]}"(s3)
                     : "{v[48:51]}"(q0), "{v[52:55]}"(q1), "{v96}"(ad0), "{v97}"(ad1), "{v98}"(vo), "{s42}"(dsts)
                     : ATTN_FAST_CLOBBERS);
    } else {
        o = (f32x16){};
        attn_safe(o, asbf8(q0), asbf8(q1), src, dst, vo, lds + kh * 16384 + vo, l);
    }
    STAMP(ws, 13); STAMPC(ws, 29);
    LAS char* red = lds + B_RED + qt * 4096 + vo;
    if (kh == 1) {
#pragma unroll
        for (int g = 0; g < 4; ++g) *(LAS f32x4*)(red + g * 1024) = (f32x4){o[4 * g], o[4 * g + 1], o[4 * g + 2], o[4 * g + 3]};
    }
    WAIT_LGKM0(); BARRIER();
    if (kh == 0) {
        f32x16 p;
#pragma unroll
        for (int g = 0; g < 4; ++g) { const f32x4 t = *(const LAS f32x4*)(red + g * 1024); p[4 * g] = t[0]; p[4 * g + 1] = t[1]; p[4 * g + 2] = t[2]; p[4 * g + 3] = t[3]; }
        if (safe) {
            const float ma = swap32_lo(o[13]), mb = swap32_lo(p[13]);
            const float mn = __builtin_fmaxf(ma, mb), fa = EX2(ma - mn), fb = EX2(mb - mn);
#pragma unroll
            for (int e = 0; e < 16; ++e) o[e] = o[e] * fa + p[e] * fb;
        } else {
#pragma unroll
            for (int e = 0; e < 16; ++e) o[e] += p[e];
        }
        const float inv = 1.0f / swap32_lo(o[12]);
#pragma unroll
        for (int e = 0; e < 16; ++e) o[e] = (e >= 12 && h == 0) ? 0.f : o[e] * inv;
        char* od = ws + WS_O + ((size_t)tglob * 16 + hd) * 2048;
        gstore16_sc1(od, vo, pack8(o, 0)); gstore16_sc1(od + 1024, vo, pack8(o, 1));
    }
    WAIT_VM(0); BARRIER();
    if (tid == 0) flag_store(ws, WS_FO + qb * 256 + hd * 8, tag);
    STAMP(ws, 14);
}

DI void phaseC(const char* __restrict__ x, const char* __restrict__ wo, const char* __restrict__ w2, const char* __restrict__ b2,
               char* __restrict__ ws, char* __restrict__ out, char* lds_generic, int rb, int ng, u64 tag, bool bump) {
    LAS char* lds = (LAS char*)lds_generic;
    const unsigned lds0 = (unsigned)(uintptr_t)lds_generic;
    const int tid = threadIdx.x, l = tid & 63, w = rfl(tid >> 6) & 7, h = l >> 5;
    const int tt = w & 1, kq = w >> 1, tglob = rb * 2 + tt;
    STAMP(ws, 20);
    if (w == 0) flag_wait(ws, WS_FH + (rb >> 1) * 256, 14, tag, l);
    BARRIER();
    STAMP(ws, 21);
    const unsigned vo = (l >> 3) * 896 + (((l & 7) ^ (((l >> 4) & 1) << 2)) * 16);
    const unsigned vl = l * 16;
    const char* w2src = w2 + ng * 128 + (size_t)(448 * kq + 32 * tt) * 896;
    const unsigned wdst = lds0 + kq * 8192 + tt * 4096;
    const char* hsrc = ws + WS_H + ((size_t)tglob * 56 + 14 * kq) * 2048;
    const char* osrc = ws + WS_O + ((size_t)tglob * 16 + 4 * kq) * 2048;
    u32x4 f[9][4];
    auto issue = [&](int st) {
        if (st < 7) {
#pragma unroll
            for (int e = 0; e < 4; ++e) glds16(w2src + (size_t)(st * 64 + e * 8) * 896, vo, wdst + (st & 3) * 32768 + e * 1024);
#pragma unroll
            for (int e = 0; e < 4; ++e) f[st][e] = gload16_sc1(hsrc + st * 4096 + e * 1024, vl);
        } else {
            const int hd = 4 * kq + 2 * (st - 7) + tt; const int r0 = hd < 15 ? 28 * hd : 416;
#pragma unroll
            for (int e = 0; e < 4; ++e) glds16(wo + ng * 128 + (size_t)(r0 + e * 8) * 896, vo, wdst + (st & 3) * 32768 + e * 1024);
#pragma unroll
            for (int e = 0; e < 4; ++e) f[st][e] = gload16_sc1(osrc + (st - 7) * 4096 + e * 1024, vl);
        }
    };
    issue(0); issue(1); issue(2); issue(3);
    const int q = (l & 15) >> 2, p = l & 3, cb = (l >> 4) & 1;
    const int tr0 = (4 * h + q) * 128 + (((2 * cb + (p >> 1)) ^ ((q >> 1) << 2)) * 16) + (p & 1) * 8, tr1 = tr0 ^ 64;
    f32x16 a0 = {}, a1 = {};
#define PINF(st) asm volatile("" : "+v"(f[st][0]), "+v"(f[st][1]), "+v"(f[st][2]), "+v"(f[st][3]))
#pragma unroll
    for (int st = 0; st < 9; ++st) {
        if (st == 0) WAIT_VM(24); else if (st <= 6) WAIT_VM(16); else if (st == 7) WAIT_VM(8); else WAIT_VM(0);
        PINF(st);
        if (st == 4 && w == 0) flag_wait(ws, WS_FO + (rb >> 1) * 256, 16, tag, l);
        WAIT_LGKM0(); BARRIER();
        if (st >= 1 && st + 3 < 9) issue(st + 3);
        const LAS char* wb = lds + (st & 3) * 32768 + kq * 8192;
        if (st < 7) {
#pragma unroll
            for (int u = 0; u < 2; ++u)
#pragma unroll
                for (int s = 0; s < 2; ++s) {
                    const LAS char* wp = wb + u * 4096 + s * 2048;
                    a0 = MFMA(trfrag(wp + tr0, wp + tr0 + 1024), asbf8(f[st][2 * u + s]), a0);
                    a1 = MFMA(trfrag(wp + tr1, wp + tr1 + 1024), asbf8(f[st][2 * u + s]), a1);
                }
        } else {
#pragma unroll
            for (int u = 0; u < 2; ++u) {
                const int hd = 4 * kq + 2 * (st - 7) + u;
                const LAS char* wp = wb + u * 4096 + (hd == 15 ? 512 : 0);
                a0 = MFMA(trfrag(wp + tr0, wp + tr0 + 1024), asbf8(f[st][2 * u]), a0);
                a1 = MFMA(trfrag(wp + tr1, wp + tr1 + 1024), asbf8(f[st][2 * u]), a1);
                a0 = MFMA(trfrag(wp + tr0 + 2048, wp + tr0 + 2048 + 512), asbf8(f[st][2 * u + 1]), a0);
                a1 = MFMA(trfrag(wp + tr1 + 2048, wp + tr1 + 2048 + 512), asbf8(f[st][2 * u + 1]), a1);
            }
        }
    }
    STAMP(ws, 22);
    const int tok = tid >> 3, ch = tid & 7;
    const size_t goff = (size_t)(rb * 64 + tok) * 896 + ng * 128 + ch * 16;
    const u32x4 xv = *(const u32x4*)(x + goff), bv = *(const u32x4*)(b2 + ng * 128 + ch * 16);
    WAIT_LGKM0(); BARRIER();
    {
        LAS char* rw = lds + (kq * 64 + tt * 32 + (l & 31)) * C_RROW + h * 16;
#pragma unroll
        for (int g = 0; g < 4; ++g) {
            *(LAS f32x4*)(rw + g * 32) = (f32x4){a0[4 * g], a0[4 * g + 1], a0[4 * g + 2], a0[4 * g + 3]};
            *(LAS f32x4*)(rw + 128 + g * 32) = (f32x4){a1[4 * g], a1[4 * g + 1], a1[4 * g + 2], a1[4 * g + 3]};
        }
    }
    WAIT_LGKM0(); BARRIER();
    f32x4 s0 = {}, s1 = {};
#pragma unroll
    for (int k = 0; k < 4; ++k) { const LAS char* rr = lds + (k * 64 + tok) * C_RROW + ch * 32; s0 += *(const LAS f32x4*)rr; s1 += *(const LAS f32x4*)(rr + 16); }
    u32x4 ov;
    ov[0] = cvtpk(s0[0] + bflo(xv[0]) + bflo(bv[0]), s0[1] + bfhi(xv[0]) + bfhi(bv[0]));
    ov[1] = cvtpk(s0[2] + bflo(xv[1]) + bflo(bv[1]), s0[3] + bfhi(xv[1]) + bfhi(bv[1]));
    ov[2] = cvtpk(s1[0] + bflo(xv[2]) + bflo(bv[2]), s1[1] + bfhi(xv[2]) + bfhi(bv[2]));
    ov[3] = cvtpk(s1[2] + bflo(xv[3]) + bflo(bv[3]), s1[3] + bfhi(xv[3]) + bfhi(bv[3]));
    *(u32x4*)(out + goff) = ov;
    if (bump && tid == 0) flag_store(ws, WS_EPOCH, tag);
    STAMP(ws, 23);
}

__global__ void __launch_bounds__(512, 2) fused_block(const char* x_, const char* wqkv_, const char* wo_, const char* w1_, const char* b1_,
                                                      const char* w2_, const char* b2_, char* out_, char* ws_) {
    unsigned long long t_entry; asm volatile("s_memrealtime %0" : "=s"(t_entry) :: "memory");
    const char* const __attribute__((address_space(4)))* ka = (const char* const __attribute__((address_space(4)))*)__builtin_amdgcn_kernarg_segment_ptr();
    asm volatile("" : "+s"(ka) :: "memory");
    const char* x = ka[0]; const char* wqkv = ka[1]; const char* wo = ka[2]; const char* w1 = ka[3]; const char* b1 = ka[4]; const char* w2 = ka[5]; const char* b2 = ka[6]; char* out = (char*)ka[7]; char* ws = (char*)ka[8];
    extern __shared__ __attribute__((aligned(16))) char lds[];
    const int b = blockIdx.x, xcd = b & 7, i = b >> 3;
    const int Rb = i >> 1, c = 2 * xcd + (i & 1);
    u64 tag;
    phaseA(x, wqkv, w1, b1, ws, lds, Rb, c, tag);
    phaseB(ws, lds, c, Rb, tag);
    if (i < 28) phaseC(x, wo, w2, b2, ws, out, lds, 4 * xcd + i / 7, i % 7, tag, b == 0);
    if (b == 0) while (__builtin_amdgcn_s_memrealtime() - t_entry < 6000ull) __builtin_amdgcn_s_sleep(1);
}

extern "C" void kernel_launch(void* const* d_in, const int* in_sizes, int n_in, void* d_out, int out_size, void* d_ws, size_t ws_size, hipStream_t stream) {
    const char* x = (const char*)d_in[0]; const char* wqkv = (const char*)d_in[1]; const char* wo = (const char*)d_in[2];
    const char* w1 = (const char*)d_in[4]; const char* b1 = (const char*)d_in[5]; const char* w2 = (const char*)d_in[6]; const char* b2 = (const char*)d_in[7];
    (void)hipFuncSetAttribute((const void*)fused_block, hipFuncAttributeMaxDynamicSharedMemorySize, L_TOTAL);
    hipLaunchKernelGGL(fused_block, dim3(256), dim3(512), L_TOTAL, stream, x, wqkv, wo, w1, b1, w2, b2, (char*)d_out, (char*)d_ws);
}
```

```cpp
#include <hip/hip_runtime.h>
#include <hip/hip_bf16.h>
#include <stdint.h>

typedef __bf16 bf16x2 __attribute__((ext_vector_type(2)));
typedef __bf16 bf16x8 __attribute__((ext_vector_type(8)));
typedef float f32x2 __attribute__((ext_vector_type(2)));
typedef float f32x4 __attribute__((ext_vector_type(4)));
typedef float f32x16 __attribute__((ext_vector_type(16)));
typedef short s16x4 __attribute__((ext_vector_type(4)));
typedef unsigned u32x2 __attribute__((ext_vector_type(2)));
typedef unsigned u32x4 __attribute__((ext_vector_type(4)));
typedef __attribute__((address_space(1))) unsigned long long gu64;
#define LAS __attribute__((address_space(3)))
#define DI __device__ __forceinline__
#define MFMA(a, b, c) __builtin_amdgcn_mfma_f32_32x32x16_bf16((a), (b), (c), 0, 0, 0)

constexpr size_t WS_EPOCH = 0;
constexpr size_t WS_FKV = 256;
constexpr size_t WS_FH = WS_FKV + 16 * 256;
constexpr size_t WS_FO = WS_FH + 16 * 256;
constexpr size_t WS_KN = 16384;
constexpr size_t WS_Q = 32768;
constexpr size_t WS_KV = WS_Q + 16 * 64 * 2048;
constexpr size_t WS_H = WS_KV + 16 * 64 * 4096;
constexpr size_t WS_O = WS_H + (size_t)64 * 56 * 2048;

DI unsigned cvtpk(float lo, float hi) { f32x2 v = {lo, hi}; bf16x2 b = __builtin_convertvector(v, bf16x2); return __builtin_bit_cast(unsigned, b); }
DI bf16x2 asbf2(unsigned v) { return __builtin_bit_cast(bf16x2, v); }
DI bf16x8 asbf8(u32x4 v) { return __builtin_bit_cast(bf16x8, v); }
DI float bflo(unsigned v) { return __uint_as_float(v << 16); }
DI float bfhi(unsigned v) { return __uint_as_float(v & 0xffff0000u); }
DI unsigned rfl(unsigned x) { return __builtin_amdgcn_readfirstlane(x); }
DI const void* uptr(const void* p) { const uint64_t v = (uint64_t)p; return (const void*)(((uint64_t)rfl((unsigned)(v >> 32)) << 32) | rfl((unsigned)v)); }
DI u32x4 gload16(const void* sbase, unsigned voff) { u32x4 r; asm volatile("s_nop 4\n\tglobal_load_dwordx4 %0, %1, %2" : "=v"(r) : "v"(voff), "s"(uptr(sbase)) : "memory"); return r; }
DI u32x4 gload16_sc1(const void* sbase, unsigned voff) { u32x4 r; asm volatile("global_load_dwordx4 %0, %1, %2 sc1" : "=v"(r) : "v"(voff), "s"(uptr(sbase)) : "memory"); return r; }
DI void gstore16_sc1(void* sbase, unsigned voff, u32x4 v) { asm volatile("global_store_dwordx4 %0, %1, %2 sc1\n\ts_nop 1" :: "v"(voff), "v"(v), "s"(uptr(sbase)) : "memory"); }
DI s16x4 trread(const LAS char* p) { return __builtin_bit_cast(s16x4, __builtin_amdgcn_ds_read_tr16_b64_v4i16((LAS s16x4*)p)); }
DI bf16x8 trfrag(const LAS char* p0, const LAS char* p1) {
    s16x4 a = trread(p0), b = trread(p1);
    return __builtin_bit_cast(bf16x8, __builtin_shufflevector(a, b, 0, 1, 2, 3, 4, 5, 6, 7));
}
DI bf16x8 ldsfrag(const LAS char* p) { return *(const LAS bf16x8*)p; }
template <int CTRL> DI float dppf(float v) { return __int_as_float(__builtin_amdgcn_update_dpp(0, __float_as_int(v), CTRL, 0xf, 0xf, true)); }
DI u32x4 pack8(const f32x16& a, int s) {
    u32x4 p = {cvtpk(a[8 * s], a[8 * s + 1]), cvtpk(a[8 * s + 2], a[8 * s + 3]), cvtpk(a[8 * s + 4], a[8 * s + 5]), cvtpk(a[8 * s + 6], a[8 * s + 7])};
    return p;
}
DI float swap32_hi(float v) { auto sw = __builtin_amdgcn_permlane32_swap(__float_as_uint(v), __float_as_uint(v), false, false); return __uint_as_float(sw[1]); }
DI float xhalf_sum(float v) { auto sw = __builtin_amdgcn_permlane32_swap(__float_as_uint(v), __float_as_uint(v), false, false); return __uint_as_float(sw[0]) + __uint_as_float(sw[1]); }
DI float xhalf_max(float v) { auto sw = __builtin_amdgcn_permlane32_swap(__float_as_uint(v), __float_as_uint(v), false, false); return __builtin_fmaxf(__uint_as_float(sw[0]), __uint_as_float(sw[1])); }
DI float row32_max(float v) {
    v = __builtin_fmaxf(v, dppf<0xB1>(v)); v = __builtin_fmaxf(v, dppf<0x4E>(v)); v = __builtin_fmaxf(v, dppf<0x141>(v)); v = __builtin_fmaxf(v, dppf<0x140>(v));
    auto sw = __builtin_amdgcn_permlane16_swap(__float_as_uint(v), __float_as_uint(v), false, false); return __builtin_fmaxf(__uint_as_float(sw[0]), __uint_as_float(sw[1]));
}
#define BARRIER() asm volatile("s_barrier" ::: "memory")
#define WAIT_VM(n) asm volatile("s_waitcnt vmcnt(" #n ")" ::: "memory")
#define WAIT_LGKM0() asm volatile("s_waitcnt lgkmcnt(0)" ::: "memory")

typedef unsigned long long u64;
DI void flag_store(char* ws, size_t off, u64 tag) { __hip_atomic_store((gu64*)(ws + off), tag, __ATOMIC_RELAXED, __HIP_MEMORY_SCOPE_AGENT); }
DI void flag_wait(const char* ws, size_t off, int n, u64 tag, int l) {
    gu64* p = (gu64*)(ws + off) + (l < n ? l : 0);
    for (unsigned spins = 0; spins < (1u << 20); ++spins) {
        const u64 v = __hip_atomic_load(p, __ATOMIC_RELAXED, __HIP_MEMORY_SCOPE_AGENT);
        if (__all(v == tag)) break;
        __builtin_amdgcn_s_sleep(2);
    }
}
template <int N, int MSTEP, int LSTEP>
DI void dma_run(const void* sbase_, unsigned voff, unsigned lds_dst_) {
    const void* sbase = uptr(sbase_); const unsigned lds_dst = rfl(lds_dst_);
    asm volatile("s_mov_b64 s[60:61], %1\n\t"
                 "s_mov_b32 m0, %2\n\t"
                 "s_nop 3\n\t"
                 ".rept %c3\n\t"
                 "global_load_lds_dwordx4 %0, s[60:61]\n\t"
                 "s_add_u32 m0, m0, %c5\n\t"
                 "s_add_u32 s60, s60, %c4\n\t"
                 "s_addc_u32 s61, s61, 0\n\t"
                 ".endr"
                 :: "v"(voff), "s"(sbase), "s"(lds_dst), "i"(N), "i"(MSTEP), "i"(LSTEP) : "s60", "s61", "scc", "memory");
}
#define ATTN_FAST_ASM \
    "s_mov_b32 s43, m0\n\t" \
    "s_mov_b32 m0, s42\n\t" \
    "s_add_u32 s40, s40, 16384\n\t" \
    "s_addc_u32 s41, s41, 0\n\t" \
    "global_load_lds_dwordx4 v98, s[40:41] offset:0 sc1\n\t" \
    "global_load_lds_dwordx4 v98, s[40:41] offset:1024 sc1\n\t" \
    "global_load_lds_dwordx4 v98, s[40:41] offset:2048 sc1\n\t" \
    "global_load_lds_dwordx4 v98, s[40:41] offset:3072 sc1\n\t" \
    "ds_read_b128 v[56:59], v96 offset:32768\n\t" \
    "ds_read_b128 v[60:63], v96 offset:33792\n\t" \
    "ds_read_b128 v[64:67], v96 offset:36864\n\t" \
    "ds_read_b128 v[68:71], v96 offset:37888\n\t" \
    "ds_read_b128 v[72:75], v96 offset:34816\n\t" \
    "ds_read_b128 v[76:79], v96 offset:35840\n\t" \
    "s_waitcnt lgkmcnt(4)\n\t" \
    "v_mfma_f32_32x32x16_bf16 v[16:31], v[56:59], v[48:51], 0\n\t" \
    "v_mfma_f32_32x32x16_bf16 v[16:31], v[60:63], v[52:55], v[16:31]\n\t" \
    "s_nop 7\n\t" \
    "s_nop 7\n\t" \
    "v_exp_f32 v16, v16\n\t" \
    "v_exp_f32 v17, v17\n\t" \
    "ds_read_b128 v[56:59], v96 offset:40960\n\t" \
    "v_exp_f32 v18, v18\n\t" \
    "v_exp_f32 v19, v19\n\t" \
    "v_cvt_pk_bf16_f32 v88, v16, v17\n\t" \
    "v_cvt_pk_bf16_f32 v89, v18, v19\n\t" \
    "s_waitcnt lgkmcnt(2)\n\t" \
    "v_mfma_f32_32x32x16_bf16 v[32:47], v[64:67], v[48:51], 0\n\t" \
    "ds_read_b128 v[60:63], v96 offset:41984\n\t" \
    "s_mov_b32 m0, s48\n\t" \
    "s_nop 0\n\t" \
    "global_load_lds_dwordx4 v215, s[46:47]\n\t" \
    "s_add_u32 s48, s48, 1024\n\t" \
    "s_add_u32 s46, s46, 7168\n\t" \
    "s_addc_u32 s47, s47, 0\n\t" \
    "v_exp_f32 v20, v20\n\t" \
    "v_exp_f32 v21, v21\n\t" \
    "v_exp_f32 v22, v22\n\t" \
    "v_exp_f32 v23, v23\n\t" \
    "v_cvt_pk_bf16_f32 v90, v20, v21\n\t" \
    "v_cvt_pk_bf16_f32 v91, v22, v23\n\t" \
    "v_mfma_f32_32x32x16_bf16 v[32:47], v[68:71], v[52:55], v[32:47]\n\t" \
    "ds_read_b128 v[80:83], v96 offset:38912\n\t" \
    "v_exp_f32 v24, v24\n\t" \
    "v_exp_f32 v25, v25\n\t" \
    "v_exp_f32 v26, v26\n\t" \
    "v_exp_f32 v27, v27\n\t" \
    "v_cvt_pk_bf16_f32 v92, v24, v25\n\t" \
    "v_cvt_pk_bf16_f32 v93, v26, v27\n\t" \
    "v_mfma_f32_32x32x16_bf16 v[0:15], v[72:75], v[88:91], 0\n\t" \
    "ds_read_b128 v[84:87], v96 offset:39936\n\t" \
    "s_mov_b32 m0, s48\n\t" \
    "s_nop 0\n\t" \
    "global_load_lds_dwordx4 v215, s[46:47]\n\t" \
    "s_add_u32 s48, s48, 1024\n\t" \
    "s_add_u32 s46, s46, 7168\n\t" \
    "s_addc_u32 s47, s47, 0\n\t" \
    "v_exp_f32 v28, v28\n\t" \
    "v_exp_f32 v29, v29\n\t" \
    "v_exp_f32 v30, v30\n\t" \
    "v_exp_f32 v31, v31\n\t" \
    "v_cvt_pk_bf16_f32 v94, v28, v29\n\t" \
    "v_cvt_pk_bf16_f32 v95, v30, v31\n\t" \
    "v_exp_f32 v32, v32\n\t" \
    "v_exp_f32 v33, v33\n\t" \
    "v_mfma_f32_32x32x16_bf16 v[0:15], v[76:79], v[92:95], v[0:15]\n\t" \
    "ds_read_b128 v[64:67], v96 offset:45056\n\t" \
    "v_exp_f32 v34, v34\n\t" \
    "v_exp_f32 v35, v35\n\t" \
    "v_cvt_pk_bf16_f32 v88, v32, v33\n\t" \
    "v_cvt_pk_bf16_f32 v89, v34, v35\n\t" \
    "s_waitcnt lgkmcnt(2)\n\t" \
    "v_mfma_f32_32x32x16_bf16 v[16:31], v[56:59], v[48:51], 0\n\t" \
    "ds_read_b128 v[68:71], v96 offset:46080\n\t" \
    "s_mov_b32 m0, s48\n\t" \
    "s_nop 0\n\t" \
    "global_load_lds_dwordx4 v215, s[46:47]\n\t" \
    "s_add_u32 s48, s48, 1024\n\t" \
    "s_add_u32 s46, s46, 7168\n\t" \
    "s_addc_u32 s47, s47, 0\n\t" \
    "v_exp_f32 v36, v36\n\t" \
    "v_exp_f32 v37, v37\n\t" \
    "v_exp_f32 v38, v38\n\t" \
    "v_exp_f32 v39, v39\n\t" \
    "v_cvt_pk_bf16_f32 v90, v36, v37\n\t" \
    "v_cvt_pk_bf16_f32 v91, v38, v39\n\t" \
    "v_mfma_f32_32x32x16_bf16 v[16:31], v[60:63], v[52:55], v[16:31]\n\t" \
    "ds_read_b128 v[72:75], v96 offset:43008\n\t" \
    "v_exp_f32 v40, v40\n\t" \
    "v_exp_f32 v41, v41\n\t" \
    "v_exp_f32 v42, v42\n\t" \
    "v_exp_f32 v43, v43\n\t" \
    "v_cvt_pk_bf16_f32 v92, v40, v41\n\t" \
    "v_cvt_pk_bf16_f32 v93, v42, v43\n\t" \
    "v_mfma_f32_32x32x16_bf16 v[0:15], v[80:83], v[88:91], v[0:15]\n\t" \
    "ds_read_b128 v[76:79], v96 offset:44032\n\t" \
    "s_mov_b32 m0, s48\n\t" \
    "s_nop 0\n\t" \
    "global_load_lds_dwordx4 v215, s[46:47]\n\t" \
    "s_add_u32 s48, s48, 29696\n\t" \
    "s_add_u32 s46, s46, 35840\n\t" \
    "s_addc_u32 s47, s47, 0\n\t" \
    "v_exp_f32 v44, v44\n\t" \
    "v_exp_f32 v45, v45\n\t" \
    "v_exp_f32 v46, v46\n\t" \
    "v_exp_f32 v47, v47\n\t" \
    "v_cvt_pk_bf16_f32 v94, v44, v45\n\t" \
    "v_cvt_pk_bf16_f32 v95, v46, v47\n\t" \
    "s_waitcnt vmcnt(8)\n\t" \
    "s_barrier\n\t" \
    "s_cmp_eq_u32 s58, 0\n\t" \
    "s_cbranch_scc1 3f\n\t" \
    "v_mov_b32 v212, s52\n\t" \
    "v_mov_b32 v213, s53\n\t" \
    "v_mov_b32 v218, 0\n\t" \
    "s_mov_b64 exec, 1\n\t" \
    "global_store_dwordx2 v218, v[212:213], s[56:57] sc1\n\t" \
    "s_mov_b64 exec, -1\n\t" \
    "3:\n\t" \
    "v_exp_f32 v16, v16\n\t" \
    "v_exp_f32 v17, v17\n\t" \
    "v_mfma_f32_32x32x16_bf16 v[0:15], v[84:87], v[92:95], v[0:15]\n\t" \
    "ds_read_b128 v[56:59], v97 offset:0\n\t" \
    "v_exp_f32 v18, v18\n\t" \
    "v_exp_f32 v19, v19\n\t" \
    "v_cvt_pk_bf16_f32 v88, v16, v17\n\t" \
    "v_cvt_pk_bf16_f32 v89, v18, v19\n\t" \
    "s_waitcnt lgkmcnt(2)\n\t" \
    "v_mfma_f32_32x32x16_bf16 v[32:47], v[64:67], v[48:51], 0\n\t" \
    "ds_read_b128 v[60:63], v97 offset:1024\n\t" \
    "v_exp_f32 v20, v20\n\t" \
    "v_exp_f32 v21, v21\n\t" \
    "v_exp_f32 v22, v22\n\t" \
    "v_exp_f32 v23, v23\n\t" \
    "v_cvt_pk_bf16_f32 v90, v20, v21\n\t" \
    "v_cvt_pk_bf16_f32 v91, v22, v23\n\t" \
    "v_mfma_f32_32x32x16_bf16 v[32:47], v[68:71], v[52:55], v[32:47]\n\t" \
    "ds_read_b128 v[80:83], v96 offset:47104\n\t" \
    "v_exp_f32 v24, v24\n\t" \
    "v_exp_f32 v25, v25\n\t" \
    "v_exp_f32 v26, v26\n\t" \
    "v_exp_f32 v27, v27\n\t" \
    "v_cvt_pk_bf16_f32 v92, v24, v25\n\t" \
    "v_cvt_pk_bf16_f32 v93, v26, v27\n\t" \
    "v_mfma_f32_32x32x16_bf16 v[0:15], v[72:75], v[88:91], v[0:15]\n\t" \
    "ds_read_b128 v[84:87], v96 offset:48128\n\t" \
    "v_exp_f32 v28, v28\n\t" \
    "v_exp_f32 v29, v29\n\t" \
    "v_exp_f32 v30, v30\n\t" \
    "v_exp_f32 v31, v31\n\t" \
    "v_cvt_pk_bf16_f32 v94, v28, v29\n\t" \
    "v_cvt_pk_bf16_f32 v95, v30, v31\n\t" \
    "v_exp_f32 v32, v32\n\t" \
    "v_exp_f32 v33, v33\n\t" \
    "v_mfma_f32_32x32x16_bf16 v[0:15], v[76:79], v[92:95], v[0:15]\n\t" \
    "ds_read_b128 v[64:67], v97 offset:4096\n\t" \
    "v_exp_f32 v34, v34\n\t" \
    "v_exp_f32 v35, v35\n\t" \
    "v_cvt_pk_bf16_f32 v88, v32, v33\n\t" \
    "v_cvt_pk_bf16_f32 v89, v34, v35\n\t" \
    "s_waitcnt lgkmcnt(2)\n\t" \
    "v_mfma_f32_32x32x16_bf16 v[16:31], v[56:59], v[48:51], 0\n\t" \
    "ds_read_b128 v[68:71], v97 offset:5120\n\t" \
    "s_mov_b32 m0, s48\n\t" \
    "s_nop 0\n\t" \
    "global_load_lds_dwordx4 v215, s[46:47]\n\t" \
    "s_add_u32 s48, s48, 1024\n\t" \
    "s_add_u32 s46, s46, 7168\n\t" \
    "s_addc_u32 s47, s47, 0\n\t" \
    "v_exp_f32 v36, v36\n\t" \
    "v_exp_f32 v37, v37\n\t" \
    "v_exp_f32 v38, v38\n\t" \
    "v_exp_f32 v39, v39\n\t" \
    "v_cvt_pk_bf16_f32 v90, v36, v37\n\t" \
    "v_cvt_pk_bf16_f32 v91, v38, v39\n\t" \
    "v_mfma_f32_32x32x16_bf16 v[16:31], v[60:63], v[52:55], v[16:31]\n\t" \
    "ds_read_b128 v[72:75], v97 offset:2048\n\t" \
    "v_exp_f32 v40, v40\n\t" \
    "v_exp_f32 v41, v41\n\t" \
    "v_exp_f32 v42, v42\n\t" \
    "v_exp_f32 v43, v43\n\t" \
    "v_cvt_pk_bf16_f32 v92, v40, v41\n\t" \
    "v_cvt_pk_bf16_f32 v93, v42, v43\n\t" \
    "v_mfma_f32_32x32x16_bf16 v[0:15], v[80:83], v[88:91], v[0:15]\n\t" \
    "ds_read_b128 v[76:79], v97 offset:3072\n\t" \
    "s_mov_b32 m0, s48\n\t" \
    "s_nop 0\n\t" \
    "global_load_lds_dwordx4 v215, s[46:47]\n\t" \
    "s_add_u32 s48, s48, 1024\n\t" \
    "s_add_u32 s46, s46, 7168\n\t" \
    "s_addc_u32 s47, s47, 0\n\t" \
    "v_exp_f32 v44, v44\n\t" \
    "v_exp_f32 v45, v45\n\t" \
    "v_exp_f32 v46, v46\n\t" \
    "v_exp_f32 v47, v47\n\t" \
    "v_cvt_pk_bf16_f32 v94, v44, v45\n\t" \
    "v_cvt_pk_bf16_f32 v95, v46, v47\n\t" \
    "v_exp_f32 v16, v16\n\t" \
    "v_exp_f32 v17, v17\n\t" \
    "v_mfma_f32_32x32x16_bf16 v[0:15], v[84:87], v[92:95], v[0:15]\n\t" \
    "ds_read_b128 v[56:59], v97 offset:8192\n\t" \
    "v_exp_f32 v18, v18\n\t" \
    "v_exp_f32 v19, v19\n\t" \
    "v_cvt_pk_bf16_f32 v88, v16, v17\n\t" \
    "v_cvt_pk_bf16_f32 v89, v18, v19\n\t" \
    "s_waitcnt lgkmcnt(2)\n\t" \
    "v_mfma_f32_32x32x16_bf16 v[32:47], v[64:67], v[48:51], 0\n\t" \
    "ds_read_b128 v[60:63], v97 offset:9216\n\t" \
    "s_mov_b32 m0, s48\n\t" \
    "s_nop 0\n\t" \
    "global_load_lds_dwordx4 v215, s[46:47]\n\t" \
    "s_add_u32 s48, s48, 1024\n\t" \
    "s_add_u32 s46, s46, 7168\n\t" \
    "s_addc_u32 s47, s47, 0\n\t" \
    "v_exp_f32 v20, v20\n\t" \
    "v_exp_f32 v21, v21\n\t" \
    "v_exp_f32 v22, v22\n\t" \
    "v_exp_f32 v23, v23\n\t" \
    "v_cvt_pk_bf16_f32 v90, v20, v21\n\t" \
    "v_cvt_pk_bf16_f32 v91, v22, v23\n\t" \
    "v_mfma_f32_32x32x16_bf16 v[32:47], v[68:71], v[52:55], v[32:47]\n\t" \
    "ds_read_b128 v[80:83], v97 offset:6144\n\t" \
    "v_exp_f32 v24, v24\n\t" \
    "v_exp_f32 v25, v25\n\t" \
    "v_exp_f32 v26, v26\n\t" \
    "v_exp_f32 v27, v27\n\t" \
    "v_cvt_pk_bf16_f32 v92, v24, v25\n\t" \
    "v_cvt_pk_bf16_f32 v93, v26, v27\n\t" \
    "v_mfma_f32_32x32x16_bf16 v[0:15], v[72:75], v[88:91], v[0:15]\n\t" \
    "ds_read_b128 v[84:87], v97 offset:7168\n\t" \
    "s_mov_b32 m0, s48\n\t" \
    "s_nop 0\n\t" \
    "global_load_lds_dwordx4 v215, s[46:47]\n\t" \
    "s_add_u32 s48, s48, 29696\n\t" \
    "s_add_u32 s46, s46, 35840\n\t" \
    "s_addc_u32 s47, s47, 0\n\t" \
    "v_exp_f32 v28, v28\n\t" \
    "v_exp_f32 v29, v29\n\t" \
    "v_exp_f32 v30, v30\n\t" \
    "v_exp_f32 v31, v31\n\t" \
    "v_cvt_pk_bf16_f32 v94, v28, v29\n\t" \
    "v_cvt_pk_bf16_f32 v95, v30, v31\n\t" \
    "v_exp_f32 v32, v32\n\t" \
    "v_exp_f32 v33, v33\n\t" \
    "v_mfma_f32_32x32x16_bf16 v[0:15], v[76:79], v[92:95], v[0:15]\n\t" \
    "ds_read_b128 v[64:67], v97 offset:12288\n\t" \
    "v_exp_f32 v34, v34\n\t" \
    "v_exp_f32 v35, v35\n\t" \
    "v_cvt_pk_bf16_f32 v88, v32, v33\n\t" \
    "v_cvt_pk_bf16_f32 v89, v34, v35\n\t" \
    "s_waitcnt lgkmcnt(2)\n\t" \
    "v_mfma_f32_32x32x16_bf16 v[16:31], v[56:59], v[48:51], 0\n\t" \
    "ds_read_b128 v[68:71], v97 offset:13312\n\t" \
    "v_exp_f32 v36, v36\n\t" \
    "v_exp_f32 v37, v37\n\t" \
    "v_exp_f32 v38, v38\n\t" \
    "v_exp_f32 v39, v39\n\t" \
    "v_cvt_pk_bf16_f32 v90, v36, v37\n\t" \
    "v_cvt_pk_bf16_f32 v91, v38, v39\n\t" \
    "v_mfma_f32_32x32x16_bf16 v[16:31], v[60:63], v[52:55], v[16:31]\n\t" \
    "ds_read_b128 v[72:75], v97 offset:10240\n\t" \
    "v_exp_f32 v40, v40\n\t" \
    "v_exp_f32 v41, v41\n\t" \
    "v_exp_f32 v42, v42\n\t" \
    "v_exp_f32 v43, v43\n\t" \
    "v_cvt_pk_bf16_f32 v92, v40, v41\n\t" \
    "v_cvt_pk_bf16_f32 v93, v42, v43\n\t" \
    "v_mfma_f32_32x32x16_bf16 v[0:15], v[80:83], v[88:91], v[0:15]\n\t" \
    "ds_read_b128 v[76:79], v97 offset:11264\n\t" \
    "v_exp_f32 v44, v44\n\t" \
    "v_exp_f32 v45, v45\n\t" \
    "v_exp_f32 v46, v46\n\t" \
    "v_exp_f32 v47, v47\n\t" \
    "v_cvt_pk_bf16_f32 v94, v44, v45\n\t" \
    "v_cvt_pk_bf16_f32 v95, v46, v47\n\t" \
    "s_waitcnt vmcnt(8)\n\t" \
    "s_barrier\n\t" \
    "s_add_u32 m0, s42, 32768\n\t" \
    "s_add_u32 s40, s40, 16384\n\t" \
    "s_addc_u32 s41, s41, 0\n\t" \
    "v_exp_f32 v16, v16\n\t" \
    "v_exp_f32 v17, v17\n\t" \
    "v_mfma_f32_32x32x16_bf16 v[0:15], v[84:87], v[92:95], v[0:15]\n\t" \
    "ds_read_b128 v[56:59], v96 offset:0\n\t" \
    "v_exp_f32 v18, v18\n\t" \
    "v_exp_f32 v19, v19\n\t" \
    "v_cvt_pk_bf16_f32 v88, v16, v17\n\t" \
    "v_cvt_pk_bf16_f32 v89, v18, v19\n\t" \
    "s_waitcnt lgkmcnt(2)\n\t" \
    "v_mfma_f32_32x32x16_bf16 v[32:47], v[64:67], v[48:51], 0\n\t" \
    "ds_read_b128 v[60:63], v96 offset:1024\n\t" \
    "global_load_lds_dwordx4 v98, s[40:41] offset:0 sc1\n\t" \
    "global_load_lds_dwordx4 v98, s[40:41] offset:1024 sc1\n\t" \
    "v_exp_f32 v20, v20\n\t" \
    "v_exp_f32 v21, v21\n\t" \
    "v_exp_f32 v22, v22\n\t" \
    "v_exp_f32 v23, v23\n\t" \
    "v_cvt_pk_bf16_f32 v90, v20, v21\n\t" \
    "v_cvt_pk_bf16_f32 v91, v22, v23\n\t" \
    "v_mfma_f32_32x32x16_bf16 v[32:47], v[68:71], v[52:55], v[32:47]\n\t" \
    "ds_read_b128 v[80:83], v97 offset:14336\n\t" \
    "global_load_lds_dwordx4 v98, s[40:41] offset:2048 sc1\n\t" \
    "v_exp_f32 v24, v24\n\t" \
    "v_exp_f32 v25, v25\n\t" \
    "v_exp_f32 v26, v26\n\t" \
    "v_exp_f32 v27, v27\n\t" \
    "v_cvt_pk_bf16_f32 v92, v24, v25\n\t" \
    "v_cvt_pk_bf16_f32 v93, v26, v27\n\t" \
    "v_mfma_f32_32x32x16_bf16 v[0:15], v[72:75], v[88:91], v[0:15]\n\t" \
    "ds_read_b128 v[84:87], v97 offset:15360\n\t" \
    "global_load_lds_dwordx4 v98, s[40:41] offset:3072 sc1\n\t" \
    "v_exp_f32 v28, v28\n\t" \
    "v_exp_f32 v29, v29\n\t" \
    "v_exp_f32 v30, v30\n\t" \
    "v_exp_f32 v31, v31\n\t" \
    "v_cvt_pk_bf16_f32 v94, v28, v29\n\t" \
    "v_cvt_pk_bf16_f32 v95, v30, v31\n\t" \
    "v_exp_f32 v32, v32\n\t" \
    "v_exp_f32 v33, v33\n\t" \
    "v_mfma_f32_32x32x16_bf16 v[0:15], v[76:79], v[92:95], v[0:15]\n\t" \
    "ds_read_b128 v[64:67], v96 offset:4096\n\t" \
    "v_exp_f32 v34, v34\n\t" \
    "v_exp_f32 v35, v35\n\t" \
    "v_cvt_pk_bf16_f32 v88, v32, v33\n\t" \
    "v_cvt_pk_bf16_f32 v89, v34, v35\n\t" \
    "s_waitcnt lgkmcnt(2)\n\t" \
    "v_mfma_f32_32x32x16_bf16 v[16:31], v[56:59], v[48:51], 0\n\t" \
    "ds_read_b128 v[68:71], v96 offset:5120\n\t" \
    "v_exp_f32 v36, v36\n\t" \
    "v_exp_f32 v37, v37\n\t" \
    "v_exp_f32 v38, v38\n\t" \
    "v_exp_f32 v39, v39\n\t" \
    "v_cvt_pk_bf16_f32 v90, v36, v37\n\t" \
    "v_cvt_pk_bf16_f32 v91, v38, v39\n\t" \
    "v_mfma_f32_32x32x16_bf16 v[16:31], v[60:63], v[52:55], v[16:31]\n\t" \
    "ds_read_b128 v[72:75], v96 offset:2048\n\t" \
    "v_exp_f32 v40, v40\n\t" \
    "v_exp_f32 v41, v41\n\t" \
    "v_exp_f32 v42, v42\n\t" \
    "v_exp_f32 v43, v43\n\t" \
    "v_cvt_pk_bf16_f32 v92, v40, v41\n\t" \
    "v_cvt_pk_bf16_f32 v93, v42, v43\n\t" \
    "v_mfma_f32_32x32x16_bf16 v[0:15], v[80:83], v[88:91], v[0:15]\n\t" \
    "ds_read_b128 v[76:79], v96 offset:3072\n\t" \
    "v_exp_f32 v44, v44\n\t" \
    "v_exp_f32 v45, v45\n\t" \
    "v_exp_f32 v46, v46\n\t" \
    "v_exp_f32 v47, v47\n\t" \
    "v_cvt_pk_bf16_f32 v94, v44, v45\n\t" \
    "v_cvt_pk_bf16_f32 v95, v46, v47\n\t" \
    "v_exp_f32 v16, v16\n\t" \
    "v_exp_f32 v17, v17\n\t" \
    "v_mfma_f32_32x32x16_bf16 v[0:15], v[84:87], v[92:95], v[0:15]\n\t" \
    "ds_read_b128 v[56:59], v96 offset:8192\n\t" \
    "v_exp_f32 v18, v18\n\t" \
    "v_exp_f32 v19, v19\n\t" \
    "v_cvt_pk_bf16_f32 v88, v16, v17\n\t" \
    "v_cvt_pk_bf16_f32 v89, v18, v19\n\t" \
    "s_waitcnt lgkmcnt(2)\n\t" \
    "v_mfma_f32_32x32x16_bf16 v[32:47], v[64:67], v[48:51], 0\n\t" \
    "ds_read_b128 v[60:63], v96 offset:9216\n\t" \
    "v_exp_f32 v20, v20\n\t" \
    "v_exp_f32 v21, v21\n\t" \
    "v_exp_f32 v22, v22\n\t" \
    "v_exp_f32 v23, v23\n\t" \
    "v_cvt_pk_bf16_f32 v90, v20, v21\n\t" \
    "v_cvt_pk_bf16_f32 v91, v22, v23\n\t" \
    "v_mfma_f32_32x32x16_bf16 v[32:47], v[68:71], v[52:55], v[32:47]\n\t" \
    "ds_read_b128 v[80:83], v96 offset:6144\n\t" \
    "v_exp_f32 v24, v24\n\t" \
    "v_exp_f32 v25, v25\n\t" \
    "v_exp_f32 v26, v26\n\t" \
    "v_exp_f32 v27, v27\n\t" \
    "v_cvt_pk_bf16_f32 v92, v24, v25\n\t" \
    "v_cvt_pk_bf16_f32 v93, v26, v27\n\t" \
    "v_mfma_f32_32x32x16_bf16 v[0:15], v[72:75], v[88:91], v[0:15]\n\t" \
    "ds_read_b128 v[84:87], v96 offset:7168\n\t" \
    "global_load_dwordx2 v[212:213], v217, s[50:51] sc1\n\t" \
    "v_exp_f32 v28, v28\n\t" \
    "v_exp_f32 v29, v29\n\t" \
    "v_exp_f32 v30, v30\n\t" \
    "v_exp_f32 v31, v31\n\t" \
    "v_cvt_pk_bf16_f32 v94, v28, v29\n\t" \
    "v_cvt_pk_bf16_f32 v95, v30, v31\n\t" \
    "v_exp_f32 v32, v32\n\t" \
    "v_exp_f32 v33, v33\n\t" \
    "v_mfma_f32_32x32x16_bf16 v[0:15], v[76:79], v[92:95], v[0:15]\n\t" \
    "ds_read_b128 v[64:67], v96 offset:12288\n\t" \
    "v_exp_f32 v34, v34\n\t" \
    "v_exp_f32 v35, v35\n\t" \
    "v_cvt_pk_bf16_f32 v88, v32, v33\n\t" \
    "v_cvt_pk_bf16_f32 v89, v34, v35\n\t" \
    "s_waitcnt lgkmcnt(2)\n\t" \
    "v_mfma_f32_32x32x16_bf16 v[16:31], v[56:59], v[48:51], 0\n\t" \
    "ds_read_b128 v[68:71], v96 offset:13312\n\t" \
    "v_exp_f32 v36, v36\n\t" \
    "v_exp_f32 v37, v37\n\t" \
    "v_exp_f32 v38, v38\n\t" \
    "v_exp_f32 v39, v39\n\t" \
    "v_cvt_pk_bf16_f32 v90, v36, v37\n\t" \
    "v_cvt_pk_bf16_f32 v91, v38, v39\n\t" \
    "v_mfma_f32_32x32x16_bf16 v[16:31], v[60:63], v[52:55], v[16:31]\n\t" \
    "ds_read_b128 v[72:75], v96 offset:10240\n\t" \
    "v_exp_f32 v40, v40\n\t" \
    "v_exp_f32 v41, v41\n\t" \
    "v_exp_f32 v42, v42\n\t" \
    "v_exp_f32 v43, v43\n\t" \
    "v_cvt_pk_bf16_f32 v92, v40, v41\n\t" \
    "v_cvt_pk_bf16_f32 v93, v42, v43\n\t" \
    "v_mfma_f32_32x32x16_bf16 v[0:15], v[80:83], v[88:91], v[0:15]\n\t" \
    "ds_read_b128 v[76:79], v96 offset:11264\n\t" \
    "v_exp_f32 v44, v44\n\t" \
    "v_exp_f32 v45, v45\n\t" \
    "v_exp_f32 v46, v46\n\t" \
    "v_exp_f32 v47, v47\n\t" \
    "v_cvt_pk_bf16_f32 v94, v44, v45\n\t" \
    "v_cvt_pk_bf16_f32 v95, v46, v47\n\t" \
    "s_waitcnt vmcnt(0)\n\t" \
    "s_barrier\n\t" \
    "v_cmp_ne_u64_e32 vcc, s[52:53], v[212:213]\n\t" \
    "s_nop 1\n\t" \
    "s_cmp_eq_u64 vcc, 0\n\t" \
    "s_cbranch_scc1 2f\n\t" \
    "s_mov_b32 s49, 0x100000\n\t" \
    "1:\n\t" \
    "s_sleep 2\n\t" \
    "global_load_dwordx2 v[212:213], v217, s[50:51] sc1\n\t" \
    "s_waitcnt vmcnt(0)\n\t" \
    "v_cmp_ne_u64_e32 vcc, s[52:53], v[212:213]\n\t" \
    "s_nop 1\n\t" \
    "s_cmp_eq_u64 vcc, 0\n\t" \
    "s_cbranch_scc1 2f\n\t" \
    "s_sub_u32 s49, s49, 1\n\t" \
    "s_cmp_lg_u32 s49, 0\n\t" \
    "s_cbranch_scc1 1b\n\t" \
    "2:\n\t" \
    "s_add_u32 m0, s42, 65536\n\t" \
    "s_add_u32 s40, s40, 16384\n\t" \
    "s_addc_u32 s41, s41, 0\n\t" \
    "v_exp_f32 v16, v16\n\t" \
    "v_exp_f32 v17, v17\n\t" \
    "v_mfma_f32_32x32x16_bf16 v[0:15], v[84:87], v[92:95], v[0:15]\n\t" \
    "ds_read_b128 v[56:59], v96 offset:32768\n\t" \
    "v_exp_f32 v18, v18\n\t" \
    "v_exp_f32 v19, v19\n\t" \
    "v_cvt_pk_bf16_f32 v88, v16, v17\n\t" \
    "v_cvt_pk_bf16_f32 v89, v18, v19\n\t" \
    "s_waitcnt lgkmcnt(2)\n\t" \
    "v_mfma_f32_32x32x16_bf16 v[32:47], v[64:67], v[48:51], 0\n\t" \
    "ds_read_b128 v[60:63], v96 offset:33792\n\t" \
    "global_load_lds_dwordx4 v98, s[40:41] offset:0 sc1\n\t" \
    "global_load_lds_dwordx4 v98, s[40:41] offset:1024 sc1\n\t" \
    "v_exp_f32 v20, v20\n\t" \
    "v_exp_f32 v21, v21\n\t" \
    "v_exp_f32 v22, v22\n\t" \
    "v_exp_f32 v23, v23\n\t" \
    "v_cvt_pk_bf16_f32 v90, v20, v21\n\t" \
    "v_cvt_pk_bf16_f32 v91, v22, v23\n\t" \
    "v_mfma_f32_32x32x16_bf16 v[32:47], v[68:71], v[52:55], v[32:47]\n\t" \
    "ds_read_b128 v[80:83], v96 offset:14336\n\t" \
    "global_load_lds_dwordx4 v98, s[40:41] offset:2048 sc1\n\t" \
    "v_exp_f32 v24, v24\n\t" \
    "v_exp_f32 v25, v25\n\t" \
    "v_exp_f32 v26, v26\n\t" \
    "v_exp_f32 v27, v27\n\t" \
    "v_cvt_pk_bf16_f32 v92, v24, v25\n\t" \
    "v_cvt_pk_bf16_f32 v93, v26, v27\n\t" \
    "v_mfma_f32_32x32x16_bf16 v[0:15], v[72:75], v[88:91], v[0:15]\n\t" \
    "ds_read_b128 v[84:87], v96 offset:15360\n\t" \
    "global_load_lds_dwordx4 v98, s[40:41] offset:3072 sc1\n\t" \
    "v_exp_f32 v28, v28\n\t" \
    "v_exp_f32 v29, v29\n\t" \
    "v_exp_f32 v30, v30\n\t" \
    "v_exp_f32 v31, v31\n\t" \
    "v_cvt_pk_bf16_f32 v94, v28, v29\n\t" \
    "v_cvt_pk_bf16_f32 v95, v30, v31\n\t" \
    "v_exp_f32 v32, v32\n\t" \
    "v_exp_f32 v33, v33\n\t" \
    "v_mfma_f32_32x32x16_bf16 v[0:15], v[76:79], v[92:95], v[0:15]\n\t" \
    "ds_read_b128 v[64:67], v96 offset:36864\n\t" \
    "v_exp_f32 v34, v34\n\t" \
    "v_exp_f32 v35, v35\n\t" \
    "v_cvt_pk_bf16_f32 v88, v32, v33\n\t" \
    "v_cvt_pk_bf16_f32 v89, v34, v35\n\t" \
    "s_waitcnt lgkmcnt(2)\n\t" \
    "v_mfma_f32_32x32x16_bf16 v[16:31], v[56:59], v[48:51], 0\n\t" \
    "ds_read_b128 v[68:71], v96 offset:37888\n\t" \
    "global_load_dwordx4 v[100:103], v98, s[44:45] offset:0 sc1\n\t" \
    "v_exp_f32 v36, v36\n\t" \
    "v_exp_f32 v37, v37\n\t" \
    "v_exp_f32 v38, v38\n\t" \
    "v_exp_f32 v39, v39\n\t" \
    "v_cvt_pk_bf16_f32 v90, v36, v37\n\t" \
    "v_cvt_pk_bf16_f32 v91, v38, v39\n\t" \
    "v_mfma_f32_32x32x16_bf16 v[16:31], v[60:63], v[52:55], v[16:31]\n\t" \
    "ds_read_b128 v[72:75], v96 offset:34816\n\t" \
    "global_load_dwordx4 v[104:107], v98, s[44:45] offset:1024 sc1\n\t" \
    "v_exp_f32 v40, v40\n\t" \
    "v_exp_f32 v41, v41\n\t" \
    "v_exp_f32 v42, v42\n\t" \
    "v_exp_f32 v43, v43\n\t" \
    "v_cvt_pk_bf16_f32 v92, v40, v41\n\t" \
    "v_cvt_pk_bf16_f32 v93, v42, v43\n\t" \
    "v_mfma_f32_32x32x16_bf16 v[0:15], v[80:83], v[88:91], v[0:15]\n\t" \
    "ds_read_b128 v[76:79], v96 offset:35840\n\t" \
    "global_load_dwordx4 v[108:111], v98, s[44:45] offset:2048 sc1\n\t" \
    "v_exp_f32 v44, v44\n\t" \
    "v_exp_f32 v45, v45\n\t" \
    "v_exp_f32 v46, v46\n\t" \
    "v_exp_f32 v47, v47\n\t" \
    "v_cvt_pk_bf16_f32 v94, v44, v45\n\t" \
    "v_cvt_pk_bf16_f32 v95, v46, v47\n\t" \
    "v_exp_f32 v16, v16\n\t" \
    "v_exp_f32 v17, v17\n\t" \
    "v_mfma_f32_32x32x16_bf16 v[0:15], v[84:87], v[92:95], v[0:15]\n\t" \
    "ds_read_b128 v[56:59], v96 offset:40960\n\t" \
    "v_exp_f32 v18, v18\n\t" \
    "v_exp_f32 v19, v19\n\t" \
    "v_cvt_pk_bf16_f32 v88, v16, v17\n\t" \
    "v_cvt_pk_bf16_f32 v89, v18, v19\n\t" \
    "s_waitcnt lgkmcnt(2)\n\t" \
    "v_mfma_f32_32x32x16_bf16 v[32:47], v[64:67], v[48:51], 0\n\t" \
    "ds_read_b128 v[60:63], v96 offset:41984\n\t" \
    "global_load_dwordx4 v[112:115], v98, s[44:45] offset:3072 sc1\n\t" \
    "v_exp_f32 v20, v20\n\t" \
    "v_exp_f32 v21, v21\n\t" \
    "v_exp_f32 v22, v22\n\t" \
    "v_exp_f32 v23, v23\n\t" \
    "v_cvt_pk_bf16_f32 v90, v20, v21\n\t" \
    "v_cvt_pk_bf16_f32 v91, v22, v23\n\t" \
    "v_mfma_f32_32x32x16_bf16 v[32:47], v[68:71], v[52:55], v[32:47]\n\t" \
    "ds_read_b128 v[80:83], v96 offset:38912\n\t" \
    "s_add_u32 s44, s44, 4096\n\t" \
    "s_addc_u32 s45, s45, 0\n\t" \
    "global_load_dwordx4 v[116:119], v98, s[44:45] offset:0 sc1\n\t" \
    "v_exp_f32 v24, v24\n\t" \
    "v_exp_f32 v25, v25\n\t" \
    "v_exp_f32 v26, v26\n\t" \
    "v_exp_f32 v27, v27\n\t" \
    "v_cvt_pk_bf16_f32 v92, v24, v25\n\t" \
    "v_cvt_pk_bf16_f32 v93, v26, v27\n\t" \
    "v_mfma_f32_32x32x16_bf16 v[0:15], v[72:75], v[88:91], v[0:15]\n\t" \
    "ds_read_b128 v[84:87], v96 offset:39936\n\t" \
    "global_load_dwordx4 v[120:123], v98, s[44:45] offset:1024 sc1\n\t" \
    "v_exp_f32 v28, v28\n\t" \
    "v_exp_f32 v29, v29\n\t" \
    "v_exp_f32 v30, v30\n\t" \
    "v_exp_f32 v31, v31\n\t" \
    "v_cvt_pk_bf16_f32 v94, v28, v29\n\t" \
    "v_cvt_pk_bf16_f32 v95, v30, v31\n\t" \
    "v_exp_f32 v32, v32\n\t" \
    "v_exp_f32 v33, v33\n\t" \
    "v_mfma_f32_32x32x16_bf16 v[0:15], v[76:79], v[92:95], v[0:15]\n\t" \
    "ds_read_b128 v[64:67], v96 offset:45056\n\t" \
    "v_exp_f32 v34, v34\n\t" \
    "v_exp_f32 v35, v35\n\t" \
    "v_cvt_pk_bf16_f32 v88, v32, v33\n\t" \
    "v_cvt_pk_bf16_f32 v89, v34, v35\n\t" \
    "s_waitcnt lgkmcnt(2)\n\t" \
    "v_mfma_f32_32x32x16_bf16 v[16:31], v[56:59], v[48:51], 0\n\t" \
    "ds_read_b128 v[68:71], v96 offset:46080\n\t" \
    "global_load_dwordx4 v[124:127], v98, s[44:45] offset:2048 sc1\n\t" \
    "v_exp_f32 v36, v36\n\t" \
    "v_exp_f32 v37, v37\n\t" \
    "v_exp_f32 v38, v38\n\t" \
    "v_exp_f32 v39, v39\n\t" \
    "v_cvt_pk_bf16_f32 v90, v36, v37\n\t" \
    "v_cvt_pk_bf16_f32 v91, v38, v39\n\t" \
    "v_mfma_f32_32x32x16_bf16 v[16:31], v[60:63], v[52:55], v[16:31]\n\t" \
    "ds_read_b128 v[72:75], v96 offset:43008\n\t" \
    "global_load_dwordx4 v[128:131], v98, s[44:45] offset:3072 sc1\n\t" \
    "v_exp_f32 v40, v40\n\t" \
    "v_exp_f32 v41, v41\n\t" \
    "v_exp_f32 v42, v42\n\t" \
    "v_exp_f32 v43, v43\n\t" \
    "v_cvt_pk_bf16_f32 v92, v40, v41\n\t" \
    "v_cvt_pk_bf16_f32 v93, v42, v43\n\t" \
    "v_mfma_f32_32x32x16_bf16 v[0:15], v[80:83], v[88:91], v[0:15]\n\t" \
    "ds_read_b128 v[76:79], v96 offset:44032\n\t" \
    "s_add_u32 s44, s44, 4096\n\t" \
    "s_addc_u32 s45, s45, 0\n\t" \
    "global_load_dwordx4 v[132:135], v98, s[44:45] offset:0 sc1\n\t" \
    "v_exp_f32 v44, v44\n\t" \
    "v_exp_f32 v45, v45\n\t" \
    "v_exp_f32 v46, v46\n\t" \
    "v_exp_f32 v47, v47\n\t" \
    "v_cvt_pk_bf16_f32 v94, v44, v45\n\t" \
    "v_cvt_pk_bf16_f32 v95, v46, v47\n\t" \
    "s_waitcnt vmcnt(9)\n\t" \
    "s_barrier\n\t" \
    "s_mov_b32 m0, s42\n\t" \
    "s_add_u32 s40, s40, 16384\n\t" \
    "s_addc_u32 s41, s41, 0\n\t" \
    "v_exp_f32 v16, v16\n\t" \
    "v_exp_f32 v17, v17\n\t" \
    "v_mfma_f32_32x32x16_bf16 v[0:15], v[84:87], v[92:95], v[0:15]\n\t" \
    "ds_read_b128 v[56:59], v97 offset:0\n\t" \
    "v_exp_f32 v18, v18\n\t" \
    "v_exp_f32 v19, v19\n\t" \
    "v_cvt_pk_bf16_f32 v88, v16, v17\n\t" \
    "v_cvt_pk_bf16_f32 v89, v18, v19\n\t" \
    "s_waitcnt lgkmcnt(2)\n\t" \
    "v_mfma_f32_32x32x16_bf16 v[32:47], v[64:67], v[48:51], 0\n\t" \
    "ds_read_b128 v[60:63], v97 offset:1024\n\t" \
    "global_load_lds_dwordx4 v98, s[40:41] offset:0 sc1\n\t" \
    "global_load_lds_dwordx4 v98, s[40:41] offset:1024 sc1\n\t" \
    "v_exp_f32 v20, v20\n\t" \
    "v_exp_f32 v21, v21\n\t" \
    "v_exp_f32 v22, v22\n\t" \
    "v_exp_f32 v23, v23\n\t" \
    "v_cvt_pk_bf16_f32 v90, v20, v21\n\t" \
    "v_cvt_pk_bf16_f32 v91, v22, v23\n\t" \
    "v_mfma_f32_32x32x16_bf16 v[32:47], v[68:71], v[52:55], v[32:47]\n\t" \
    "ds_read_b128 v[80:83], v96 offset:47104\n\t" \
    "global_load_lds_dwordx4 v98, s[40:41] offset:2048 sc1\n\t" \
    "v_exp_f32 v24, v24\n\t" \
    "v_exp_f32 v25, v25\n\t" \
    "v_exp_f32 v26, v26\n\t" \
    "v_exp_f32 v27, v27\n\t" \
    "v_cvt_pk_bf16_f32 v92, v24, v25\n\t" \
    "v_cvt_pk_bf16_f32 v93, v26, v27\n\t" \
    "v_mfma_f32_32x32x16_bf16 v[0:15], v[72:75], v[88:91], v[0:15]\n\t" \
    "ds_read_b128 v[84:87], v96 offset:48128\n\t" \
    "global_load_lds_dwordx4 v98, s[40:41] offset:3072 sc1\n\t" \
    "v_exp_f32 v28, v28\n\t" \
    "v_exp_f32 v29, v29\n\t" \
    "v_exp_f32 v30, v30\n\t" \
    "v_exp_f32 v31, v31\n\t" \
    "v_cvt_pk_bf16_f32 v94, v28, v29\n\t" \
    "v_cvt_pk_bf16_f32 v95, v30, v31\n\t" \
    "v_exp_f32 v32, v32\n\t" \
    "v_exp_f32 v33, v33\n\t" \
    "v_mfma_f32_32x32x16_bf16 v[0:15], v[76:79], v[92:95], v[0:15]\n\t" \
    "ds_read_b128 v[64:67], v97 offset:4096\n\t" \
    "v_exp_f32 v34, v34\n\t" \
    "v_exp_f32 v35, v35\n\t" \
    "v_cvt_pk_bf16_f32 v88, v32, v33\n\t" \
    "v_cvt_pk_bf16_f32 v89, v34, v35\n\t" \
    "s_waitcnt lgkmcnt(2)\n\t" \
    "v_mfma_f32_32x32x16_bf16 v[16:31], v[56:59], v[48:51], 0\n\t" \
    "ds_read_b128 v[68:71], v97 offset:5120\n\t" \
    "global_load_dwordx4 v[136:139], v98, s[44:45] offset:1024 sc1\n\t" \
    "v_exp_f32 v36, v36\n\t" \
    "v_exp_f32 v37, v37\n\t" \
    "v_exp_f32 v38, v38\n\t" \
    "v_exp_f32 v39, v39\n\t" \
    "v_cvt_pk_bf16_f32 v90, v36, v37\n\t" \
    "v_cvt_pk_bf16_f32 v91, v38, v39\n\t" \
    "v_mfma_f32_32x32x16_bf16 v[16:31], v[60:63], v[52:55], v[16:31]\n\t" \
    "ds_read_b128 v[72:75], v97 offset:2048\n\t" \
    "v_exp_f32 v40, v40\n\t" \
    "v_exp_f32 v41, v41\n\t" \
    "v_exp_f32 v42, v42\n\t" \
    "v_exp_f32 v43, v43\n\t" \
    "v_cvt_pk_bf16_f32 v92, v40, v41\n\t" \
    "v_cvt_pk_bf16_f32 v93, v42, v43\n\t" \
    "v_mfma_f32_32x32x16_bf16 v[0:15], v[80:83], v[88:91], v[0:15]\n\t" \
    "ds_read_b128 v[76:79], v97 offset:3072\n\t" \
    "global_load_dwordx4 v[140:143], v98, s[44:45] offset:2048 sc1\n\t" \
    "v_exp_f32 v44, v44\n\t" \
    "v_exp_f32 v45, v45\n\t" \
    "v_exp_f32 v46, v46\n\t" \
    "v_exp_f32 v47, v47\n\t" \
    "v_cvt_pk_bf16_f32 v94, v44, v45\n\t" \
    "v_cvt_pk_bf16_f32 v95, v46, v47\n\t" \
    "v_exp_f32 v16, v16\n\t" \
    "v_exp_f32 v17, v17\n\t" \
    "v_mfma_f32_32x32x16_bf16 v[0:15], v[84:87], v[92:95], v[0:15]\n\t" \
    "ds_read_b128 v[56:59], v97 offset:8192\n\t" \
    "v_exp_f32 v18, v18\n\t" \
    "v_exp_f32 v19, v19\n\t" \
    "v_cvt_pk_bf16_f32 v88, v16, v17\n\t" \
    "v_cvt_pk_bf16_f32 v89, v18, v19\n\t" \
    "s_waitcnt lgkmcnt(2)\n\t" \
    "v_mfma_f32_32x32x16_bf16 v[32:47], v[64:67], v[48:51], 0\n\t" \
    "ds_read_b128 v[60:63], v97 offset:9216\n\t" \
    "global_load_dwordx4 v[144:147], v98, s[44:45] offset:3072 sc1\n\t" \
    "v_exp_f32 v20, v20\n\t" \
    "v_exp_f32 v21, v21\n\t" \
    "v_exp_f32 v22, v22\n\t" \
    "v_exp_f32 v23, v23\n\t" \
    "v_cvt_pk_bf16_f32 v90, v20, v21\n\t" \
    "v_cvt_pk_bf16_f32 v91, v22, v23\n\t" \
    "v_mfma_f32_32x32x16_bf16 v[32:47], v[68:71], v[52:55], v[32:47]\n\t" \
    "ds_read_b128 v[80:83], v97 offset:6144\n\t" \
    "v_exp_f32 v24, v24\n\t" \
    "v_exp_f32 v25, v25\n\t" \
    "v_exp_f32 v26, v26\n\t" \
    "v_exp_f32 v27, v27\n\t" \
    "v_cvt_pk_bf16_f32 v92, v24, v25\n\t" \
    "v_cvt_pk_bf16_f32 v93, v26, v27\n\t" \
    "v_mfma_f32_32x32x16_bf16 v[0:15], v[72:75], v[88:91], v[0:15]\n\t" \
    "ds_read_b128 v[84:87], v97 offset:7168\n\t" \
    "s_add_u32 s44, s44, 4096\n\t" \
    "s_addc_u32 s45, s45, 0\n\t" \
    "global_load_dwordx4 v[148:151], v98, s[44:45] offset:0 sc1\n\t" \
    "v_exp_f32 v28, v28\n\t" \
    "v_exp_f32 v29, v29\n\t" \
    "v_exp_f32 v30, v30\n\t" \
    "v_exp_f32 v31, v31\n\t" \
    "v_cvt_pk_bf16_f32 v94, v28, v29\n\t" \
    "v_cvt_pk_bf16_f32 v95, v30, v31\n\t" \
    "v_exp_f32 v32, v32\n\t" \
    "v_exp_f32 v33, v33\n\t" \
    "v_mfma_f32_32x32x16_bf16 v[0:15], v[76:79], v[92:95], v[0:15]\n\t" \
    "ds_read_b128 v[64:67], v97 offset:12288\n\t" \
    "v_exp_f32 v34, v34\n\t" \
    "v_exp_f32 v35, v35\n\t" \
    "v_cvt_pk_bf16_f32 v88, v32, v33\n\t" \
    "v_cvt_pk_bf16_f32 v89, v34, v35\n\t" \
    "s_waitcnt lgkmcnt(2)\n\t" \
    "v_mfma_f32_32x32x16_bf16 v[16:31], v[56:59], v[48:51], 0\n\t" \
    "ds_read_b128 v[68:71], v97 offset:13312\n\t" \
    "global_load_dwordx4 v[152:155], v98, s[44:45] offset:1024 sc1\n\t" \
    "v_exp_f32 v36, v36\n\t" \
    "v_exp_f32 v37, v37\n\t" \
    "v_exp_f32 v38, v38\n\t" \
    "v_exp_f32 v39, v39\n\t" \
    "v_cvt_pk_bf16_f32 v90, v36, v37\n\t" \
    "v_cvt_pk_bf16_f32 v91, v38, v39\n\t" \
    "v_mfma_f32_32x32x16_bf16 v[16:31], v[60:63], v[52:55], v[16:31]\n\t" \
    "ds_read_b128 v[72:75], v97 offset:10240\n\t" \
    "v_exp_f32 v40, v40\n\t" \
    "v_exp_f32 v41, v41\n\t" \
    "v_exp_f32 v42, v42\n\t" \
    "v_exp_f32 v43, v43\n\t" \
    "v_cvt_pk_bf16_f32 v92, v40, v41\n\t" \
    "v_cvt_pk_bf16_f32 v93, v42, v43\n\t" \
    "v_mfma_f32_32x32x16_bf16 v[0:15], v[80:83], v[88:91], v[0:15]\n\t" \
    "ds_read_b128 v[76:79], v97 offset:11264\n\t" \
    "global_load_dwordx4 v[156:159], v98, s[44:45] offset:2048 sc1\n\t" \
    "v_exp_f32 v44, v44\n\t" \
    "v_exp_f32 v45, v45\n\t" \
    "v_exp_f32 v46, v46\n\t" \
    "v_exp_f32 v47, v47\n\t" \
    "v_cvt_pk_bf16_f32 v94, v44, v45\n\t" \
    "v_cvt_pk_bf16_f32 v95, v46, v47\n\t" \
    "s_waitcnt vmcnt(6)\n\t" \
    "s_barrier\n\t" \
    "s_add_u32 m0, s42, 32768\n\t" \
    "s_add_u32 s40, s40, 16384\n\t" \
    "s_addc_u32 s41, s41, 0\n\t" \
    "v_exp_f32 v16, v16\n\t" \
    "v_exp_f32 v17, v17\n\t" \
    "v_mfma_f32_32x32x16_bf16 v[0:15], v[84:87], v[92:95], v[0:15]\n\t" \
    "ds_read_b128 v[56:59], v96 offset:0\n\t" \
    "v_exp_f32 v18, v18\n\t" \
    "v_exp_f32 v19, v19\n\t" \
    "v_cvt_pk_bf16_f32 v88, v16, v17\n\t" \
    "v_cvt_pk_bf16_f32 v89, v18, v19\n\t" \
    "s_waitcnt lgkmcnt(2)\n\t" \
    "v_mfma_f32_32x32x16_bf16 v[32:47], v[64:67], v[48:51], 0\n\t" \
    "ds_read_b128 v[60:63], v96 offset:1024\n\t" \
    "global_load_lds_dwordx4 v98, s[40:41] offset:0 sc1\n\t" \
    "global_load_lds_dwordx4 v98, s[40:41] offset:1024 sc1\n\t" \
    "v_exp_f32 v20, v20\n\t" \
    "v_exp_f32 v21, v21\n\t" \
    "v_exp_f32 v22, v22\n\t" \
    "v_exp_f32 v23, v23\n\t" \
    "v_cvt_pk_bf16_f32 v90, v20, v21\n\t" \
    "v_cvt_pk_bf16_f32 v91, v22, v23\n\t" \
    "v_mfma_f32_32x32x16_bf16 v[32:47], v[68:71], v[52:55], v[32:47]\n\t" \
    "ds_read_b128 v[80:83], v97 offset:14336\n\t" \
    "global_load_lds_dwordx4 v98, s[40:41] offset:2048 sc1\n\t" \
    "v_exp_f32 v24, v24\n\t" \
    "v_exp_f32 v25, v25\n\t" \
    "v_exp_f32 v26, v26\n\t" \
    "v_exp_f32 v27, v27\n\t" \
    "v_cvt_pk_bf16_f32 v92, v24, v25\n\t" \
    "v_cvt_pk_bf16_f32 v93, v26, v27\n\t" \
    "v_mfma_f32_32x32x16_bf16 v[0:15], v[72:75], v[88:91], v[0:15]\n\t" \
    "ds_read_b128 v[84:87], v97 offset:15360\n\t" \
    "global_load_lds_dwordx4 v98, s[40:41] offset:3072 sc1\n\t" \
    "v_exp_f32 v28, v28\n\t" \
    "v_exp_f32 v29, v29\n\t" \
    "v_exp_f32 v30, v30\n\t" \
    "v_exp_f32 v31, v31\n\t" \
    "v_cvt_pk_bf16_f32 v94, v28, v29\n\t" \
    "v_cvt_pk_bf16_f32 v95, v30, v31\n\t" \
    "v_exp_f32 v32, v32\n\t" \
    "v_exp_f32 v33, v33\n\t" \
    "v_mfma_f32_32x32x16_bf16 v[0:15], v[76:79], v[92:95], v[0:15]\n\t" \
    "ds_read_b128 v[64:67], v96 offset:4096\n\t" \
    "v_exp_f32 v34, v34\n\t" \
    "v_exp_f32 v35, v35\n\t" \
    "v_cvt_pk_bf16_f32 v88, v32, v33\n\t" \
    "v_cvt_pk_bf16_f32 v89, v34, v35\n\t" \
    "s_waitcnt lgkmcnt(2)\n\t" \
    "v_mfma_f32_32x32x16_bf16 v[16:31], v[56:59], v[48:51], 0\n\t" \
    "ds_read_b128 v[68:71], v96 offset:5120\n\t" \
    "global_load_dwordx4 v[160:163], v98, s[44:45] offset:3072 sc1\n\t" \
    "v_exp_f32 v36, v36\n\t" \
    "v_exp_f32 v37, v37\n\t" \
    "v_exp_f32 v38, v38\n\t" \
    "v_exp_f32 v39, v39\n\t" \
    "v_cvt_pk_bf16_f32 v90, v36, v37\n\t" \
    "v_cvt_pk_bf16_f32 v91, v38, v39\n\t" \
    "v_mfma_f32_32x32x16_bf16 v[16:31], v[60:63], v[52:55], v[16:31]\n\t" \
    "ds_read_b128 v[72:75], v96 offset:2048\n\t" \
    "v_exp_f32 v40, v40\n\t" \
    "v_exp_f32 v41, v41\n\t" \
    "v_exp_f32 v42, v42\n\t" \
    "v_exp_f32 v43, v43\n\t" \
    "v_cvt_pk_bf16_f32 v92, v40, v41\n\t" \
    "v_cvt_pk_bf16_f32 v93, v42, v43\n\t" \
    "v_mfma_f32_32x32x16_bf16 v[0:15], v[80:83], v[88:91], v[0:15]\n\t" \
    "ds_read_b128 v[76:79], v96 offset:3072\n\t" \
    "s_add_u32 s44, s44, 4096\n\t" \
    "s_addc_u32 s45, s45, 0\n\t" \
    "global_load_dwordx4 v[164:167], v98, s[44:45] offset:0 sc1\n\t" \
    "v_exp_f32 v44, v44\n\t" \
    "v_exp_f32 v45, v45\n\t" \
    "v_exp_f32 v46, v46\n\t" \
    "v_exp_f32 v47, v47\n\t" \
    "v_cvt_pk_bf16_f32 v94, v44, v45\n\t" \
    "v_cvt_pk_bf16_f32 v95, v46, v47\n\t" \
    "v_exp_f32 v16, v16\n\t" \
    "v_exp_f32 v17, v17\n\t" \
    "v_mfma_f32_32x32x16_bf16 v[0:15], v[84:87], v[92:95], v[0:15]\n\t" \
    "ds_read_b128 v[56:59], v96 offset:8192\n\t" \
    "v_exp_f32 v18, v18\n\t" \
    "v_exp_f32 v19, v19\n\t" \
    "v_cvt_pk_bf16_f32 v88, v16, v17\n\t" \
    "v_cvt_pk_bf16_f32 v89, v18, v19\n\t" \
    "s_waitcnt lgkmcnt(2)\n\t" \
    "v_mfma_f32_32x32x16_bf16 v[32:47], v[64:67], v[48:51], 0\n\t" \
    "ds_read_b128 v[60:63], v96 offset:9216\n\t" \
    "global_load_dwordx4 v[168:171], v98, s[44:45] offset:1024 sc1\n\t" \
    "v_exp_f32 v20, v20\n\t" \
    "v_exp_f32 v21, v21\n\t" \
    "v_exp_f32 v22, v22\n\t" \
    "v_exp_f32 v23, v23\n\t" \
    "v_cvt_pk_bf16_f32 v90, v20, v21\n\t" \
    "v_cvt_pk_bf16_f32 v91, v22, v23\n\t" \
    "v_mfma_f32_32x32x16_bf16 v[32:47], v[68:71], v[52:55], v[32:47]\n\t" \
    "ds_read_b128 v[80:83], v96 offset:6144\n\t" \
    "v_exp_f32 v24, v24\n\t" \
    "v_exp_f32 v25, v25\n\t" \
    "v_exp_f32 v26, v26\n\t" \
    "v_exp_f32 v27, v27\n\t" \
    "v_cvt_pk_bf16_f32 v92, v24, v25\n\t" \
    "v_cvt_pk_bf16_f32 v93, v26, v27\n\t" \
    "v_mfma_f32_32x32x16_bf16 v[0:15], v[72:75], v[88:91], v[0:15]\n\t" \
    "ds_read_b128 v[84:87], v96 offset:7168\n\t" \
    "global_load_dwordx4 v[172:175], v98, s[44:45] offset:2048 sc1\n\t" \
    "v_exp_f32 v28, v28\n\t" \
    "v_exp_f32 v29, v29\n\t" \
    "v_exp_f32 v30, v30\n\t" \
    "v_exp_f32 v31, v31\n\t" \
    "v_cvt_pk_bf16_f32 v94, v28, v29\n\t" \
    "v_cvt_pk_bf16_f32 v95, v30, v31\n\t" \
    "v_exp_f32 v32, v32\n\t" \
    "v_exp_f32 v33, v33\n\t" \
    "v_mfma_f32_32x32x16_bf16 v[0:15], v[76:79], v[92:95], v[0:15]\n\t" \
    "ds_read_b128 v[64:67], v96 offset:12288\n\t" \
    "v_exp_f32 v34, v34\n\t" \
    "v_exp_f32 v35, v35\n\t" \
    "v_cvt_pk_bf16_f32 v88, v32, v33\n\t" \
    "v_cvt_pk_bf16_f32 v89, v34, v35\n\t" \
    "s_waitcnt lgkmcnt(2)\n\t" \
    "v_mfma_f32_32x32x16_bf16 v[16:31], v[56:59], v[48:51], 0\n\t" \
    "ds_read_b128 v[68:71], v96 offset:13312\n\t" \
    "global_load_dwordx4 v[176:179], v98, s[44:45] offset:3072 sc1\n\t" \
    "v_exp_f32 v36, v36\n\t" \
    "v_exp_f32 v37, v37\n\t" \
    "v_exp_f32 v38, v38\n\t" \
    "v_exp_f32 v39, v39\n\t" \
    "v_cvt_pk_bf16_f32 v90, v36, v37\n\t" \
    "v_cvt_pk_bf16_f32 v91, v38, v39\n\t" \
    "v_mfma_f32_32x32x16_bf16 v[16:31], v[60:63], v[52:55], v[16:31]\n\t" \
    "ds_read_b128 v[72:75], v96 offset:10240\n\t" \
    "v_exp_f32 v40, v40\n\t" \
    "v_exp_f32 v41, v41\n\t" \
    "v_exp_f32 v42, v42\n\t" \
    "v_exp_f32 v43, v43\n\t" \
    "v_cvt_pk_bf16_f32 v92, v40, v41\n\t" \
    "v_cvt_pk_bf16_f32 v93, v42, v43\n\t" \
    "v_mfma_f32_32x32x16_bf16 v[0:15], v[80:83], v[88:91], v[0:15]\n\t" \
    "ds_read_b128 v[76:79], v96 offset:11264\n\t" \
    "s_add_u32 s44, s44, 4096\n\t" \
    "s_addc_u32 s45, s45, 0\n\t" \
    "global_load_dwordx4 v[180:183], v98, s[44:45] offset:0 sc1\n\t" \
    "v_exp_f32 v44, v44\n\t" \
    "v_exp_f32 v45, v45\n\t" \
    "v_exp_f32 v46, v46\n\t" \
    "v_exp_f32 v47, v47\n\t" \
    "v_cvt_pk_bf16_f32 v94, v44, v45\n\t" \
    "v_cvt_pk_bf16_f32 v95, v46, v47\n\t" \
    "s_waitcnt vmcnt(6)\n\t" \
    "s_barrier\n\t" \
    "s_add_u32 m0, s42, 65536\n\t" \
    "s_add_u32 s40, s40, 16384\n\t" \
    "s_addc_u32 s41, s41, 0\n\t" \
    "v_exp_f32 v16, v16\n\t" \
    "v_exp_f32 v17, v17\n\t" \
    "v_mfma_f32_32x32x16_bf16 v[0:15], v[84:87], v[92:95], v[0:15]\n\t" \
    "ds_read_b128 v[56:59], v96 offset:32768\n\t" \
    "v_exp_f32 v18, v18\n\t" \
    "v_exp_f32 v19, v19\n\t" \
    "v_cvt_pk_bf16_f32 v88, v16, v17\n\t" \
    "v_cvt_pk_bf16_f32 v89, v18, v19\n\t" \
    "s_waitcnt lgkmcnt(2)\n\t" \
    "v_mfma_f32_32x32x16_bf16 v[32:47], v[64:67], v[48:51], 0\n\t" \
    "ds_read_b128 v[60:63], v96 offset:33792\n\t" \
    "global_load_lds_dwordx4 v98, s[40:41] offset:0 sc1\n\t" \
    "global_load_lds_dwordx4 v98, s[40:41] offset:1024 sc1\n\t" \
    "v_exp_f32 v20, v20\n\t" \
    "v_exp_f32 v21, v21\n\t" \
    "v_exp_f32 v22, v22\n\t" \
    "v_exp_f32 v23, v23\n\t" \
    "v_cvt_pk_bf16_f32 v90, v20, v21\n\t" \
    "v_cvt_pk_bf16_f32 v91, v22, v23\n\t" \
    "v_mfma_f32_32x32x16_bf16 v[32:47], v[68:71], v[52:55], v[32:47]\n\t" \
    "ds_read_b128 v[80:83], v96 offset:14336\n\t" \
    "global_load_lds_dwordx4 v98, s[40:41] offset:2048 sc1\n\t" \
    "v_exp_f32 v24, v24\n\t" \
    "v_exp_f32 v25, v25\n\t" \
    "v_exp_f32 v26, v26\n\t" \
    "v_exp_f32 v27, v27\n\t" \
    "v_cvt_pk_bf16_f32 v92, v24, v25\n\t" \
    "v_cvt_pk_bf16_f32 v93, v26, v27\n\t" \
    "v_mfma_f32_32x32x16_bf16 v[0:15], v[72:75], v[88:91], v[0:15]\n\t" \
    "ds_read_b128 v[84:87], v96 offset:15360\n\t" \
    "global_load_lds_dwordx4 v98, s[40:41] offset:3072 sc1\n\t" \
    "v_exp_f32 v28, v28\n\t" \
    "v_exp_f32 v29, v29\n\t" \
    "v_exp_f32 v30, v30\n\t" \
    "v_exp_f32 v31, v31\n\t" \
    "v_cvt_pk_bf16_f32 v94, v28, v29\n\t" \
    "v_cvt_pk_bf16_f32 v95, v30, v31\n\t" \
    "v_exp_f32 v32, v32\n\t" \
    "v_exp_f32 v33, v33\n\t" \
    "v_mfma_f32_32x32x16_bf16 v[0:15], v[76:79], v[92:95], v[0:15]\n\t" \
    "ds_read_b128 v[64:67], v96 offset:36864\n\t" \
    "v_exp_f32 v34, v34\n\t" \
    "v_exp_f32 v35, v35\n\t" \
    "v_cvt_pk_bf16_f32 v88, v32, v33\n\t" \
    "v_cvt_pk_bf16_f32 v89, v34, v35\n\t" \
    "s_waitcnt lgkmcnt(2)\n\t" \
    "v_mfma_f32_32x32x16_bf16 v[16:31], v[56:59], v[48:51], 0\n\t" \
    "ds_read_b128 v[68:71], v96 offset:37888\n\t" \
    "global_load_dwordx4 v[184:187], v98, s[44:45] offset:1024 sc1\n\t" \
    "v_exp_f32 v36, v36\n\t" \
    "v_exp_f32 v37, v37\n\t" \
    "v_exp_f32 v38, v38\n\t" \
    "v_exp_f32 v39, v39\n\t" \
    "v_cvt_pk_bf16_f32 v90, v36, v37\n\t" \
    "v_cvt_pk_bf16_f32 v91, v38, v39\n\t" \
    "v_mfma_f32_32x32x16_bf16 v[16:31], v[60:63], v[52:55], v[16:31]\n\t" \
    "ds_read_b128 v[72:75], v96 offset:34816\n\t" \
    "v_exp_f32 v40, v40\n\t" \
    "v_exp_f32 v41, v41\n\t" \
    "v_exp_f32 v42, v42\n\t" \
    "v_exp_f32 v43, v43\n\t" \
    "v_cvt_pk_bf16_f32 v92, v40, v41\n\t" \
    "v_cvt_pk_bf16_f32 v93, v42, v43\n\t" \
    "v_mfma_f32_32x32x16_bf16 v[0:15], v[80:83], v[88:91], v[0:15]\n\t" \
    "ds_read_b128 v[76:79], v96 offset:35840\n\t" \
    "global_load_dwordx4 v[188:191], v98, s[44:45] offset:2048 sc1\n\t" \
    "v_exp_f32 v44, v44\n\t" \
    "v_exp_f32 v45, v45\n\t" \
    "v_exp_f32 v46, v46\n\t" \
    "v_exp_f32 v47, v47\n\t" \
    "v_cvt_pk_bf16_f32 v94, v44, v45\n\t" \
    "v_cvt_pk_bf16_f32 v95, v46, v47\n\t" \
    "v_exp_f32 v16, v16\n\t" \
    "v_exp_f32 v17, v17\n\t" \
    "v_mfma_f32_32x32x16_bf16 v[0:15], v[84:87], v[92:95], v[0:15]\n\t" \
    "ds_read_b128 v[56:59], v96 offset:40960\n\t" \
    "v_exp_f32 v18, v18\n\t" \
    "v_exp_f32 v19, v19\n\t" \
    "v_cvt_pk_bf16_f32 v88, v16, v17\n\t" \
    "v_cvt_pk_bf16_f32 v89, v18, v19\n\t" \
    "s_waitcnt lgkmcnt(2)\n\t" \
    "v_mfma_f32_32x32x16_bf16 v[32:47], v[64:67], v[48:51], 0\n\t" \
    "ds_read_b128 v[60:63], v96 offset:41984\n\t" \
    "global_load_dwordx4 v[192:195], v98, s[44:45] offset:3072 sc1\n\t" \
    "v_exp_f32 v20, v20\n\t" \
    "v_exp_f32 v21, v21\n\t" \
    "v_exp_f32 v22, v22\n\t" \
    "v_exp_f32 v23, v23\n\t" \
    "v_cvt_pk_bf16_f32 v90, v20, v21\n\t" \
    "v_cvt_pk_bf16_f32 v91, v22, v23\n\t" \
    "v_mfma_f32_32x32x16_bf16 v[32:47], v[68:71], v[52:55], v[32:47]\n\t" \
    "ds_read_b128 v[80:83], v96 offset:38912\n\t" \
    "v_exp_f32 v24, v24\n\t" \
    "v_exp_f32 v25, v25\n\t" \
    "v_exp_f32 v26, v26\n\t" \
    "v_exp_f32 v27, v27\n\t" \
    "v_cvt_pk_bf16_f32 v92, v24, v25\n\t" \
    "v_cvt_pk_bf16_f32 v93, v26, v27\n\t" \
    "v_mfma_f32_32x32x16_bf16 v[0:15], v[72:75], v[88:91], v[0:15]\n\t" \
    "ds_read_b128 v[84:87], v96 offset:39936\n\t" \
    "s_add_u32 s44, s44, 4096\n\t" \
    "s_addc_u32 s45, s45, 0\n\t" \
    "global_load_dwordx4 v[196:199], v98, s[44:45] offset:0 sc1\n\t" \
    "v_exp_f32 v28, v28\n\t" \
    "v_exp_f32 v29, v29\n\t" \
    "v_exp_f32 v30, v30\n\t" \
    "v_exp_f32 v31, v31\n\t" \
    "v_cvt_pk_bf16_f32 v94, v28, v29\n\t" \
    "v_cvt_pk_bf16_f32 v95, v30, v31\n\t" \
    "v_exp_f32 v32, v32\n\t" \
    "v_exp_f32 v33, v33\n\t" \
    "v_mfma_f32_32x32x16_bf16 v[0:15], v[76:79], v[92:95], v[0:15]\n\t" \
    "ds_read_b128 v[64:67], v96 offset:45056\n\t" \
    "v_exp_f32 v34, v34\n\t" \
    "v_exp_f32 v35, v35\n\t" \
    "v_cvt_pk_bf16_f32 v88, v32, v33\n\t" \
    "v_cvt_pk_bf16_f32 v89, v34, v35\n\t" \
    "s_waitcnt lgkmcnt(2)\n\t" \
    "v_mfma_f32_32x32x16_bf16 v[16:31], v[56:59], v[48:51], 0\n\t" \
    "ds_read_b128 v[68:71], v96 offset:46080\n\t" \
    "global_load_dwordx4 v[200:203], v98, s[44:45] offset:1024 sc1\n\t" \
    "v_exp_f32 v36, v36\n\t" \
    "v_exp_f32 v37, v37\n\t" \
    "v_exp_f32 v38, v38\n\t" \
    "v_exp_f32 v39, v39\n\t" \
    "v_cvt_pk_bf16_f32 v90, v36, v37\n\t" \
    "v_cvt_pk_bf16_f32 v91, v38, v39\n\t" \
    "v_mfma_f32_32x32x16_bf16 v[16:31], v[60:63], v[52:55], v[16:31]\n\t" \
    "ds_read_b128 v[72:75], v96 offset:43008\n\t" \
    "v_exp_f32 v40, v40\n\t" \
    "v_exp_f32 v41, v41\n\t" \
    "v_exp_f32 v42, v42\n\t" \
    "v_exp_f32 v43, v43\n\t" \
    "v_cvt_pk_bf16_f32 v92, v40, v41\n\t" \
    "v_cvt_pk_bf16_f32 v93, v42, v43\n\t" \
    "v_mfma_f32_32x32x16_bf16 v[0:15], v[80:83], v[88:91], v[0:15]\n\t" \
    "ds_read_b128 v[76:79], v96 offset:44032\n\t" \
    "global_load_dwordx4 v[204:207], v98, s[44:45] offset:2048 sc1\n\t" \
    "v_exp_f32 v44, v44\n\t" \
    "v_exp_f32 v45, v45\n\t" \
    "v_exp_f32 v46, v46\n\t" \
    "v_exp_f32 v47, v47\n\t" \
    "v_cvt_pk_bf16_f32 v94, v44, v45\n\t" \
    "v_cvt_pk_bf16_f32 v95, v46, v47\n\t" \
    "s_waitcnt vmcnt(6)\n\t" \
    "s_barrier\n\t" \
    "v_exp_f32 v16, v16\n\t" \
    "v_exp_f32 v17, v17\n\t" \
    "v_mfma_f32_32x32x16_bf16 v[0:15], v[84:87], v[92:95], v[0:15]\n\t" \
    "ds_read_b128 v[56:59], v97 offset:0\n\t" \
    "v_exp_f32 v18, v18\n\t" \
    "v_exp_f32 v19, v19\n\t" \
    "v_cvt_pk_bf16_f32 v88, v16, v17\n\t" \
    "v_cvt_pk_bf16_f32 v89, v18, v19\n\t" \
    "s_waitcnt lgkmcnt(2)\n\t" \
    "v_mfma_f32_32x32x16_bf16 v[32:47], v[64:67], v[48:51], 0\n\t" \
    "ds_read_b128 v[60:63], v97 offset:1024\n\t" \
    "global_load_dwordx4 v[208:211], v98, s[44:45] offset:3072 sc1\n\t" \
    "v_exp_f32 v20, v20\n\t" \
    "v_exp_f32 v21, v21\n\t" \
    "v_exp_f32 v22, v22\n\t" \
    "v_exp_f32 v23, v23\n\t" \
    "v_cvt_pk_bf16_f32 v90, v20, v21\n\t" \
    "v_cvt_pk_bf16_f32 v91, v22, v23\n\t" \
    "v_mfma_f32_32x32x16_bf16 v[32:47], v[68:71], v[52:55], v[32:47]\n\t" \
    "ds_read_b128 v[80:83], v96 offset:47104\n\t" \
    "v_exp_f32 v24, v24\n\t" \
    "v_exp_f32 v25, v25\n\t" \
    "v_exp_f32 v26, v26\n\t" \
    "v_exp_f32 v27, v27\n\t" \
    "v_cvt_pk_bf16_f32 v92, v24, v25\n\t" \
    "v_cvt_pk_bf16_f32 v93, v26, v27\n\t" \
    "v_mfma_f32_32x32x16_bf16 v[0:15], v[72:75], v[88:91], v[0:15]\n\t" \
    "ds_read_b128 v[84:87], v96 offset:48128\n\t" \
    "v_exp_f32 v28, v28\n\t" \
    "v_exp_f32 v29, v29\n\t" \
    "v_exp_f32 v30, v30\n\t" \
    "v_exp_f32 v31, v31\n\t" \
    "v_cvt_pk_bf16_f32 v94, v28, v29\n\t" \
    "v_cvt_pk_bf16_f32 v95, v30, v31\n\t" \
    "v_exp_f32 v32, v32\n\t" \
    "v_exp_f32 v33, v33\n\t" \
    "v_mfma_f32_32x32x16_bf16 v[0:15], v[76:79], v[92:95], v[0:15]\n\t" \
    "ds_read_b128 v[64:67], v97 offset:4096\n\t" \
    "v_exp_f32 v34, v34\n\t" \
    "v_exp_f32 v35, v35\n\t" \
    "v_cvt_pk_bf16_f32 v88, v32, v33\n\t" \
    "v_cvt_pk_bf16_f32 v89, v34, v35\n\t" \
    "s_waitcnt lgkmcnt(2)\n\t" \
    "v_mfma_f32_32x32x16_bf16 v[16:31], v[56:59], v[48:51], 0\n\t" \
    "ds_read_b128 v[68:71], v97 offset:5120\n\t" \
    "v_exp_f32 v36, v36\n\t" \
    "v_exp_f32 v37, v37\n\t" \
    "v_exp_f32 v38, v38\n\t" \
    "v_exp_f32 v39, v39\n\t" \
    "v_cvt_pk_bf16_f32 v90, v36, v37\n\t" \
    "v_cvt_pk_bf16_f32 v91, v38, v39\n\t" \
    "v_mfma_f32_32x32x16_bf16 v[16:31], v[60:63], v[52:55], v[16:31]\n\t" \
    "ds_read_b128 v[72:75], v97 offset:2048\n\t" \
    "v_exp_f32 v40, v40\n\t" \
    "v_exp_f32 v41, v41\n\t" \
    "v_exp_f32 v42, v42\n\t" \
    "v_exp_f32 v43, v43\n\t" \
    "v_cvt_pk_bf16_f32 v92, v40, v41\n\t" \
    "v_cvt_pk_bf16_f32 v93, v42, v43\n\t" \
    "v_mfma_f32_32x32x16_bf16 v[0:15], v[80:83], v[88:91], v[0:15]\n\t" \
    "ds_read_b128 v[76:79], v97 offset:3072\n\t" \
    "v_exp_f32 v44, v44\n\t" \
    "v_exp_f32 v45, v45\n\t" \
    "v_exp_f32 v46, v46\n\t" \
    "v_exp_f32 v47, v47\n\t" \
    "v_cvt_pk_bf16_f32 v94, v44, v45\n\t" \
    "v_cvt_pk_bf16_f32 v95, v46, v47\n\t" \
    "v_exp_f32 v16, v16\n\t" \
    "v_exp_f32 v17, v17\n\t" \
    "v_mfma_f32_32x32x16_bf16 v[0:15], v[84:87], v[92:95], v[0:15]\n\t" \
    "ds_read_b128 v[56:59], v97 offset:8192\n\t" \
    "v_exp_f32 v18, v18\n\t" \
    "v_exp_f32 v19, v19\n\t" \
    "v_cvt_pk_bf16_f32 v88, v16, v17\n\t" \
    "v_cvt_pk_bf16_f32 v89, v18, v19\n\t" \
    "s_waitcnt lgkmcnt(2)\n\t" \
    "v_mfma_f32_32x32x16_bf16 v[32:47], v[64:67], v[48:51], 0\n\t" \
    "ds_read_b128 v[60:63], v97 offset:9216\n\t" \
    "v_exp_f32 v20, v20\n\t" \
    "v_exp_f32 v21, v21\n\t" \
    "v_exp_f32 v22, v22\n\t" \
    "v_exp_f32 v23, v23\n\t" \
    "v_cvt_pk_bf16_f32 v90, v20, v21\n\t" \
    "v_cvt_pk_bf16_f32 v91, v22, v23\n\t" \
    "v_mfma_f32_32x32x16_bf16 v[32:47], v[68:71], v[52:55], v[32:47]\n\t" \
    "ds_read_b128 v[80:83], v97 offset:6144\n\t" \
    "v_exp_f32 v24, v24\n\t" \
    "v_exp_f32 v25, v25\n\t" \
    "v_exp_f32 v26, v26\n\t" \
    "v_exp_f32 v27, v27\n\t" \
    "v_cvt_pk_bf16_f32 v92, v24, v25\n\t" \
    "v_cvt_pk_bf16_f32 v93, v26, v27\n\t" \
    "v_mfma_f32_32x32x16_bf16 v[0:15], v[72:75], v[88:91], v[0:15]\n\t" \
    "ds_read_b128 v[84:87], v97 offset:7168\n\t" \
    "v_exp_f32 v28, v28\n\t" \
    "v_exp_f32 v29, v29\n\t" \
    "v_exp_f32 v30, v30\n\t" \
    "v_exp_f32 v31, v31\n\t" \
    "v_cvt_pk_bf16_f32 v94, v28, v29\n\t" \
    "v_cvt_pk_bf16_f32 v95, v30, v31\n\t" \
    "v_exp_f32 v32, v32\n\t" \
    "v_exp_f32 v33, v33\n\t" \
    "v_mfma_f32_32x32x16_bf16 v[0:15], v[76:79], v[92:95], v[0:15]\n\t" \
    "ds_read_b128 v[64:67], v97 offset:12288\n\t" \
    "v_exp_f32 v34, v34\n\t" \
    "v_exp_f32 v35, v35\n\t" \
    "v_cvt_pk_bf16_f32 v88, v32, v33\n\t" \
    "v_cvt_pk_bf16_f32 v89, v34, v35\n\t" \
    "s_waitcnt lgkmcnt(2)\n\t" \
    "v_mfma_f32_32x32x16_bf16 v[16:31], v[56:59], v[48:51], 0\n\t" \
    "ds_read_b128 v[68:71], v97 offset:13312\n\t" \
    "v_exp_f32 v36, v36\n\t" \
    "v_exp_f32 v37, v37\n\t" \
    "v_exp_f32 v38, v38\n\t" \
    "v_exp_f32 v39, v39\n\t" \
    "v_cvt_pk_bf16_f32 v90, v36, v37\n\t" \
    "v_cvt_pk_bf16_f32 v91, v38, v39\n\t" \
    "v_mfma_f32_32x32x16_bf16 v[16:31], v[60:63], v[52:55], v[16:31]\n\t" \
    "ds_read_b128 v[72:75], v97 offset:10240\n\t" \
    "v_exp_f32 v40, v40\n\t" \
    "v_exp_f32 v41, v41\n\t" \
    "v_exp_f32 v42, v42\n\t" \
    "v_exp_f32 v43, v43\n\t" \
    "v_cvt_pk_bf16_f32 v92, v40, v41\n\t" \
    "v_cvt_pk_bf16_f32 v93, v42, v43\n\t" \
    "v_mfma_f32_32x32x16_bf16 v[0:15], v[80:83], v[88:91], v[0:15]\n\t" \
    "ds_read_b128 v[76:79], v97 offset:11264\n\t" \
    "v_exp_f32 v44, v44\n\t" \
    "v_exp_f32 v45, v45\n\t" \
    "v_exp_f32 v46, v46\n\t" \
    "v_exp_f32 v47, v47\n\t" \
    "v_cvt_pk_bf16_f32 v94, v44, v45\n\t" \
    "v_cvt_pk_bf16_f32 v95, v46, v47\n\t" \
    "v_exp_f32 v16, v16\n\t" \
    "v_exp_f32 v17, v17\n\t" \
    "v_mfma_f32_32x32x16_bf16 v[0:15], v[84:87], v[92:95], v[0:15]\n\t" \
    "v_exp_f32 v18, v18\n\t" \
    "v_exp_f32 v19, v19\n\t" \
    "v_cvt_pk_bf16_f32 v88, v16, v17\n\t" \
    "v_cvt_pk_bf16_f32 v89, v18, v19\n\t" \
    "s_waitcnt lgkmcnt(1)\n\t" \
    "v_mfma_f32_32x32x16_bf16 v[32:47], v[64:67], v[48:51], 0\n\t" \
    "v_exp_f32 v20, v20\n\t" \
    "v_exp_f32 v21, v21\n\t" \
    "v_exp_f32 v22, v22\n\t" \
    "v_exp_f32 v23, v23\n\t" \
    "v_cvt_pk_bf16_f32 v90, v20, v21\n\t" \
    "v_cvt_pk_bf16_f32 v91, v22, v23\n\t" \
    "v_mfma_f32_32x32x16_bf16 v[32:47], v[68:71], v[52:55], v[32:47]\n\t" \
    "ds_read_b128 v[80:83], v97 offset:14336\n\t" \
    "v_exp_f32 v24, v24\n\t" \
    "v_exp_f32 v25, v25\n\t" \
    "v_exp_f32 v26, v26\n\t" \
    "v_exp_f32 v27, v27\n\t" \
    "v_cvt_pk_bf16_f32 v92, v24, v25\n\t" \
    "v_cvt_pk_bf16_f32 v93, v26, v27\n\t" \
    "v_mfma_f32_32x32x16_bf16 v[0:15], v[72:75], v[88:91], v[0:15]\n\t" \
    "ds_read_b128 v[84:87], v97 offset:15360\n\t" \
    "v_exp_f32 v28, v28\n\t" \
    "v_exp_f32 v29, v29\n\t" \
    "v_exp_f32 v30, v30\n\t" \
    "v_exp_f32 v31, v31\n\t" \
    "v_cvt_pk_bf16_f32 v94, v28, v29\n\t" \
    "v_cvt_pk_bf16_f32 v95, v30, v31\n\t" \
    "v_exp_f32 v32, v32\n\t" \
    "v_exp_f32 v33, v33\n\t" \
    "v_mfma_f32_32x32x16_bf16 v[0:15], v[76:79], v[92:95], v[0:15]\n\t" \
    "v_exp_f32 v34, v34\n\t" \
    "v_exp_f32 v35, v35\n\t" \
    "v_cvt_pk_bf16_f32 v88, v32, v33\n\t" \
    "v_cvt_pk_bf16_f32 v89, v34, v35\n\t" \
    "s_waitcnt lgkmcnt(1)\n\t" \
    "v_exp_f32 v36, v36\n\t" \
    "v_exp_f32 v37, v37\n\t" \
    "v_exp_f32 v38, v38\n\t" \
    "v_exp_f32 v39, v39\n\t" \
    "v_cvt_pk_bf16_f32 v90, v36, v37\n\t" \
    "v_cvt_pk_bf16_f32 v91, v38, v39\n\t" \
    "v_exp_f32 v40, v40\n\t" \
    "v_exp_f32 v41, v41\n\t" \
    "v_exp_f32 v42, v42\n\t" \
    "v_exp_f32 v43, v43\n\t" \
    "v_cvt_pk_bf16_f32 v92, v40, v41\n\t" \
    "v_cvt_pk_bf16_f32 v93, v42, v43\n\t" \
    "v_mfma_f32_32x32x16_bf16 v[0:15], v[80:83], v[88:91], v[0:15]\n\t" \
    "v_exp_f32 v44, v44\n\t" \
    "v_exp_f32 v45, v45\n\t" \
    "v_exp_f32 v46, v46\n\t" \
    "v_exp_f32 v47, v47\n\t" \
    "v_cvt_pk_bf16_f32 v94, v44, v45\n\t" \
    "v_cvt_pk_bf16_f32 v95, v46, v47\n\t" \
    "s_waitcnt lgkmcnt(0)\n\t" \
    "s_nop 0\n\t" \
    "v_mfma_f32_32x32x16_bf16 v[0:15], v[84:87], v[92:95], v[0:15]\n\t" \
    "s_mov_b32 m0, s43\n\t" \
    "s_setprio 0\n\t" \
    "s_waitcnt vmcnt(0)\n\t" \
    "s_nop 7\n\t" \
    "s_nop 7\n\t"
#define ATTN_FAST_CLOBBERS "v16", "v17", "v18", "v19", "v20", "v21", "v22", "v23", "v24", "v25", "v26", "v27", "v28", "v29", "v30", "v31", "v32", "v33", "v34", "v35", "v36", "v37", "v38", "v39", "v40", "v41", "v42", "v43", "v44", "v45", "v46", "v47", "v56", "v57", "v58", "v59", "v60", "v61", "v62", "v63", "v64", "v65", "v66", "v67", "v68", "v69", "v70", "v71", "v72", "v73", "v74", "v75", "v76", "v77", "v78", "v79", "v80", "v81", "v82", "v83", "v84", "v85", "v86", "v87", "v88", "v89", "v90", "v91", "v92", "v93", "v94", "v95", "v212", "v213", "v214", "v218", "s43", "s49", "vcc", "scc", "memory"

constexpr int A_XA = 0;
constexpr int A_XB = 57344;
constexpr int A_WA = 114688;
constexpr int A_QT = 143360;
constexpr int A_KN = A_QT + 8192;
constexpr int A_SX = 157696;
constexpr int A_RS = A_SX + 512;
constexpr int A_CS = A_RS + 512;
constexpr int A_SQ = A_CS + 7 * 512;
constexpr int L_CTL = A_SQ + 1024;
constexpr int A_B1 = L_CTL + 256;
constexpr int L_TOTAL = 163840;
constexpr int A_SLOT = 28672;
#define SBAR() __builtin_amdgcn_sched_barrier(0)
#define PIN(x) asm volatile("" : "+v"(x))

DI void dma1_sc1(const void* sbase_, unsigned voff, unsigned lds_dst_) {
    const void* sbase = uptr(sbase_); const unsigned lds_dst = rfl(lds_dst_);
    asm volatile("s_mov_b32 m0, %2\n\ts_nop 3\n\tglobal_load_lds_dwordx4 %0, %1 sc1" :: "v"(voff), "s"(sbase), "s"(lds_dst) : "memory");
}
DI void set_m0(unsigned v) { asm volatile("s_mov_b32 m0, %0\n\ts_nop 0" :: "s"(v) : "memory"); }
template <int OFF> DI void dma_off_sc1(const void* sbase, unsigned voff) { asm volatile("global_load_lds_dwordx4 %0, %1 offset:%c2 sc1" :: "v"(voff), "s"(sbase), "i"(OFF) : "memory"); }
DI void dma4_sc1(const void* sbase_, unsigned voff, unsigned lds_dst_) {
    const void* sbase = uptr(sbase_); const unsigned lds_dst = rfl(lds_dst_);
    asm volatile("s_mov_b32 m0, %2\n\ts_nop 3\n\tglobal_load_lds_dwordx4 %0, %1 sc1\n\tglobal_load_lds_dwordx4 %0, %1 offset:1024 sc1\n\t"
                 "global_load_lds_dwordx4 %0, %1 offset:2048 sc1\n\tglobal_load_lds_dwordx4 %0, %1 offset:3072 sc1" :: "v"(voff), "s"(sbase), "s"(lds_dst) : "memory");
}
DI float dot2(unsigned a, unsigned b, float c) { return __builtin_amdgcn_fdot2_f32_bf16(asbf2(a), asbf2(b), c, false); }
DI u32x4 asu4(bf16x8 v) { return __builtin_bit_cast(u32x4, v); }
DI bf16x8 r1_feat(float cs, unsigned bias16, int h) { const unsigned hi = cvtpk(cs, cs); const float lo = cs - bflo(hi); const unsigned lw = cvtpk(lo, lo);
    const u32x4 v = {h ? 0u : hi, h ? 0u : lw, h ? 0u : (bias16 | (bias16 << 16)), 0u}; return asbf8(v); }
DI unsigned split2(float v) { const unsigned hi = cvtpk(v, v); return cvtpk(bflo(hi), v - bflo(hi)); }
DI bf16x8 r1_tok(float nm, float sigma, int h) { const unsigned w = split2(nm), sg = split2(sigma); const u32x4 v = {h ? 0u : w, h ? 0u : w, h ? 0u : sg, 0u}; return asbf8(v); }

DI float gelu_scaled(float a, float k0, float k1, float sg) {
    const float e = __builtin_amdgcn_exp2f(a * __builtin_fmaf(a * a, k1, k0));
    return a * __builtin_amdgcn_rcpf(__builtin_fmaf(e, sg, sg));
}

DI f32x2 gelu_scaled2(f32x2 a, float k0, float k1, float sg) {
    const f32x2 z = a * __builtin_elementwise_fma(a * a, (f32x2){k1, k1}, (f32x2){k0, k0});
    const f32x2 e = {__builtin_amdgcn_exp2f(z[0]), __builtin_amdgcn_exp2f(z[1])};
    const f32x2 d = __builtin_elementwise_fma(e, (f32x2){sg, sg}, (f32x2){sg, sg});
    return a * (f32x2){__builtin_amdgcn_rcpf(d[0]), __builtin_amdgcn_rcpf(d[1])};
}
template <int MSTEP>
DI void dma_next(const void* sbase, unsigned& voff, unsigned& ldsaddr) {
    asm volatile("s_mov_b32 m0, %1\n\ts_add_u32 %1, %1, 1024\n\tglobal_load_lds_dwordx4 %0, %2\n\tv_add_u32 %0, %c3, %0" : "+v"(voff), "+s"(ldsaddr) : "s"(sbase), "i"(MSTEP) : "scc", "memory");
}
DI void dma_fix(const void* sbase, unsigned voff, unsigned& ldsaddr) {
    asm volatile("s_mov_b32 m0, %0\n\ts_add_u32 %0, %0, 1024\n\tglobal_load_lds_dwordx4 %1, %2" : "+s"(ldsaddr) : "v"(voff), "s"(sbase) : "scc", "memory");
}
template <int N>
DI void qkv_epilogue(f32x16 (&a)[2], int j, int tglob, const float (&cs)[2], float rstd, const bf16x8 tokf, char* ws, LAS char* lds, int tt, int l) {
    const int r = l & 31, h = l >> 5, type = j >> 4;
    const unsigned vl = l * 16;
    if (type < 2) {
#pragma unroll
        for (int n = 0; n < N; ++n) a[n] = MFMA(r1_feat(cs[n], 0u, h), tokf, a[n]);
        if (type == 0) {
            const float qs = 0.18898223650461361f * 1.4426950408889634f * rstd;
#pragma unroll
            for (int i = 0; i < 16; ++i)
#pragma unroll
                for (int n = 0; n < N; ++n) a[n][i] = (i >= 12 && h == 1) ? 0.f : a[n][i] * qs;
#pragma unroll
            for (int n = 0; n < N; ++n) { char* qd = ws + WS_Q + (size_t)(((j + n) & 15) * 64 + tglob) * 2048; gstore16_sc1(qd, vl, pack8(a[n], 0)); gstore16_sc1(qd + 1024, vl, pack8(a[n], 1)); }
        } else {
            float nk[2][2] = {{0.f, 0.f}, {0.f, 0.f}};
#pragma unroll
            for (int i = 0; i < 16; ++i)
#pragma unroll
                for (int n = 0; n < N; ++n) { const float kv = (i >= 12 && h == 1) ? 0.f : a[n][i] * rstd; nk[n][i & 1] = __builtin_fmaf(kv, kv, nk[n][i & 1]); a[n][i] = kv; }
#pragma unroll
            for (int n = 0; n < N; ++n) { if (h == 1) a[n][12] = 1.0f; char* kd = ws + WS_KV + (size_t)(((j + n) & 15) * 64 + tglob) * 4096; gstore16_sc1(kd, vl, pack8(a[n], 0)); gstore16_sc1(kd + 1024, vl, pack8(a[n], 1)); }
            float nm[2];
#pragma unroll
            for (int n = 0; n < N; ++n) nm[n] = row32_max(xhalf_sum(nk[n][0] + nk[n][1]));
#pragma unroll
            for (int n = 0; n < N; ++n) if (l == 0) __hip_atomic_store((__attribute__((address_space(1))) unsigned*)(ws + WS_KN + (((j + n) & 15) * 64 + tglob) * 4), __float_as_uint(nm[n]), __ATOMIC_RELAXED, __HIP_MEMORY_SCOPE_AGENT);
        }
    } else {
#pragma unroll
        for (int n = 0; n < N; ++n) a[n] = MFMA(tokf, r1_feat(cs[n], 0u, h), a[n]);
        const bool pad = r >= 28; const float pv1 = (r == 28) ? 1.0f : 0.f;
#pragma unroll
        for (int g = 0; g < 4; ++g) {
            const f32x4 rs = *(const LAS f32x4*)(lds + A_RS + (32 * tt + 8 * g + 4 * h) * 4);
#pragma unroll
            for (int e = 0; e < 4; ++e)
#pragma unroll
                for (int n = 0; n < N; ++n) a[n][4 * g + e] = pad ? pv1 : a[n][4 * g + e] * rs[e];
        }
#pragma unroll
        for (int n = 0; n < N; ++n) { char* vd = ws + WS_KV + (size_t)(((j + n) & 15) * 64 + tglob) * 4096 + 2048; gstore16_sc1(vd, vl, pack8(a[n], 0)); gstore16_sc1(vd + 1024, vl, pack8(a[n], 1)); }
    }
}

DI void phaseA(const char* __restrict__ x, const char* __restrict__ wqkv, const char* __restrict__ w1, const char* __restrict__ b1,
               char* __restrict__ ws, char* lds_generic, int Rb, int c, int hd, int qb, u64& tag, u32x4& q0, u32x4& q1, float& bound,
               const char* w2p, const char* wop) {
    LAS char* lds = (LAS char*)lds_generic;
    const unsigned lds0 = (unsigned)(uintptr_t)lds_generic;
    const int tid = threadIdx.x, l = tid & 63, w = rfl(tid >> 6) & 7, r = l & 31, h = l >> 5;
    const int tt = w & 3, half = w >> 2;
    const bool full = c < 12;
    const int f0 = full ? 4 * c : 24 + 2 * c;
    u64 e0; asm volatile("s_load_dwordx2 %0, %1, 0x0" : "=s"(e0) : "s"(uptr(ws + WS_EPOCH)) : "memory");
    unsigned vx[7], vw[3];
    const char* xb = (const char*)uptr(x + (size_t)(Rb * 128 + 16 * w) * 896);
    unsigned lxa = rfl(lds0 + A_XA + w * 7168);
#pragma unroll
    for (int m = 0; m < 7; ++m) { const unsigned i = 64 * m + l, row = (i * 2341u) >> 16, p = i - row * 28; vx[m] = row * 896 + (p ^ ((row >> 2) & 3u)) * 16; dma_fix(xb, vx[m], lxa); }
#pragma unroll
    for (int t = 0; t < 3; ++t) { const unsigned i = 64 * t + l, row = (i * 5462u) >> 16, ch = i - row * 12; vw[t] = row * 2688 + (ch < 11 ? ch : 10u) * 16; }
    const int kbw = w < 6 ? 2 * w : 6 + w;
    const char* wg = wqkv + 168 * c - (c & 1) * 8 + (size_t)kbw * 16 * 2688;
    const char* wa0 = (const char*)uptr(wg); const char* wa1 = (const char*)uptr(wg + (w < 6 ? 16 * 2688 : 0));
    unsigned lwa = rfl(lds0 + A_WA + kbw * 3072), lwa1 = rfl(lds0 + A_WA + kbw * 3072 + (w < 6 ? 3072 : 0));
    BARRIER();
#pragma unroll
    for (int t = 0; t < 3; ++t) dma_fix(wa0, vw[t], lwa);
#pragma unroll
    for (int t = 0; t < 3; ++t) if (w < 6) dma_fix(wa1, vw[t], lwa1);
    BARRIER();
    const char* xb2 = (const char*)uptr(xb + 448);
    unsigned lxb = rfl(lds0 + A_XB + w * 7168);
#pragma unroll
    for (int m = 0; m < 3; ++m) dma_fix(xb2, vx[m], lxb);
    if (w >= 5) {
        const unsigned t = (blockIdx.x >> 3) * 192u + (w - 5) * 64u + l, g = c >> 2;
        const bool isq = rfl(t) < 1792u;
        const unsigned u = isq ? t : t - 1792u, row = (isq ? 224u : 0u) + (u >> 3), col = (isq ? 672u * g : (g < 3u ? 1024u * g : 3072u)) + (u & 7u) * 128u, rs = isq ? 2688u : 3584u;
        asm volatile("s_mov_b32 m0, %2\n\ts_nop 3\n\tglobal_load_lds_dword %0, %1" :: "v"((row < 448u ? row : 447u) * rs + (col < rs - 4u ? col : rs - 4u)), "s"(uptr(isq ? wqkv : w1)), "s"(rfl(lds0 + A_RS)) : "memory");
    }
    const int troff = (8 * h + ((l & 15) >> 2)) * 64 + ((l >> 4) & 1) * 32 + (l & 3) * 8;
    const int troffq = (8 * h + ((l & 15) >> 2)) * 192 + ((l >> 4) & 1) * 32 + (l & 3) * 8 + (c & 1) * 8;
    const LAS char* xrd0 = lds + (32 * tt + r) * 448 + ((h ^ (r >> 2)) & 3) * 16;
    const LAS char* xrd1 = lds + (32 * tt + r) * 448 + (((2 + h) ^ (r >> 2)) & 3) * 16;
    bf16x8 xf[28];
    float p0 = 0.f, p1 = 0.f, p2 = 0.f, p3 = 0.f;
#define USE7(cf) asm volatile("" :: "v"(cf[0]), "v"(cf[1]), "v"(cf[2]), "v"(cf[3]), "v"(cf[4]), "v"(cf[5]), "v"(cf[6]) : "memory")
#define SUMSQ(S0, D, TAG) do { _Pragma("unroll") for (int s = 0; s < 14; ++s) { const u32x4 xx = asu4(xf[(S0) + s]); const unsigned u0 = xx[D], u1 = xx[(D) + 1]; \
        if (s & 1) { p2 = dot2(u0, u0, p2); p3 = dot2(u1, u1, p3); } else { p0 = dot2(u0, u0, p0); p1 = dot2(u1, u1, p1); } } \
        asm volatile("; " TAG : "+v"(p0), "+v"(p1), "+v"(p2), "+v"(p3)); } while (0)
    if (w >= 5) WAIT_VM(4); else WAIT_VM(3);
#define PATCH_ONES(AREA) do { if (l < (w < 6 ? 32 : 16)) *(LAS unsigned short*)(lds + (AREA) + (16 * kbw + l) * 192 + 168 + (c & 1) * 8) = 0x3f80; } while (0)
    PATCH_ONES(A_WA);
    asm volatile("s_waitcnt lgkmcnt(0)" : "+s"(e0) :: "memory");
    tag = e0 + 0x9E3779B97F4A7C15ull;
    BARRIER();
    f32x16 a0 = {}, a1 = {};
    unsigned ones = 0x3f803f80u; PIN(ones);
    const int j0 = 3 * c + 2 * half;
    const bool v0 = j0 >= 32, v1 = j0 + 1 >= 32;
#define WFRAG(P, s) trfrag((P) + (s) * 3072, (P) + (s) * 3072 + 768)
#define MM(ACC, ISV, WF, XF) do { if (ISV) ACC = MFMA(XF, WF, ACC); else ACC = MFMA(WF, XF, ACC); } while (0)
#define XFRAG(S0, s) xf[(S0) + (s)] = ldsfrag((((s) & 1) ? xrd1 : xrd0) + ((S0) ? A_XB : A_XA) + ((s) >> 1) * 64)
#define TWO_STEPS(S0, PB, ISV0, ISV1, HOOK) do { \
        bf16x8 wq[3], wk[3]; \
        wk[0] = WFRAG(PB + 56, 0); wq[0] = WFRAG(PB, 0); XFRAG(S0, 0); wk[1] = WFRAG(PB + 56, 1); wq[1] = WFRAG(PB, 1); XFRAG(S0, 1); XFRAG(S0, 2); XFRAG(S0, 3); \
        _Pragma("unroll") for (int s = 0; s < 14; ++s) { \
            if (s + 2 < 14) { wk[(s + 2) % 3] = WFRAG(PB + 56, s + 2); wq[(s + 2) % 3] = WFRAG(PB, s + 2); } \
            if (s < 5) { XFRAG(S0, (4 + 2 * s) % 14); XFRAG(S0, (5 + 2 * s) % 14); } \
            SBAR(); \
            MM(a0, ISV0, wq[s % 3], xf[S0 + s]); \
            MM(a1, ISV1, wk[s % 3], xf[S0 + s]); \
            if (s == 5) { USE7((xf + S0)); USE7((xf + S0 + 7)); BARRIER(); }        \
            HOOK(s); \
            SBAR(); \
        } } while (0)
#define ONE_STEPS(S0, PB, ISV0, HOOK) do { \
        bf16x8 wv[4]; \
        wv[0] = WFRAG(PB, 0); XFRAG(S0, 0); wv[1] = WFRAG(PB, 1); XFRAG(S0, 1); wv[2] = WFRAG(PB, 2); XFRAG(S0, 2); XFRAG(S0, 3); \
        _Pragma("unroll") for (int s = 0; s < 14; ++s) { \
            if (s + 3 < 14) wv[(s + 3) & 3] = WFRAG(PB, s + 3); \
            if (s < 5) { XFRAG(S0, (4 + 2 * s) % 14); XFRAG(S0, (5 + 2 * s) % 14); } \
            SBAR(); \
            MM(a0, ISV0, wv[s & 3], xf[S0 + s]); \
            if (s == 5) { USE7((xf + S0)); USE7((xf + S0 + 7)); BARRIER(); }        \
            HOOK(s); \
            SBAR(); \
        } } while (0)
#define QKV_HALF(S0, AREA, HOOK) do { \
        const LAS char* pb = lds + (AREA) + troffq + half * 112; \
        if (half == 0) { if (!v1) TWO_STEPS(S0, pb, false, false, HOOK); else if (!v0) TWO_STEPS(S0, pb, false, true, HOOK); else TWO_STEPS(S0, pb, true, true, HOOK); } \
        else { if (!v0) ONE_STEPS(S0, pb, false, HOOK); else ONE_STEPS(S0, pb, true, HOOK); } } while (0)
    const char* wb0 = (const char*)uptr(wa0 + 14 * 16 * 2688); const char* wb1 = (const char*)uptr(wa1 + 14 * 16 * 2688);
    unsigned lwb = rfl(lds0 + A_XA + kbw * 3072), lwb1 = rfl(lds0 + A_XA + kbw * 3072 + (w < 6 ? 3072 : 0));
#define HOOK_WB(s) do { if ((s) < 4) dma_fix(xb2, vx[(3 + (s)) % 7], lxb); if ((s) >= 5 && (s) < 8) dma_fix(wb0, vw[((s) + 1) % 3], lwb); if ((s) >= 8 && (s) < 11 && w < 6) dma_fix(wb1, vw[((s) + 1) % 3], lwb1); \
        if ((s) == 12 && w == 7) asm volatile("s_mov_b32 m0, %2\n\ts_nop 3\n\tglobal_load_lds_dword %0, %1" :: "v"((full ? l : (l & 31)) * 4), "s"(uptr(b1 + f0 * 64)), "s"(rfl(lds0 + A_B1)) : "memory"); } while (0)
    QKV_HALF(0, A_WA, HOOK_WB);
    WAIT_VM(0); PATCH_ONES(A_XA); WAIT_LGKM0(); BARRIER();
    const unsigned vo1 = (l >> 2) * 3584 + (l & 3) * 16;
    const char* w1p = (const char*)uptr(w1 + (f0 + half) * 64);
    unsigned vw1 = vo1 + tt * 7 * 16 * 3584, lw1 = rfl(lds0 + A_XB + half * A_SLOT + tt * 7168);
#define HOOK_W01(s) do { if ((s) >= 5 && (s) < 12) dma_next<16 * 3584>(w1p, vw1, lw1); } while (0)
    QKV_HALF(14, A_XA, HOOK_W01);
#define PIN7(cf) asm volatile("" : "+v"(cf[0]), "+v"(cf[1]), "+v"(cf[2]), "+v"(cf[3]), "+v"(cf[4]), "+v"(cf[5]), "+v"(cf[6]))
#define CS_FRAG(F) do { const u32x4 u = asu4(F); c0 = dot2(u[0], ones, c0); c1 = dot2(u[1], ones, c1); c2 = dot2(u[2], ones, c2); c3 = dot2(u[3], ones, c3); } while (0)
#define CS_STORE(T) do { const float cp = xhalf_sum((c0 + c1) + (c2 + c3)); if (h == 0) *(LAS float*)(lds + A_CS + (T) * 512 + r * 16 + tt * 4) = cp; } while (0)
    {
        const LAS char* c_lo = lds + A_WA + troffq + half * 112 + tt * 3072;
        const LAS char* c_3 = lds + troffq + half * 112 + (tt < 2 ? A_WA + (12 + tt) * 3072 : A_XA + (tt - 2) * 3072);
        const LAS char* c_hi = lds + A_XA + troffq + half * 112 + (tt + 2) * 3072;
#define CS_QKV(OFF, T) do { bf16x8 cf[7]; \
            cf[0] = WFRAG(c_lo + (OFF), 0); cf[1] = WFRAG(c_lo + (OFF), 4); cf[2] = WFRAG(c_lo + (OFF), 8); cf[3] = WFRAG(c_3 + (OFF), 0); \
            cf[4] = WFRAG(c_hi + (OFF), 0); cf[5] = WFRAG(c_hi + (OFF), 4); cf[6] = WFRAG(c_hi + (OFF), 8); PIN7(cf); \
            float c0 = 0.f, c1 = 0.f, c2 = 0.f, c3 = 0.f; _Pragma("unroll") for (int i = 0; i < 7; ++i) CS_FRAG(cf[i]); CS_STORE(T); } while (0)
        if (half == 0) { SUMSQ(0, 0, "a"); SUMSQ(14, 0, "c"); } else { SUMSQ(0, 2, "b"); SUMSQ(14, 2, "d"); }
        CS_QKV(0, 2 * half);
        if (half == 0) CS_QKV(56, 1);
        const float ps = xhalf_sum((p0 + p1) + (p2 + p3));
        if (h == 0) *(LAS float*)(lds + A_SQ + half * 512 + (32 * tt + r) * 4) = ps;
        if (half == 1) {
            if (!v0) { if (h == 1) *(LAS float*)(lds + A_SX + (32 * tt + r) * 4) = a0[12]; }
            else if (r == 28) {
#pragma unroll
                for (int g = 0; g < 4; ++g) *(LAS f32x4*)(lds + A_SX + (32 * tt + 8 * g + 4 * h) * 4) = (f32x4){a0[4 * g], a0[4 * g + 1], a0[4 * g + 2], a0[4 * g + 3]};
            }
        }
    }
    WAIT_LGKM0(); BARRIER();
    const float mean = *(const LAS float*)(lds + A_SX + (32 * tt + r) * 4) * (1.0f / 448.0f);
    const float var = __builtin_fmaxf((*(const LAS float*)(lds + A_SQ + (32 * tt + r) * 4) + *(const LAS float*)(lds + A_SQ + 512 + (32 * tt + r) * 4)) * (1.0f / 448.0f) - mean * mean, 0.f);
    const float rstd = __builtin_amdgcn_rsqf(var + 1e-5f), sigma = (var + 1e-5f) * rstd;
    const bf16x8 tokf = r1_tok(-mean, sigma, h);
    const int tglob = Rb * 4 + tt;
    const unsigned vl = l * 16;
    if (h == 0) *(LAS float*)(lds + A_RS + (32 * tt + r) * 4) = rstd;
#define CS_LOAD(T) ({ const f32x4 cp_ = *(const LAS f32x4*)(lds + A_CS + (T) * 512 + r * 16); (cp_[0] + cp_[1]) + (cp_[2] + cp_[3]); })
    { const float csa = CS_LOAD(2 * half), csb = CS_LOAD(1);
      WAIT_LGKM0();
      f32x16 aa[2] = {a0, a1}; const float cc[2] = {csa, csb};
      if (half == 1) qkv_epilogue<1>(aa, j0, tglob, cc, rstd, tokf, ws, lds, tt, l);
      else if (j0 != 15) qkv_epilogue<2>(aa, j0, tglob, cc, rstd, tokf, ws, lds, tt, l);
      else { qkv_epilogue<1>(aa, j0, tglob, cc, rstd, tokf, ws, lds, tt, l);
             f32x16 ab[2] = {a1, a1}; const float cd[2] = {csb, csb}; qkv_epilogue<1>(ab, j0 + 1, tglob, cd, rstd, tokf, ws, lds, tt, l); } }
    const char* hd0 = ws + WS_H + ((size_t)tglob * 56 + f0 + half) * 2048;
    const unsigned vfl = w != 0 ? 14336u + w * 128u : (unsigned)WS_FKV + (l < 16 ? ((16 + hd) / 3) * 256 + l * 8 : l < 32 ? ((32 + hd) / 3) * 256 + (l - 16) * 8 : (hd / 3) * 256 + qb * 8);
    u32x2 fl;
#define KV_POLL() asm volatile("global_load_dwordx2 %0, %1, %2 sc1" : "=v"(fl) : "v"(vfl), "s"(uptr(ws)) : "memory")
#define KV_CHECK() do {   \
        for (unsigned spins = 0; spins < (1u << 20) && !__all((((u64)fl[1] << 32) | fl[0]) == tag); ++spins) { __builtin_amdgcn_s_sleep(2); KV_POLL(); asm volatile("s_waitcnt vmcnt(0)" : "+v"(fl) :: "memory"); } } while (0)
    const int qt = tt, kh = half;
    const char* kvsrc = ws + WS_KV + (size_t)hd * 262144 + (size_t)kh * 131072 + qt * 4096;
    const unsigned kvdst = lds0 + kh * 16384 + qt * 4096;
    unsigned knw;
#define ATTN_REQ_Q() do { \
        dma1_sc1(ws + WS_Q + (size_t)(hd * 64 + qb * 4 + qt) * 2048 + kh * 1024, vl, lds0 + A_QT + qt * 2048 + kh * 1024); \
        if (w == 0) { asm volatile("s_mov_b32 m0, %2\n\ts_nop 3\n\tglobal_load_lds_dword %0, %1 sc1" :: "v"(l * 4), "s"(uptr(ws + WS_KN + hd * 256)), "s"(rfl(lds0 + A_KN)) : "memory"); } \
          \
        if (w == 1 || w == 2) { asm volatile("s_mov_b32 m0, %2\n\ts_nop 3\n\tglobal_load_lds_dword %0, %1 sc1" :: "v"(l * 128), "s"(uptr(ws + WS_KV + (size_t)hd * 262144 + qb * 16384 + (w - 1) * 8192)), "s"(rfl(lds0 + A_KN + 256)) : "memory"); } } while (0)
#define ATTN_GET_Q() do { q0 = *(const LAS u32x4*)(lds + A_QT + qt * 2048 + vl); q1 = *(const LAS u32x4*)(lds + A_QT + qt * 2048 + 1024 + vl); knw = *(const LAS unsigned*)(lds + A_KN + l * 4); } while (0)
#define W2_TOUCH() do { const int u_ = (blockIdx.x >> 3) * 8 + w; const char* tp_ = u_ < 196 ? w2p + (size_t)u_ * 8192 : u_ < 245 ? wop + (size_t)(u_ - 196) * 8192 : w2p; \
        asm volatile("s_mov_b32 m0, %2\n\ts_nop 3\n\tglobal_load_lds_dword %0, %1" :: "v"(l * 128), "s"(uptr(tp_)), "s"(rfl(lds0 + A_KN + 512)) : "memory"); } while (0)
    if (tid == 64) *(LAS unsigned*)(lds + L_CTL) = 0u;
    if (half == 0) WAIT_VM(4); else WAIT_VM(2);
    BARRIER();
    const char* w1q = (const char*)uptr(w1p + (full ? 128 : 0));
    vw1 = vo1 + tt * 7 * 16 * 3584; lw1 = rfl(lds0 + (half == 0 ? A_WA : A_XA) + tt * 7168);
    f32x16 b0 = {};
    const float gk0 = (-2.0f * 0.7978845608028654f * 1.4426950408889634f) * rstd, gk1 = gk0 * 0.044715f * rstd * rstd;
#define W1FRAG(P, s) trfrag((P) + (s) * 1024, (P) + (s) * 1024 + 256)
#define CS_PART(PW, T) do { bf16x8 cf[7]; _Pragma("unroll") for (int i = 0; i < 7; ++i) cf[i] = W1FRAG(PW + tt * 1024, 4 * i); PIN7(cf); float c0 = 0.f, c1 = 0.f, c2 = 0.f, c3 = 0.f; _Pragma("unroll") for (int i = 0; i < 7; ++i) CS_FRAG(cf[i]); CS_STORE(T); } while (0)
#define BIAS16(u) ((unsigned)*(const LAS unsigned short*)(lds + A_B1 + ((2 * (u) + half) * 32 + r) * 2))
#define GELU_EL(ACC, i) ACC[i] = gelu_scaled(ACC[i], gk0, gk1, sigma)
    {
        const LAS char* pw = lds + A_XB + half * A_SLOT + troff;
        bf16x8 wf[4];
        wf[0] = W1FRAG(pw, 0); wf[1] = W1FRAG(pw, 1); wf[2] = W1FRAG(pw, 2);
#pragma unroll
        for (int s = 0; s < 28; ++s) {
            if (s + 3 < 28) wf[(s + 3) & 3] = W1FRAG(pw, s + 3);
            SBAR();
            b0 = MFMA(wf[s & 3], xf[s], b0);
            if ((s & 1) == 0 && s < 14 && full) dma_next<16 * 3584>(w1q, vw1, lw1);
            if (s == 9) { if (full) WAIT_VM(5); else WAIT_VM(0); BARRIER(); if (tid == 0) flag_store(ws, WS_FKV + c * 256 + Rb * 8, tag); }
            SBAR();
        }
        CS_PART(pw, 3 + half);
    }
    asm volatile("s_waitcnt vmcnt(0) lgkmcnt(0)" ::: "memory");
    BARRIER();
    b0 = MFMA(r1_feat(CS_LOAD(3 + half), BIAS16(0), h), tokf, b0);
    f32x16 bt;
    if (full) {
        KV_POLL();
        W2_TOUCH();
        f32x16 b1v = {};
        const LAS char* pw = lds + (half == 0 ? A_WA : A_XA) + troff;
        bf16x8 wf[4];
        wf[0] = W1FRAG(pw, 0); wf[1] = W1FRAG(pw, 1); wf[2] = W1FRAG(pw, 2);
#pragma unroll
        for (int s2 = 0; s2 < 14; ++s2) {
            const int s = 2 * s2;
            if (s + 3 < 28) wf[(s + 3) & 3] = W1FRAG(pw, s + 3);
            SBAR();
            b1v = MFMA(wf[s & 3], xf[s], b1v);
            if (s2 == 9) { asm volatile("s_waitcnt vmcnt(2)" : "+v"(fl) :: "memory"); if (w == 0) KV_CHECK(); BARRIER(); ATTN_REQ_Q(); }
            SBAR();
            if (s + 4 < 28) wf[(s + 4) & 3] = W1FRAG(pw, s + 4);
            SBAR();
            b1v = MFMA(wf[(s + 1) & 3], xf[s + 1], b1v);
            if (s2 >= 1 && s2 < 9) { const int e = 2 * (s2 - 1); GELU_EL(b0, e); GELU_EL(b0, e + 1); PIN(b0[e]); PIN(b0[e + 1]); }
            if (s2 == 5) gstore16_sc1((char*)hd0, vl, pack8(b0, 0));
            if (s2 == 10) gstore16_sc1((char*)hd0 + 1024, vl, pack8(b0, 1));
            SBAR();
        }
        CS_PART(pw, 5 + half);
        WAIT_LGKM0(); WAIT_VM(1); BARRIER();
        bt = MFMA(r1_feat(CS_LOAD(5 + half), BIAS16(1), h), tokf, b1v);
        hd0 += 4096;
    } else {
        KV_POLL();
        W2_TOUCH();
        asm volatile("s_waitcnt vmcnt(1)" : "+v"(fl) :: "memory");
        if (w == 0) KV_CHECK();
        BARRIER();
        ATTN_REQ_Q();
        WAIT_VM(0); BARRIER();
        bt = b0;
    }
    float kn = 0.f, qn = 0.f;
    const char* ks0 = (const char*)uptr(kvsrc); const char* ks1 = (const char*)uptr(kvsrc + 16384);
    const unsigned kd0 = rfl(kvdst + 32768), kd1 = rfl(kvdst + 65536);
#pragma unroll
    for (int i = 0; i < 4; ++i) {
        if (i == 0) set_m0(kd0); if (i == 2) set_m0(kd1);
        if ((i & 1) == 0) { dma_off_sc1<0>(i < 2 ? ks0 : ks1, vl); dma_off_sc1<1024>(i < 2 ? ks0 : ks1, vl); } else { dma_off_sc1<2048>(i < 2 ? ks0 : ks1, vl); dma_off_sc1<3072>(i < 2 ? ks0 : ks1, vl); }
        if (i == 0) ATTN_GET_Q();
        if (i == 1) { kn = xhalf_max(row32_max(__uint_as_float(knw))); PIN(kn); }
        if (i == 2) { float qa = 0.f, qb2 = 0.f; _Pragma("unroll") for (int e = 0; e < 4; ++e) { qa = dot2(q0[e], q0[e], qa); qb2 = dot2(q1[e], q1[e], qb2); } qn = xhalf_sum(qa + qb2); PIN(qn); }
        if (i == 3) { bound = __builtin_amdgcn_sqrtf(qn * kn) * 1.02f + 1e-3f; if (__any(!(bound <= 48.0f)) && l == 0) *(LAS unsigned*)(lds + L_CTL) = 1u; }
        { const f32x2 g2 = gelu_scaled2((f32x2){bt[4 * i], bt[4 * i + 1]}, gk0, gk1, sigma), g3 = gelu_scaled2((f32x2){bt[4 * i + 2], bt[4 * i + 3]}, gk0, gk1, sigma);
          bt[4 * i] = g2[0]; bt[4 * i + 1] = g2[1]; bt[4 * i + 2] = g3[0]; bt[4 * i + 3] = g3[1]; PIN(bt[4 * i]); PIN(bt[4 * i + 1]); PIN(bt[4 * i + 2]); PIN(bt[4 * i + 3]); }
        SBAR();
    }
    gstore16_sc1((char*)hd0, vl, pack8(bt, 0)); gstore16_sc1((char*)hd0 + 1024, vl, pack8(bt, 1));
}

constexpr int B_RED = 0;
constexpr int C_P0 = 98304;
#define EX2(v) __builtin_amdgcn_exp2f(v)
DI void attn_safe(f32x16& o, const bf16x8 q0, const bf16x8 q1, const char* src, unsigned dst, unsigned vo, const LAS char* rb, int l) {
    float m = -1e30f;
    const f32x16 zero = {};
    dma4_sc1(src + 2 * 16384, vo, dst);
    for (int i = 0; i < 8; ++i) {
        const LAS char* sb = rb + ((i + 1) % 3) * 32768;
#pragma unroll 1
        for (int t = 0; t < 4; ++t) {
            f32x16 sc = MFMA(ldsfrag(sb + t * 4096), q0, zero); sc = MFMA(ldsfrag(sb + t * 4096 + 1024), q1, sc);
            float mx = sc[0];
#pragma unroll
            for (int e = 1; e < 16; ++e) mx = __builtin_fmaxf(mx, sc[e]);
            mx = xhalf_max(mx);
            const float mn = __builtin_fmaxf(m, mx), al = EX2(m - mn); m = mn;
#pragma unroll
            for (int e = 0; e < 16; ++e) { o[e] *= al; sc[e] = EX2(sc[e] - mn); }
            o = MFMA(ldsfrag(sb + t * 4096 + 2048), asbf8(pack8(sc, 0)), o); o = MFMA(ldsfrag(sb + t * 4096 + 3072), asbf8(pack8(sc, 1)), o);
        }
        WAIT_LGKM0(); WAIT_VM(0); BARRIER();
        if (i + 3 < 8) dma4_sc1(src + (size_t)(i + 3) * 16384, vo, dst + ((i + 1) % 3) * 32768);
    }
    o[13] = (l >= 32) ? m : o[13];
}

DI void phaseBC(const char* __restrict__ x, const char* __restrict__ wo, const char* __restrict__ w2, const char* __restrict__ b2,
                char* __restrict__ ws, char* __restrict__ out, char* lds_generic, int hd, int qb, int rb, int ng, bool doC, bool bump,
                const char* hflag, bool hpub, u64 tag, u32x4 q0, u32x4 q1, float bound) {
    LAS char* lds = (LAS char*)lds_generic;
    const unsigned lds0 = (unsigned)(uintptr_t)lds_generic;
    const int tid = threadIdx.x, l = tid & 63, w = rfl(tid >> 6) & 7, h = l >> 5;
    const int qt = w & 3, kh = w >> 2, tglob = qb * 4 + qt;
    const unsigned vl = l * 16;
    const char* kvsrc = ws + WS_KV + (size_t)hd * 262144 + (size_t)kh * 131072 + qt * 4096;
    const unsigned kvdst = lds0 + kh * 16384 + qt * 4096;
    const int tt = w & 1, kq = w >> 1, tC = rb * 2 + tt;
    const unsigned vw2 = (l >> 3) * 896 + (((l & 7) ^ (((l >> 4) & 1) << 2)) * 16);
    const char* w2src = w2 + ng * 128 + (size_t)(448 * kq + 32 * tt) * 896;
    const char* hsrc = ws + WS_H + ((size_t)tC * 56 + 14 * kq) * 2048;
    const char* osrc = ws + WS_O + ((size_t)tC * 16 + 4 * kq) * 2048;
    const unsigned wdst = lds0 + kq * 8192 + tt * 4096;
    const unsigned ad0 = lds0 + kh * 16384 + vl, ad1 = ad0 + 65536;
    const char* sk1 = (const char*)uptr(kvsrc + 16384);
    const char* sh = (const char*)uptr(hsrc);
    const char* sw = (const char*)uptr(w2src);
    unsigned sd = rfl(wdst + C_P0);
    const char* sf = (const char*)uptr(ws + WS_FH + (rb >> 1) * 256);
    const unsigned vf = (l < 16 ? l : 0) * 8;
    unsigned vw2_ = vw2, vf_ = vf, ad0_ = ad0, ad1_ = ad1;
    asm volatile("" : "+v"(vw2_), "+v"(vf_), "+v"(ad0_), "+v"(ad1_), "+s"(sk1), "+s"(sh), "+s"(sw), "+s"(sd), "+s"(sf));
    WAIT_LGKM0(); WAIT_VM(2); BARRIER();
    const bool safe = rfl(*(const LAS unsigned*)(lds + L_CTL)) != 0u;
    f32x16 o;
    typedef unsigned u32x16 __attribute__((ext_vector_type(16)));
    u32x16 hq[7];
#define HF(n) ((u32x4){hq[(n) >> 2][4 * ((n) & 3)], hq[(n) >> 2][4 * ((n) & 3) + 1], hq[(n) >> 2][4 * ((n) & 3) + 2], hq[(n) >> 2][4 * ((n) & 3) + 3]})
    if (!safe) {
        if (h == 1) q1[2] = cvtpk(-bound, 0.f);
        asm volatile(ATTN_FAST_ASM
                     : "={v[0:15]}"(o), "+{s[40:41]}"(sk1), "+{s[44:45]}"(sh), "+{s[46:47]}"(sw), "+{s48}"(sd),
                       "={v[100:115]}"(hq[0]), "={v[116:131]}"(hq[1]), "={v[132:147]}"(hq[2]), "={v[148:163]}"(hq[3]), "={v[164:179]}"(hq[4]), "={v[180:195]}"(hq[5]), "={v[196:211]}"(hq[6])
                     : "{v[48:51]}"(q0), "{v[52:55]}"(q1), "{v96}"(ad0_), "{v97}"(ad1_), "{v98}"(vl), "{s42}"(rfl(kvdst)),
                       "{v215}"(vw2_), "{v217}"(vf_), "{s[50:51]}"(sf), "{s[52:53]}"(tag), "{s[56:57]}"(uptr(hflag)), "{s58}"(rfl((hpub && w == 0) ? 1u : 0u)), "{s59}"(w)
                     : ATTN_FAST_CLOBBERS);
    } else {
        WAIT_VM(0); BARRIER();
        if (hpub && tid == 0) __hip_atomic_store((gu64*)hflag, tag, __ATOMIC_RELAXED, __HIP_MEMORY_SCOPE_AGENT);
        o = (f32x16){};
        attn_safe(o, asbf8(q0), asbf8(q1), kvsrc, kvdst, vl, lds + kh * 16384 + vl, l);
        u32x4 hf[28];
        flag_wait(ws, WS_FH + (rb >> 1) * 256, 16, tag, l);
#pragma unroll
        for (int n = 0; n < 28; ++n) hf[n] = gload16_sc1(hsrc + n * 1024, vl);
        dma_run<4, 7168, 1024>(w2src, vw2, wdst + C_P0); dma_run<4, 7168, 1024>(w2src + 64 * 896, vw2, wdst + C_P0 + 32768);
        asm volatile("s_waitcnt vmcnt(0)" : "+v"(hf[0]), "+v"(hf[1]), "+v"(hf[2]), "+v"(hf[3]), "+v"(hf[4]), "+v"(hf[5]), "+v"(hf[6]), "+v"(hf[7]), "+v"(hf[8]), "+v"(hf[9]), "+v"(hf[10]), "+v"(hf[11]), "+v"(hf[12]), "+v"(hf[13]) :: "memory");
        asm volatile("" : "+v"(hf[14]), "+v"(hf[15]), "+v"(hf[16]), "+v"(hf[17]), "+v"(hf[18]), "+v"(hf[19]), "+v"(hf[20]), "+v"(hf[21]), "+v"(hf[22]), "+v"(hf[23]), "+v"(hf[24]), "+v"(hf[25]), "+v"(hf[26]), "+v"(hf[27]));
#pragma unroll
        for (int n = 0; n < 7; ++n)
#pragma unroll
            for (int e = 0; e < 16; ++e) hq[n][e] = hf[4 * n + (e >> 2)][e & 3];
    }
    LAS char* red = lds + B_RED + qt * 4096 + vl;
    if (kh == 0) {
        *(LAS f32x4*)(red + 2048) = (f32x4){o[8], o[9], o[10], o[11]}; *(LAS f32x4*)(red + 3072) = (f32x4){o[12], o[13], o[14], o[15]};
    } else {
        *(LAS f32x4*)(red) = (f32x4){o[0], o[1], o[2], o[3]}; *(LAS f32x4*)(red + 1024) = (f32x4){o[4], o[5], o[6], o[7]};
        *(LAS f32x2*)(lds + B_RED + 16384 + qt * 512 + l * 8) = (f32x2){o[12], o[13]};
    }
    WAIT_LGKM0(); BARRIER();
    {
        float v[8]; float lo12, lo13, ot12, ot13;
        if (kh == 0) {
            const f32x4 t0 = *(const LAS f32x4*)(red), t1 = *(const LAS f32x4*)(red + 1024); const f32x2 t2 = *(const LAS f32x2*)(lds + B_RED + 16384 + qt * 512 + l * 8);
            lo12 = o[12]; lo13 = o[13]; ot12 = t2[0]; ot13 = t2[1];
            float fa = 1.f, fb = 1.f;
            if (safe) { const float ma = swap32_hi(lo13), mb = swap32_hi(ot13), mn = __builtin_fmaxf(ma, mb); fa = EX2(ma - mn); fb = EX2(mb - mn); }
            const float inv = __builtin_amdgcn_rcpf(swap32_hi(lo12 * fa + ot12 * fb)); fa *= inv; fb *= inv;
#pragma unroll
            for (int e = 0; e < 4; ++e) { v[e] = o[e] * fa + t0[e] * fb; v[4 + e] = o[4 + e] * fa + t1[e] * fb; }
        } else {
            const f32x4 t0 = *(const LAS f32x4*)(red + 2048), t1 = *(const LAS f32x4*)(red + 3072);
            lo12 = o[12]; lo13 = o[13]; ot12 = t1[0]; ot13 = t1[1];
            float fa = 1.f, fb = 1.f;
            if (safe) { const float ma = swap32_hi(lo13), mb = swap32_hi(ot13), mn = __builtin_fmaxf(ma, mb); fa = EX2(ma - mn); fb = EX2(mb - mn); }
            const float inv = __builtin_amdgcn_rcpf(swap32_hi(lo12 * fa + ot12 * fb)); fa *= inv; fb *= inv;
#pragma unroll
            for (int e = 0; e < 4; ++e) { v[e] = o[8 + e] * fa + t0[e] * fb; v[4 + e] = (h == 1) ? 0.f : o[12 + e] * fa + t1[e] * fb; }
        }
        const u32x4 pk = {cvtpk(v[0], v[1]), cvtpk(v[2], v[3]), cvtpk(v[4], v[5]), cvtpk(v[6], v[7])};
        gstore16_sc1(ws + WS_O + ((size_t)tglob * 16 + hd) * 2048 + kh * 1024, vl, pk);
    }
    if (!doC) {
        WAIT_VM(0); BARRIER();
        if (tid == 0) flag_store(ws, WS_FO + qb * 256 + hd * 8, tag);
        return;
    }
    auto slot_of = [](int s) { return s == 0 || s == 5 ? C_P0 : s == 1 || s == 6 ? C_P0 + 32768 : s == 2 || s == 7 ? 32768 : s == 3 || s == 8 ? 65536 : 0; };
    unsigned vrq = vw2 + 2 * 64 * 896, lrq;
    const char* w2u = (const char*)uptr(w2src);
#define W2REQ(s, e) do { if ((e) == 0) lrq = rfl(wdst + slot_of(s)); if ((e) < 3) dma_next<8 * 896>(w2u, vrq, lrq); else dma_next<40 * 896>(w2u, vrq, lrq); } while (0)
    const int hA = 4 * kq + tt, hB = hA + 2;
    const char* woA = (const char*)uptr(wo + ng * 128 + (size_t)(28 * hA) * 896); const char* woB = (const char*)uptr(wo + ng * 128 + (size_t)(hB < 15 ? 28 * hB : 416) * 896);
    unsigned vro;
#define WOREQ(s, e) do { if ((e) == 0) { lrq = rfl(wdst + slot_of(s)); vro = vw2; } dma_next<8 * 896>((s) == 7 ? woA : woB, vro, lrq); } while (0)
    const int tok = tid >> 3, ch = tid & 7;
    const size_t goff = (size_t)(rb * 64 + tok) * 896 + ng * 128 + ch * 16;
    const int q = (l & 15) >> 2, p = l & 3, cb = (l >> 4) & 1;
    const int tr0 = (4 * h + q) * 128 + (((2 * cb + (p >> 1)) ^ ((q >> 1) << 2)) * 16) + (p & 1) * 8, tr1 = tr0 ^ 64;
    f32x16 a0 = {}, a1 = {};
    u32x4 of[8];
#define C_STAGE(s, HOOK) do { const LAS char* wb = lds + slot_of(s) + kq * 8192; \
        bf16x8 wa[2], wc[2]; \
        wc[0] = trfrag(wb + tr1, wb + tr1 + 1024); wa[0] = trfrag(wb + tr0, wb + tr0 + 1024); \
        _Pragma("unroll") for (int i = 0; i < 4; ++i) { \
            if (i + 1 < 4) { const LAS char* wp = wb + ((i + 1) >> 1) * 4096 + ((i + 1) & 1) * 2048; wc[(i + 1) & 1] = trfrag(wp + tr1, wp + tr1 + 1024); wa[(i + 1) & 1] = trfrag(wp + tr0, wp + tr0 + 1024); } \
            SBAR(); \
            a0 = MFMA(wa[i & 1], asbf8(HF(4 * (s) + i)), a0); \
            a1 = MFMA(wc[i & 1], asbf8(HF(4 * (s) + i)), a1); \
            HOOK(i); \
            SBAR(); \
        } } while (0)
#define HOOK0(i) W2REQ(2, i)
#define HOOK1(i) W2REQ(3, i)
    C_STAGE(0, HOOK0);
    C_STAGE(1, HOOK1);
    WAIT_LGKM0(); WAIT_VM(4); BARRIER();
    if (tid == 0) flag_store(ws, WS_FO + qb * 256 + hd * 8, tag);
#define HOOK2(i) do { W2REQ(4 + ((i) >> 1), (2 * (i)) & 3); W2REQ(4 + ((i) >> 1), (2 * (i) + 1) & 3); } while (0)
    C_STAGE(2, HOOK2);
    WAIT_LGKM0(); if (w == 0) WAIT_VM(9); else WAIT_VM(8); BARRIER();
#define HOOK3(i) do { if ((i) < 2) { W2REQ(6, 2 * (i)); W2REQ(6, 2 * (i) + 1); } else { WOREQ(7, 2 * (i) - 4); WOREQ(7, 2 * (i) - 3); } } while (0)
    C_STAGE(3, HOOK3);
    WAIT_LGKM0(); WAIT_VM(12); BARRIER();
    u32x2 fo; const unsigned vfo = w != 0 ? 14336u + w * 128u : (unsigned)(WS_FO + (rb >> 1) * 256) + (l < 16 ? l : 0) * 8;
#define OPOLL() asm volatile("global_load_dwordx2 %0, %1, %2 sc1" : "=v"(fo) : "v"(vfo), "s"(uptr(ws)) : "memory")
    OPOLL();
    u32x4 xv, bv;
#define HOOK4(i) do { WOREQ(8, i); if ((i) == 3) { xv = gload16(x + (size_t)rb * 64 * 896 + ng * 128, (unsigned)(tok * 896 + ch * 16)); bv = gload16(b2 + ng * 128, (unsigned)(ch * 16)); } } while (0)
    C_STAGE(4, HOOK4);
    asm volatile("s_waitcnt vmcnt(6) lgkmcnt(0)" : "+v"(fo) :: "memory");
    if (w == 0) for (unsigned spins = 0; spins < (1u << 20) && !__all((((u64)fo[1] << 32) | fo[0]) == tag); ++spins) { __builtin_amdgcn_s_sleep(2); OPOLL(); asm volatile("s_waitcnt vmcnt(0)" : "+v"(fo) :: "memory"); }
    BARRIER();
#define HOOK5(i) do { of[2 * (i)] = gload16_sc1(osrc + (2 * (i)) * 1024, vl); of[2 * (i) + 1] = gload16_sc1(osrc + (2 * (i) + 1) * 1024, vl); } while (0)
#define NOHOOK(i) do {} while (0)
    C_STAGE(5, HOOK5);
    C_STAGE(6, NOHOOK);
    asm volatile("s_waitcnt vmcnt(0) lgkmcnt(0)" : "+v"(of[0]), "+v"(of[1]), "+v"(of[2]), "+v"(of[3]), "+v"(of[4]), "+v"(of[5]), "+v"(of[6]), "+v"(of[7]), "+v"(xv), "+v"(bv) :: "memory");
    BARRIER();
    {
        bf16x8 wa[2], wc[2];
#define WOFRAG(n, TR) ({ const LAS char* wp_ = lds + slot_of(7 + ((n) >> 2)) + kq * 8192 + (((n) >> 1) & 1) * 4096 + ((4 * kq + ((n) >> 1)) == 15 ? 512 : 0); \
            ((n) & 1) ? trfrag(wp_ + (TR) + 2048, wp_ + (TR) - h * 512 + 3072) : trfrag(wp_ + (TR), wp_ + (TR) + 1024); })
        wc[0] = WOFRAG(0, tr1); wa[0] = WOFRAG(0, tr0);
#pragma unroll
        for (int n = 0; n < 8; ++n) {
            if (n + 1 < 8) { wc[(n + 1) & 1] = WOFRAG(n + 1, tr1); wa[(n + 1) & 1] = WOFRAG(n + 1, tr0); }
            SBAR();
            a0 = MFMA(wa[n & 1], asbf8(of[n]), a0);
            a1 = MFMA(wc[n & 1], asbf8(of[n]), a1);
            SBAR();
        }
    }
    {
        const int tk = tt * 32 + (l & 31);
        LAS char* rw = lds + C_P0 + (kq * 64 + tk) * 256;
#pragma unroll
        for (int g = 0; g < 4; ++g) {
            *(LAS f32x4*)(rw + (((2 * g + h) ^ (tk & 7)) * 16)) = (f32x4){a0[4 * g], a0[4 * g + 1], a0[4 * g + 2], a0[4 * g + 3]};
            *(LAS f32x4*)(rw + (((8 + 2 * g + h) ^ (tk & 7)) * 16)) = (f32x4){a1[4 * g], a1[4 * g + 1], a1[4 * g + 2], a1[4 * g + 3]};
        }
    }
    WAIT_LGKM0(); BARRIER();
    f32x4 s0 = {}, s1 = {};
#pragma unroll
    for (int k = 0; k < 4; ++k) { const LAS char* rr = lds + C_P0 + (k * 64 + tok) * 256; s0 += *(const LAS f32x4*)(rr + (((2 * ch) ^ (tok & 7)) * 16)); s1 += *(const LAS f32x4*)(rr + (((2 * ch + 1) ^ (tok & 7)) * 16)); }
    u32x4 ov;
    ov[0] = cvtpk(s0[0] + bflo(xv[0]) + bflo(bv[0]), s0[1] + bfhi(xv[0]) + bfhi(bv[0]));
    ov[1] = cvtpk(s0[2] + bflo(xv[1]) + bflo(bv[1]), s0[3] + bfhi(xv[1]) + bfhi(bv[1]));
    ov[2] = cvtpk(s1[0] + bflo(xv[2]) + bflo(bv[2]), s1[1] + bfhi(xv[2]) + bfhi(bv[2]));
    ov[3] = cvtpk(s1[2] + bflo(xv[3]) + bflo(bv[3]), s1[3] + bfhi(xv[3]) + bfhi(bv[3]));
    gstore16_sc1(out + (size_t)rb * 64 * 896 + ng * 128, (unsigned)(tok * 896 + ch * 16), ov);
    if (bump && tid == 0) flag_store(ws, WS_EPOCH, tag);
}

constexpr unsigned CODE_LINES = 14;
__global__ void __launch_bounds__(512, 2) fused_block(const char* x_, const char* wqkv_, const char* wo_, const char* w1_, const char* b1_,
                                                      const char* w2_, const char* b2_, char* out_, char* ws_) {
    typedef unsigned long long u64x8 __attribute__((ext_vector_type(8)));
    u64x8 ka; unsigned long long ka8;
    asm volatile("s_load_dwordx16 %0, %2, 0x0\n\ts_load_dwordx2 %1, %2, 0x40" : "=&s"(ka), "=&s"(ka8) : "s"(__builtin_amdgcn_kernarg_segment_ptr()) : "memory");
    extern __shared__ __attribute__((aligned(16))) char lds[];
    const int b = blockIdx.x, xcd = b & 7, i = b >> 3;
    u64 tag; u32x4 q0, q1; float bound;
    { uint64_t pc; asm volatile("s_getpc_b64 %0" : "=s"(pc)); pc &= ~127ull;
      if (threadIdx.x >= 448) { const unsigned ll = threadIdx.x & 63; const unsigned o2 = (unsigned)(((blockIdx.x >> 3) * CODE_LINES + (ll < CODE_LINES ? ll : 0)) * 128);
        asm volatile("s_mov_b32 m0, %2\n\ts_nop 3\n\tglobal_load_lds_dword %0, %1" :: "v"(o2), "s"(pc), "s"((unsigned)(uintptr_t)lds + A_RS) : "memory"); } }
    asm volatile("s_waitcnt lgkmcnt(0)" : "+s"(ka), "+s"(ka8) :: "memory");
    const char* x = (const char*)ka[0]; const char* wqkv = (const char*)ka[1]; const char* wo = (const char*)ka[2]; const char* w1 = (const char*)ka[3]; const char* b1 = (const char*)ka[4];
    const char* w2 = (const char*)ka[5]; const char* b2 = (const char*)ka[6]; char* out = (char*)ka[7]; char* ws = (char*)ka8;
    const int hd = 2 * xcd + (i & 1), qb = i >> 1;
    const int RbA = 8 * (xcd >> 2) + (i >> 2), cA = 4 * (xcd & 3) + (i & 3);
    phaseA(x, wqkv, w1, b1, ws, lds, RbA, cA, hd, qb, tag, q0, q1, bound, w2, wo);
    const bool doC = i < 28;
    phaseBC(x, wo, w2, b2, ws, out, lds, hd, qb, 4 * xcd + (doC ? i / 7 : 0), doC ? i % 7 : 0, doC, b == 0,
            ws + WS_FH + RbA * 256 + cA * 8, true, tag, q0, q1, bound);
}

extern "C" void kernel_launch(void* const* d_in, const int* in_sizes, int n_in, void* d_out, int out_size, void* d_ws, size_t ws_size, hipStream_t stream) {
    const char* x = (const char*)d_in[0]; const char* wqkv = (const char*)d_in[1]; const char* wo = (const char*)d_in[2];
    const char* w1 = (const char*)d_in[4]; const char* b1 = (const char*)d_in[5]; const char* w2 = (const char*)d_in[6]; const char* b2 = (const char*)d_in[7];
    (void)hipFuncSetAttribute((const void*)fused_block, hipFuncAttributeMaxDynamicSharedMemorySize, L_TOTAL);
    hipLaunchKernelGGL(fused_block, dim3(256), dim3(512), L_TOTAL, stream, x, wqkv, wo, w1, b1, w2, b2, (char*)d_out, (char*)d_ws);
}
```
